```python
import torch

def _softmax(x, axis=-1):
    return torch.softmax(x, dim=axis)
def _gelu(x):
    return torch.nn.functional.gelu(x, approximate="tanh")
def _silu(x):
    return torch.nn.functional.silu(x)
def _sigmoid(x):
    return torch.sigmoid(x)
def _layernorm(x, axis=-1, eps=1e-5):
    m = x.mean(dim=axis, keepdim=True)
    v = x.var(dim=axis, keepdim=True, unbiased=False)
    return (x - m) / torch.sqrt(v + eps)
def _scatter_add(tgt, idx, src, axis=0):
    return tgt.clone().index_add_(axis, idx, src)
def _segment_sum(src, seg, n):
    out = torch.zeros((n,) + src.shape[1:], dtype=src.dtype)
    return out.index_add_(0, seg, src)
def _pad(x, pw):
    flat = tuple(v for pair in reversed(pw) for v in pair)
    return torch.nn.functional.pad(x, flat)

kernel_name = 'interleave_layers__grouped_kv_mqa__glu_gated_swiglu__prompt'

def setup_inputs():

    _w = (lambda a: (lambda t: t.to(torch.bfloat16) if t.is_floating_point() else t)(torch.as_tensor(a)))
    def _A_setup():
        B, S, D, H = 1, 2048, 512, 32
        Dh = D // H
        return {
            "x": _w((lambda *s: torch.randn(*s, dtype=torch.bfloat16) * 0.05)(B, S, D)),
            "wq": _w((lambda *s: torch.randn(*s, dtype=torch.bfloat16) * 0.05)(D, H * Dh)),
            "wk": _w((lambda *s: torch.randn(*s, dtype=torch.bfloat16) * 0.05)(D, Dh)),
            "wv": _w((lambda *s: torch.randn(*s, dtype=torch.bfloat16) * 0.05)(D, Dh)),
            "wo": _w((lambda *s: torch.randn(*s, dtype=torch.bfloat16) * 0.05)(H * Dh, D)),
        }
    _A_in = _A_setup()

    def _B_setup():
        B, S, D, F = 1, 2048, 512, 1792
        return {
            "x": _w((lambda *s: torch.randn(*s, dtype=torch.bfloat16) * 0.05)(B, S, D)),
            "w1": _w((lambda *s: torch.randn(*s, dtype=torch.bfloat16) * 0.4)(D, F) * (2.0 / D) ** 0.5),
            "w3": _w((lambda *s: torch.randn(*s, dtype=torch.bfloat16) * 0.4)(D, F) * (2.0 / D) ** 0.5),
            "w2": _w((lambda *s: torch.randn(*s, dtype=torch.bfloat16) * 0.4)(F, D) * (2.0 / F) ** 0.5),
        }
    _B_in = _B_setup()

    return {
        "A_x": _A_in["x"],
        "A_wq": _A_in["wq"],
        "A_wk": _A_in["wk"],
        "A_wv": _A_in["wv"],
        "A_wo": _A_in["wo"],
        "B_x": _B_in["x"],
        "B_w1": _B_in["w1"],
        "B_w3": _B_in["w3"],
        "B_w2": _B_in["w2"],
    }

def reference(A_x, A_wq, A_wk, A_wv, A_wo, B_x, B_w1, B_w3, B_w2):
    def _A_ref(x, wq, wk, wv, wo):
        B, S, D = x.shape
        H = 32
        Dh = D // H
        q = (lambda x, *d: x.permute(*d))((x @ wq).reshape(B, S, H, Dh), 0, 2, 1, 3)
        k = x @ wk
        v = x @ wv
        scores = torch.einsum("bhqd,bkd->bhqk", q, k) * (Dh ** -0.5)
        p = _softmax(scores, axis=-1)
        o = torch.einsum("bhqk,bkd->bhqd", p, v)
        return (lambda x, *d: x.permute(*d))(o, 0, 2, 1, 3).reshape(B, S, H * Dh) @ wo

    def _B_ref(x, w1, w3, w2):
        gate = torch.einsum("bsd,df->bsf", x, w1)
        up = torch.einsum("bsd,df->bsf", x, w3)
        h = _silu(gate) * up
        out = torch.einsum("bsf,fd->bsd", h, w2)
        return out

    x = A_x
    x = x + _A_ref(_layernorm(x, axis=-1), A_wq, A_wk, A_wv, A_wo)
    x = x + _B_ref(_layernorm(x, axis=-1), B_w1, B_w3, B_w2)
    x = x + _A_ref(_layernorm(x, axis=-1), A_wq, A_wk, A_wv, A_wo)
    x = x + _B_ref(_layernorm(x, axis=-1), B_w1, B_w3, B_w2)
    return x
```

```cpp
#include <hip/hip_runtime.h>
#include <hip/hip_bf16.h>
#include <stdint.h>

typedef __attribute__((ext_vector_type(8))) __bf16 bf16x8;
typedef __attribute__((ext_vector_type(16))) __bf16 bf16x16;
typedef __attribute__((ext_vector_type(16))) float f32x16;
typedef __attribute__((ext_vector_type(8))) float f32x8;
typedef __attribute__((ext_vector_type(4))) float f32x4;
typedef __attribute__((ext_vector_type(2))) float f32x2;
typedef __attribute__((ext_vector_type(4))) unsigned u32x4;
typedef __attribute__((ext_vector_type(2))) unsigned u32x2;
typedef __attribute__((ext_vector_type(4))) short s16x4;
typedef __attribute__((ext_vector_type(2))) __bf16 bf16x2;
#define LAS __attribute__((address_space(3)))

constexpr int S_ = 2048, D_ = 512, F_ = 1792;
constexpr float LN_EPS = 1e-5f;
constexpr float QSCALE = 0.25f * 1.4426950408889634f;

constexpr size_t WS_WQ = 0;
constexpr size_t WS_WO = WS_WQ + 512 * 1024;
constexpr size_t WS_W13 = WS_WO + 512 * 1024;
constexpr size_t WS_W2 = WS_W13 + 3584 * 1024;
constexpr size_t WS_CSQ = WS_W2 + 1792 * 1024;
constexpr size_t WS_CS13 = WS_CSQ + 4 * 512 * 4;
constexpr size_t WS_XA = WS_CS13 + 4 * 3584 * 4;
constexpr size_t WS_XB = WS_XA + 2048 * 1024;
constexpr size_t WS_O = WS_XB + 2048 * 1024;
constexpr size_t WS_H = WS_O + 2048 * 1024;
constexpr size_t WS_K = WS_H + 7168 * 1024;
constexpr size_t WS_V = WS_K + 65536;
constexpr size_t WS_ST = WS_V + 65536;
constexpr size_t WS_KM = WS_ST + 2048 * 8;
constexpr size_t WS_PST = WS_KM + 512;

#define MFMA32(a, b, c) __builtin_amdgcn_mfma_f32_32x32x16_bf16((a), (b), (c), 0, 0, 0)
#define SBAR() __builtin_amdgcn_sched_barrier(0)

__device__ __forceinline__ unsigned pk2(float lo, float hi) {
    bf16x2 v = {(__bf16)lo, (__bf16)hi};
    return __builtin_bit_cast(unsigned, v);
}
__device__ __forceinline__ float bflo(unsigned u) { return __uint_as_float(u << 16); }
__device__ __forceinline__ float bfhi(unsigned u) { return __uint_as_float(u & 0xffff0000u); }
__device__ __forceinline__ float xhalf(float v) {
    return __shfl_xor(v, 32);
}
__device__ __forceinline__ bf16x8 asb(u32x4 v) { return __builtin_bit_cast(bf16x8, v); }

template <int N>
__device__ __forceinline__ void glds_run(const void* sbase, unsigned voff, unsigned lds) {
    unsigned sv;
    if constexpr (N == 4)
        asm volatile("s_mov_b32 %0, m0\n\ts_mov_b32 m0, %3\n\ts_nop 0\n\t"
                     "global_load_lds_dwordx4 %1, %2\n\tglobal_load_lds_dwordx4 %1, %2 offset:1024\n\t"
                     "global_load_lds_dwordx4 %1, %2 offset:2048\n\tglobal_load_lds_dwordx4 %1, %2 offset:3072\n\ts_mov_b32 m0, %0"
                     : "=&s"(sv) : "v"(voff), "s"(sbase), "s"(lds) : "memory");
    else if constexpr (N == 3)
        asm volatile("s_mov_b32 %0, m0\n\ts_mov_b32 m0, %3\n\ts_nop 0\n\t"
                     "global_load_lds_dwordx4 %1, %2\n\tglobal_load_lds_dwordx4 %1, %2 offset:1024\n\t"
                     "global_load_lds_dwordx4 %1, %2 offset:2048\n\ts_mov_b32 m0, %0"
                     : "=&s"(sv) : "v"(voff), "s"(sbase), "s"(lds) : "memory");
    else if constexpr (N == 2)
        asm volatile("s_mov_b32 %0, m0\n\ts_mov_b32 m0, %3\n\ts_nop 0\n\t"
                     "global_load_lds_dwordx4 %1, %2\n\tglobal_load_lds_dwordx4 %1, %2 offset:1024\n\ts_mov_b32 m0, %0"
                     : "=&s"(sv) : "v"(voff), "s"(sbase), "s"(lds) : "memory");
    else
        asm volatile("s_mov_b32 %0, m0\n\ts_mov_b32 m0, %3\n\ts_nop 0\n\t"
                     "global_load_lds_dwordx4 %1, %2\n\ts_mov_b32 m0, %0"
                     : "=&s"(sv) : "v"(voff), "s"(sbase), "s"(lds) : "memory");
}
#define WAIT_BAR(N) asm volatile("s_waitcnt vmcnt(" #N ") lgkmcnt(0)\n\ts_barrier" ::: "memory")
__device__ __forceinline__ u32x4 lds16(unsigned addr) { return *(const LAS u32x4*)(uintptr_t)addr; }

__device__ __forceinline__ void prep_w_job(int job, const uint16_t* wq, const uint16_t* wo, const uint16_t* w1,
                                           const uint16_t* w3, const uint16_t* w2, char* ws, char* smem) {
    const int tid = threadIdx.x, lane = tid & 63, wid = tid >> 6;
    const uint16_t *srcA, *srcB; int ld, k0; bool is13 = false;
    char* dst; int dst_tile_stride, dst_ks_stride; float* cs = nullptr;
    if (job < 112) {
        int kq = job & 3, nb = job >> 2; is13 = true;
        srcA = w1 + nb * 64; srcB = w3 + nb * 64; ld = F_; k0 = kq * 128;
        int tile = nb * 4 + wid, grp = tile / 7, tin = tile - grp * 7;
        dst = ws + WS_W13 + ((size_t)(grp * 32 + kq * 8) * 7 + tin) * 1024; dst_ks_stride = 7 * 1024; dst_tile_stride = 0;
        cs = (float*)(ws + WS_CS13) + kq * 3584 + tile * 32;
    } else if (job < 168) {
        int j = job - 112, kb = j >> 2, nb = j & 3;
        srcA = w2 + nb * 128; srcB = srcA + 64; ld = D_; k0 = kb * 128;
        dst = ws + WS_W2 + ((size_t)(nb * 4 + wid) * 112 + kb * 8) * 1024; dst_ks_stride = 1024; dst_tile_stride = 0;
    } else {
        int j = job - 168; bool isq = j < 16; j &= 15; int kq = j >> 2, nb = j & 3;
        srcA = (isq ? wq : wo) + nb * 128; srcB = srcA + 64; ld = D_; k0 = kq * 128;
        dst = ws + (isq ? WS_WQ : WS_WO) + ((size_t)(nb * 4 + wid) * 32 + kq * 8) * 1024; dst_ks_stride = 1024; dst_tile_stride = 0;
        if (isq) cs = (float*)(ws + WS_CSQ) + kq * 512 + (nb * 4 + wid) * 32;
    }
    (void)dst_tile_stride;
    u32x4 v[8];
#pragma unroll
    for (int i = 0; i < 8; i++) {
        int c = tid + i * 256, row = c >> 4, ch = c & 15;
        const uint16_t* s = (ch < 8 ? srcA + ch * 8 : srcB + (ch - 8) * 8) + (size_t)(k0 + row) * ld;
        v[i] = *(const u32x4*)s;
    }
#pragma unroll
    for (int i = 0; i < 8; i++) {
        int c = tid + i * 256, row = c >> 4, ch = c & 15;
        *(u32x4*)(smem + row * 272 + ch * 16) = v[i];
    }
    __syncthreads();
    const int g = lane >> 4, q = (lane & 15) >> 2, p = lane & 3, h = g >> 1;
    const int c0 = is13 ? ((g & 1) * 64 + wid * 16) : (wid * 32 + (g & 1) * 16);
    float sum = 0.f;
#pragma unroll
    for (int ks = 0; ks < 8; ks++) {
        unsigned a0 = (unsigned)(uintptr_t)smem + (ks * 16 + 4 * h + q) * 272 + (c0 + 4 * p) * 2;
        s16x4 lo = __builtin_amdgcn_ds_read_tr16_b64_v4i16((LAS s16x4*)(uintptr_t)a0);
        s16x4 hi = __builtin_amdgcn_ds_read_tr16_b64_v4i16((LAS s16x4*)(uintptr_t)(a0 + 8 * 272));
        u32x2 l2 = __builtin_bit_cast(u32x2, lo), h2 = __builtin_bit_cast(u32x2, hi);
        u32x4 f = {l2[0], l2[1], h2[0], h2[1]};
        *(u32x4*)(dst + (size_t)ks * dst_ks_stride + lane * 16) = f;
#pragma unroll
        for (int i = 0; i < 4; i++) sum += bflo(f[i]) + bfhi(f[i]);
    }
    sum += xhalf(sum);
    if (cs && lane < 32) cs[lane] = sum;
}

template <bool ROWMAJOR>
__device__ __forceinline__ void kv_tile(int tt, const uint16_t* xrow, const char* xf_in, char* xf_out,
                                        const uint16_t* wk, const uint16_t* wv, char* ws, char* smem) {
    const int tid = threadIdx.x, lane = tid & 63, wid = tid >> 6, r = lane & 31, h = lane >> 5;
    u32x4 xf[4], wf[4];
#pragma unroll
    for (int i = 0; i < 4; i++) {
        int ks = wid * 4 + i;
        if constexpr (ROWMAJOR) {
            const uint16_t* s = xrow + (size_t)(tt * 32 + r) * D_ + ks * 16 + 4 * h;
            u32x2 a = *(const u32x2*)s, b = *(const u32x2*)(s + 8);
            xf[i] = u32x4{a[0], a[1], b[0], b[1]};
        } else {
            xf[i] = *(const u32x4*)(xf_in + ((size_t)(tt * 32 + ks) * 64 + lane) * 16);
        }
        const uint16_t* w = (r < 16 ? wk : wv) + (ks * 16 + 4 * h) * 16 + (r & 15);
        unsigned e[8];
#pragma unroll
        for (int j = 0; j < 8; j++) e[j] = w[(8 * (j >> 2) + (j & 3)) * 16];
        wf[i] = u32x4{e[0] | (e[1] << 16), e[2] | (e[3] << 16), e[4] | (e[5] << 16), e[6] | (e[7] << 16)};
    }
    f32x16 aT = {}, aV = {};
    float s1 = 0.f, s2 = 0.f, cs = 0.f;
#pragma unroll
    for (int i = 0; i < 4; i++) {
        if constexpr (ROWMAJOR) *(u32x4*)(xf_out + ((size_t)(tt * 32 + wid * 4 + i) * 64 + lane) * 16) = xf[i];
        aT = MFMA32(asb(wf[i]), asb(xf[i]), aT);
        aV = MFMA32(asb(xf[i]), asb(wf[i]), aV);
#pragma unroll
        for (int j = 0; j < 4; j++) {
            float a = bflo(xf[i][j]), b = bfhi(xf[i][j]);
            s1 += a + b; s2 += a * a + b * b;
            cs += bflo(wf[i][j]) + bfhi(wf[i][j]);
        }
    }
    float* red = (float*)smem;
#pragma unroll
    for (int i = 0; i < 8; i++) red[(wid * 27 + i) * 64 + lane] = aT[i];
#pragma unroll
    for (int i = 0; i < 16; i++) red[(wid * 27 + 8 + i) * 64 + lane] = aV[i];
    red[(wid * 27 + 24) * 64 + lane] = s1; red[(wid * 27 + 25) * 64 + lane] = s2; red[(wid * 27 + 26) * 64 + lane] = cs;
    __syncthreads();
    float* fin = red + 8 * 27 * 64;
    for (int i = tid; i < 27 * 64; i += 512) {
        float a = 0.f;
#pragma unroll
        for (int w = 0; w < 8; w++) a += red[w * 27 * 64 + i];
        fin[i] = a;
    }
    __syncthreads();
    if (wid == 0) {
        float t1 = fin[24 * 64 + r] + fin[24 * 64 + 32 + r], t2 = fin[25 * 64 + r] + fin[25 * 64 + 32 + r];
        float mu = t1 * (1.f / D_), var = fmaxf(t2 * (1.f / D_) - mu * mu, 0.f), rstd = rsqrtf(var + LN_EPS);
        if (h == 0) ((f32x2*)(ws + WS_ST))[tt * 32 + r] = f32x2{mu, rstd};
        float k[8];
#pragma unroll
        for (int j = 0; j < 8; j++) {
            int n = 8 * (j >> 2) + 4 * h + (j & 3);
            float c = fin[26 * 64 + n] + fin[26 * 64 + 32 + n];
            k[j] = rstd * (fin[j * 64 + lane] - mu * c);
        }
        u32x4 o = {pk2(k[0], k[1]), pk2(k[2], k[3]), pk2(k[4], k[5]), pk2(k[6], k[7])};
        int rho = (r & 0x13) | ((r & 4) << 1) | ((r & 8) >> 1);
        *(u32x4*)(ws + WS_K + ((size_t)tt * 64 + rho + 32 * h) * 16) = o;
        float n2 = 0.f;
#pragma unroll
        for (int j = 0; j < 4; j++) { float a = bflo(o[j]), b = bfhi(o[j]); n2 += a * a + b * b; }
        n2 += xhalf(n2);
#pragma unroll
        for (int m = 1; m < 32; m <<= 1) n2 = fmaxf(n2, __shfl_xor(n2, m));
        if (lane == 0) ((float*)(ws + WS_KM))[tt * 2] = n2;
    } else if (wid == 1) {
        float c = fin[26 * 64 + r] + fin[26 * 64 + 32 + r];
        float v[16], vm = 0.f;
#pragma unroll
        for (int i = 0; i < 16; i++) {
            int t = (i & 3) + 8 * (i >> 2) + 4 * h;
            float t1 = fin[24 * 64 + t] + fin[24 * 64 + 32 + t], t2 = fin[25 * 64 + t] + fin[25 * 64 + 32 + t];
            float mu = t1 * (1.f / D_), var = fmaxf(t2 * (1.f / D_) - mu * mu, 0.f), rstd = rsqrtf(var + LN_EPS);
            v[i] = rstd * (fin[(8 + i) * 64 + lane] - mu * c);
            vm = fmaxf(vm, fabsf(v[i]));
        }
        if (r >= 16) {
#pragma unroll
            for (int s = 0; s < 2; s++) {
                u32x4 o = {pk2(v[8 * s], v[8 * s + 1]), pk2(v[8 * s + 2], v[8 * s + 3]), pk2(v[8 * s + 4], v[8 * s + 5]), pk2(v[8 * s + 6], v[8 * s + 7])};
                *(u32x4*)(ws + WS_V + ((size_t)(tt * 2 + s) * 32 + (r - 16) + 16 * h) * 16) = o;
            }
        } else vm = 0.f;
#pragma unroll
        for (int m = 1; m < 64; m <<= 1) vm = fmaxf(vm, __shfl_xor(vm, m));
        if (lane == 0) ((float*)(ws + WS_KM))[tt * 2 + 1] = vm;
    }
}

__global__ void __launch_bounds__(256) k_prepw(const uint16_t* wq, const uint16_t* wo, const uint16_t* w1, const uint16_t* w3,
                                               const uint16_t* w2, char* ws) {
    extern __shared__ __attribute__((aligned(16))) char smem[];
    prep_w_job(blockIdx.x, wq, wo, w1, w3, w2, ws, smem);
}
template <bool ROWMAJOR>
__global__ void __launch_bounds__(512) k_kv(const uint16_t* xrow, const char* xf_in, char* xf_out, const uint16_t* wk,
                                            const uint16_t* wv, char* ws) {
    extern __shared__ __attribute__((aligned(16))) char smem[];
    kv_tile<ROWMAJOR>(blockIdx.x, xrow, xf_in, xf_out, wk, wv, ws, smem);
}

constexpr int ATT_K = 0, ATT_V = 65536, ATT_X = 131072, ATT_ONES = ATT_X + 16384;
__global__ void __launch_bounds__(512) k_att(const char* xf, char* ws) {
    extern __shared__ __attribute__((aligned(16))) char smem[];
    const int tid = threadIdx.x, lane = tid & 63, r = lane & 31, h = lane >> 5;
    const int wid = __builtin_amdgcn_readfirstlane(tid >> 6);
    const int xcd = blockIdx.x & 7, idx = blockIdx.x >> 3, tt = xcd * 4 + (idx >> 3), hg = idx & 7;
    const int tsub = wid & 1, hp = (wid >> 1) & 1, kh = wid >> 2;
    const unsigned lds0 = (unsigned)(uintptr_t)smem, voff = lane * 16;
    auto run_base = [&](int run) -> unsigned { return lds0 + (run < 3 ? 16384 + run * 16384 : run < 6 ? 81920 + (run - 3) * 16384 : 131072 + (run - 6) * 16384); };
    {
        const int isW = wid >> 2, a = (wid >> 1) & 1, kk = wid & 1;
        const char* src = isW ? ws + WS_WQ + ((size_t)(hg * 2 + a) * 32 + kk * 16) * 1024 : xf + ((size_t)(tt * 2 + a) * 32 + kk * 16) * 1024;
        const unsigned dstl = run_base(wid);
#pragma unroll
        for (int i = 0; i < 4; i++) glds_run<4>(src + i * 4096, voff, dstl + i * 4096);
        glds_run<2>(ws + WS_K + wid * 2048, voff, lds0 + ATT_K + wid * 2048);
        glds_run<2>(ws + WS_V + wid * 2048, voff, lds0 + ATT_V + wid * 2048);
    }
    const int tok = (tt * 2 + tsub) * 32 + r, head = hg * 4 + hp * 2 + kh;
    f32x2 st = ((const f32x2*)(ws + WS_ST))[tok];
    f32x2 km = ((const f32x2*)(ws + WS_KM))[lane];
    f32x4 cA = {}, cB = {};
#pragma unroll
    for (int q = 0; q < 4; q++) {
        const float* c = (const float*)(ws + WS_CSQ) + q * 512 + head * 16 + 4 * h;
        cA += *(const f32x4*)c; cB += *(const f32x4*)(c + 8);
    }
    WAIT_BAR(0);
    f32x16 qa = {};
    {
        const unsigned xb = run_base(tsub * 2 + kh) + voff, wb = run_base(4 + hp * 2 + kh) + voff;
#pragma unroll
        for (int i = 0; i < 16; i++) qa = MFMA32(asb(lds16(wb + i * 1024)), asb(lds16(xb + i * 1024)), qa);
    }
    WAIT_BAR(0);
    {
        const char* ks = ws + WS_K + 16384 + wid * 6144; const char* vs = ws + WS_V + 16384 + wid * 6144;
        glds_run<4>(ks, voff, lds0 + ATT_K + 16384 + wid * 6144); glds_run<2>(ks + 4096, voff, lds0 + ATT_K + 16384 + wid * 6144 + 4096);
        glds_run<4>(vs, voff, lds0 + ATT_V + 16384 + wid * 6144); glds_run<2>(vs + 4096, voff, lds0 + ATT_V + 16384 + wid * 6144 + 4096);
    }
    {
        f32x4 s0, s1;
#pragma unroll
        for (int i = 0; i < 4; i++) { s0[i] = kh ? qa[i] : qa[8 + i]; s1[i] = kh ? qa[4 + i] : qa[12 + i]; }
        LAS f32x4* ex = (LAS f32x4*)(uintptr_t)(lds0 + ATT_X + wid * 2048 + voff);
        ex[0] = s0; ex[64] = s1;
        LAS u32x2* on = (LAS u32x2*)(uintptr_t)(lds0 + ATT_ONES + tid * 8);
        *on = u32x2{0x3f803f80u, 0x3f803f80u};
    }
    asm volatile("s_waitcnt lgkmcnt(0)\n\ts_barrier" ::: "memory");
    u32x4 qf;
    float qn2;
    {
        LAS f32x4* ex = (LAS f32x4*)(uintptr_t)(lds0 + ATT_X + (wid ^ 4) * 2048 + voff);
        f32x4 p0 = ex[0], p1 = ex[64];
        float q[8];
        const float a = st[1] * QSCALE, b = -st[0] * a;
#pragma unroll
        for (int i = 0; i < 4; i++) {
            q[i] = a * ((kh ? qa[8 + i] : qa[i]) + p0[i]) + b * cA[i];
            q[4 + i] = a * ((kh ? qa[12 + i] : qa[4 + i]) + p1[i]) + b * cB[i];
        }
        qf = u32x4{pk2(q[0], q[1]), pk2(q[2], q[3]), pk2(q[4], q[5]), pk2(q[6], q[7])};
        qn2 = 0.f;
#pragma unroll
        for (int j = 0; j < 4; j++) { float x = bflo(qf[j]), y = bfhi(qf[j]); qn2 += x * x + y * y; }
        qn2 += xhalf(qn2);
    }
    float kmax2 = km[0], vmax = km[1];
#pragma unroll
    for (int m = 1; m < 64; m <<= 1) { kmax2 = fmaxf(kmax2, __shfl_xor(kmax2, m)); vmax = fmaxf(vmax, __shfl_xor(vmax, m)); }
    const bool safe = (qn2 * kmax2 <= 3600.f) && (vmax <= 1.0e12f) && (vmax >= 1.0e-9f);
    const bool need_max = !__all(safe);
    f32x16 negm = {};
    const unsigned kb0 = lds0 + ATT_K + voff;
    if (__builtin_expect(need_max, 0)) {
        float m = -INFINITY;
        for (int t = 0; t < 64; t++) {
            u32x4 kf = *(const u32x4*)(ws + WS_K + (size_t)t * 1024 + voff);
            f32x16 s = MFMA32(asb(kf), asb(qf), (f32x16){});
#pragma unroll
            for (int i = 0; i < 16; i++) m = fmaxf(m, s[i]);
        }
        m = fmaxf(m, xhalf(m));
#pragma unroll
        for (int i = 0; i < 16; i++) negm[i] = -m;
    }
    f32x16 o = {};
    unsigned ka = kb0;
    unsigned va = r < 16 ? lds0 + ATT_V + (r + 16 * h) * 16 : lds0 + ATT_ONES + (r & 15) * 16;
    const unsigned vinc = r < 16 ? 4096u : 0u;
    for (int it = 0; it < 16; it++) {
        if (it == 4) WAIT_BAR(0);
#pragma unroll
        for (int u = 0; u < 4; u++) {
            u32x4 kf = lds16(ka + u * 1024), v0 = lds16(va + u * 1024), v1 = lds16(va + u * 1024 + 512);
            f32x16 s = MFMA32(asb(kf), asb(qf), negm);
            f32x8 p0, p1;
#pragma unroll
            for (int i = 0; i < 8; i++) { p0[i] = __builtin_amdgcn_exp2f(s[i]); p1[i] = __builtin_amdgcn_exp2f(s[8 + i]); }
            o = __builtin_amdgcn_smfmac_f32_32x32x32_bf16(asb(v0), __builtin_bit_cast(bf16x16, p0), o, 0xDDDD, 0, 0);
            o = __builtin_amdgcn_smfmac_f32_32x32x32_bf16(asb(v1), __builtin_bit_cast(bf16x16, p1), o, 0xDDDD, 0, 0);
        }
        ka += 4096; va += vinc;
    }
    const float rl = 1.f / o[8];
    u32x4 of = {pk2(o[0] * rl, o[1] * rl), pk2(o[2] * rl, o[3] * rl), pk2(o[4] * rl, o[5] * rl), pk2(o[6] * rl, o[7] * rl)};
    *(u32x4*)(ws + WS_O + ((size_t)((tt * 2 + tsub) * 32 + head) * 64 + lane) * 16) = of;
}

template <int KS, bool FINAL>
__global__ void __launch_bounds__(256) k_res(const char* af, const char* wt, const char* xin, char* xout, uint16_t* out, char* ws) {
    extern __shared__ __attribute__((aligned(16))) char smem[];
    constexpr int KW = KS / 4, PD = KW < 12 ? KW : 12;
    const int tid = threadIdx.x, lane = tid & 63, r = lane & 31, h = lane >> 5;
    const int wid = __builtin_amdgcn_readfirstlane(tid >> 6);
    const int xcd = blockIdx.x & 7, idx = blockIdx.x >> 3, tt = xcd * 4 + (idx >> 3), ct = idx & 7;
    const u32x4* a0 = (const u32x4*)(af + ((size_t)(tt * 2) * KS + wid * KW) * 1024) + lane;
    const u32x4* w0 = (const u32x4*)(wt + ((size_t)(ct * 2) * KS + wid * KW) * 1024) + lane;
    u32x4 A0[KW], A1[KW], W0[KW], W1[KW];
    f32x16 acc[4] = {};
#pragma unroll
    for (int i = 0; i < PD; i++) { A0[i] = a0[i * 64]; W0[i] = w0[i * 64]; A1[i] = a0[(KS + i) * 64]; W1[i] = w0[(KS + i) * 64]; }
    const int tsub = wid & 1, nsub = wid >> 1;
    const size_t xoff = ((size_t)((tt * 2 + tsub) * 32 + (ct * 2 + nsub) * 2) * 64 + lane) * 16;
    u32x4 xr0 = *(const u32x4*)(xin + xoff), xr1 = *(const u32x4*)(xin + xoff + 1024);
    SBAR();
#pragma unroll
    for (int i = 0; i < KW; i++) {
        acc[0] = MFMA32(asb(W0[i]), asb(A0[i]), acc[0]);
        acc[1] = MFMA32(asb(W0[i]), asb(A1[i]), acc[1]);
        acc[2] = MFMA32(asb(W1[i]), asb(A0[i]), acc[2]);
        acc[3] = MFMA32(asb(W1[i]), asb(A1[i]), acc[3]);
        if (i + PD < KW) { int j = i + PD; A0[j] = a0[j * 64]; W0[j] = w0[j * 64]; A1[j] = a0[(KS + j) * 64]; W1[j] = w0[(KS + j) * 64]; }
        SBAR();
    }
    f32x4* red = (f32x4*)smem;
#pragma unroll
    for (int t = 0; t < 4; t++)
        if (t != wid) {
#pragma unroll
            for (int g = 0; g < 4; g++) red[((t * 4 + wid) * 4 + g) * 64 + lane] = f32x4{acc[t][4 * g], acc[t][4 * g + 1], acc[t][4 * g + 2], acc[t][4 * g + 3]};
        }
    __syncthreads();
    float y[16];
#pragma unroll
    for (int i = 0; i < 16; i++) y[i] = wid == 0 ? acc[0][i] : wid == 1 ? acc[1][i] : wid == 2 ? acc[2][i] : acc[3][i];
#pragma unroll
    for (int s = 0; s < 4; s++)
        if (s != wid) {
#pragma unroll
            for (int g = 0; g < 4; g++) { f32x4 p = red[((wid * 4 + s) * 4 + g) * 64 + lane]; y[4 * g] += p[0]; y[4 * g + 1] += p[1]; y[4 * g + 2] += p[2]; y[4 * g + 3] += p[3]; }
        }
    unsigned xo[8]; float s1 = 0.f, s2 = 0.f;
#pragma unroll
    for (int i = 0; i < 8; i++) {
        unsigned xr = i < 4 ? xr0[i] : xr1[i - 4], yb = pk2(y[2 * i], y[2 * i + 1]);
        xo[i] = pk2(bflo(xr) + bflo(yb), bfhi(xr) + bfhi(yb));
        float a = bflo(xo[i]), b = bfhi(xo[i]);
        s1 += a + b; s2 += a * a + b * b;
    }
    if constexpr (FINAL) {
        uint16_t* po = out + (size_t)((tt * 2 + tsub) * 32 + r) * D_ + (ct * 2 + nsub) * 32 + 4 * h;
#pragma unroll
        for (int g = 0; g < 4; g++) *(u32x2*)(po + 8 * g) = u32x2{xo[2 * g], xo[2 * g + 1]};
    } else {
        *(u32x4*)(xout + xoff) = u32x4{xo[0], xo[1], xo[2], xo[3]};
        *(u32x4*)(xout + xoff + 1024) = u32x4{xo[4], xo[5], xo[6], xo[7]};
        s1 += xhalf(s1); s2 += xhalf(s2);
        if (h == 0) ((f32x2*)(ws + WS_PST))[((tt * 2 + tsub) * 32 + r) * 16 + ct * 2 + nsub] = f32x2{s1, s2};
    }
}

constexpr int GU_STAGE = 44 * 1024;
__global__ void __launch_bounds__(256) k_gu(const char* xf, char* ws) {
    extern __shared__ __attribute__((aligned(16))) char smem[];
    const int tid = threadIdx.x, lane = tid & 63, r = lane & 31, h = lane >> 5;
    const int wid = __builtin_amdgcn_readfirstlane(tid >> 6);
    const int xcd = blockIdx.x & 7, idx = blockIdx.x >> 3;
    const int tt = (xcd & 1) * 8 + (idx >> 2), nt = (xcd >> 1) * 4 + (idx & 3);
    const unsigned lds0 = (unsigned)(uintptr_t)smem, voff = lane * 16;
    const char* xsrc = xf + (size_t)(tt * 4 + wid) * 32 * 1024;
    const char* wsrc = ws + WS_W13 + ((size_t)nt * 32 * 7 + wid * 7) * 1024;
    auto stage = [&](int s) {
        const unsigned b = lds0 + (s % 3) * GU_STAGE;
        glds_run<4>(xsrc + s * 4096, voff, b + wid * 4096);
        glds_run<4>(wsrc + s * 28 * 1024, voff, b + 16384 + wid * 7168);
        glds_run<3>(wsrc + s * 28 * 1024 + 4096, voff, b + 16384 + wid * 7168 + 4096);
    };
    const int tok = (tt * 4 + wid) * 32 + r;
    f32x4 ps[4];
#pragma unroll
    for (int i = 0; i < 4; i++) ps[i] = ((const f32x4*)(ws + WS_PST))[tok * 8 + h * 4 + i];
    float s1 = 0.f, s2 = 0.f;
#pragma unroll
    for (int i = 0; i < 4; i++) { s1 += ps[i][0] + ps[i][2]; s2 += ps[i][1] + ps[i][3]; }
    stage(0); stage(1);
    s1 += xhalf(s1); s2 += xhalf(s2);
    const float mu = s1 * (1.f / D_), rstd = rsqrtf(fmaxf(s2 * (1.f / D_) - mu * mu, 0.f) + LN_EPS);
    f32x16 acc[7] = {};
#pragma unroll
    for (int s = 0; s < 8; s++) {
        if (s < 7) WAIT_BAR(11); else WAIT_BAR(0);
        if (s + 2 < 8) stage(s + 2);
        const unsigned b = lds0 + (s % 3) * GU_STAGE + voff;
#pragma unroll
        for (int k = 0; k < 4; k++) {
            u32x4 x = lds16(b + wid * 4096 + k * 1024);
#pragma unroll
            for (int t = 0; t < 7; t++) acc[t] = MFMA32(asb(lds16(b + 16384 + (k * 7 + t) * 1024)), asb(x), acc[t]);
        }
    }
    const float nb = -mu * rstd;
#pragma unroll
    for (int t = 0; t < 7; t++) {
        const int tile = nt * 7 + t;
        f32x4 c[4] = {};
#pragma unroll
        for (int q = 0; q < 4; q++)
#pragma unroll
            for (int g = 0; g < 4; g++) c[g] += *(const f32x4*)((const float*)(ws + WS_CS13) + q * 3584 + tile * 32 + 8 * g + 4 * h);
        float hv[8];
#pragma unroll
        for (int i = 0; i < 8; i++) {
            float g = rstd * acc[t][i] + nb * c[i >> 2][i & 3];
            float u = rstd * acc[t][8 + i] + nb * c[2 + (i >> 2)][i & 3];
            float sg = g * __builtin_amdgcn_rcpf(1.f + __builtin_amdgcn_exp2f(-1.4426950408889634f * g));
            hv[i] = sg * u;
        }
        *(u32x4*)(ws + WS_H + ((size_t)((tt * 4 + wid) * 112 + tile) * 64 + lane) * 16) =
            u32x4{pk2(hv[0], hv[1]), pk2(hv[2], hv[3]), pk2(hv[4], hv[5]), pk2(hv[6], hv[7])};
    }
}

extern "C" void kernel_launch(void* const* d_in, const int* in_sizes, int n_in, void* d_out, int out_size, void* d_ws,
                              size_t ws_size, hipStream_t stream) {
    const uint16_t* x = (const uint16_t*)d_in[0];
    const uint16_t* wq = (const uint16_t*)d_in[1];
    const uint16_t* wk = (const uint16_t*)d_in[2];
    const uint16_t* wv = (const uint16_t*)d_in[3];
    const uint16_t* wo = (const uint16_t*)d_in[4];
    const uint16_t* w1 = (const uint16_t*)d_in[6];
    const uint16_t* w3 = (const uint16_t*)d_in[7];
    const uint16_t* w2 = (const uint16_t*)d_in[8];
    char* ws = (char*)d_ws;
    uint16_t* out = (uint16_t*)d_out;
    (void)hipFuncSetAttribute((const void*)k_att, hipFuncAttributeMaxDynamicSharedMemorySize, 160 * 1024);
    (void)hipFuncSetAttribute((const void*)k_gu, hipFuncAttributeMaxDynamicSharedMemorySize, 3 * GU_STAGE);
    constexpr int KV_LDS = 9 * 27 * 64 * 4;
    k_prepw<<<200, 256, 128 * 272, stream>>>(wq, wo, w1, w3, w2, ws);
    k_kv<true><<<64, 512, KV_LDS, stream>>>(x, nullptr, ws + WS_XA, wk, wv, ws);
    for (int l = 0; l < 2; l++) {
        if (l == 1) k_kv<false><<<64, 512, KV_LDS, stream>>>(nullptr, ws + WS_XA, nullptr, wk, wv, ws);
        k_att<<<256, 512, 160 * 1024, stream>>>(ws + WS_XA, ws);
        k_res<32, false><<<256, 256, 65536, stream>>>(ws + WS_O, ws + WS_WO, ws + WS_XA, ws + WS_XB, nullptr, ws);
        k_gu<<<256, 256, 3 * GU_STAGE, stream>>>(ws + WS_XB, ws);
        if (l == 0) k_res<112, false><<<256, 256, 65536, stream>>>(ws + WS_H, ws + WS_W2, ws + WS_XB, ws + WS_XA, nullptr, ws);
        else k_res<112, true><<<256, 256, 65536, stream>>>(ws + WS_H, ws + WS_W2, ws + WS_XB, nullptr, out, ws);
    }
}
```

```cpp
#include <hip/hip_runtime.h>
#include <hip/hip_bf16.h>
#include <stdint.h>

typedef __attribute__((ext_vector_type(16))) float f32x16;
typedef __attribute__((ext_vector_type(4))) float f32x4;
typedef __attribute__((ext_vector_type(2))) float f32x2;
typedef __attribute__((ext_vector_type(32))) unsigned u32x32;
typedef __attribute__((ext_vector_type(8))) unsigned u32x8;
typedef __attribute__((ext_vector_type(4))) unsigned u32x4;
typedef __attribute__((ext_vector_type(2))) unsigned u32x2;
typedef __attribute__((ext_vector_type(4))) short s16x4;
typedef __attribute__((ext_vector_type(2))) __bf16 bf16x2;
#define LAS __attribute__((address_space(3)))
#define RU 1
#define CODE_KB 44
#define KNAME "_Z6k_mainPcPtPKtS2_S2_S2_S2_S2_S2_S2_"
#define GU_MAP 0

constexpr int D_ = 512, F_ = 1792;
constexpr float LN_EPS = 1e-5f;
constexpr float QSCALE = 0.25f * 1.4426950408889634f;

constexpr unsigned FL_O = 0, FL_X1 = 8192, FL_H = FL_X1 + 16384, FL_X2 = FL_H + 4096, FL_KC = FL_X2 + 4096, FL_W = FL_KC + 2048, FL_KV = FL_KC + 4096, WS_ST = FL_KV + 16384, FL_END = WS_ST + 32768;
static_assert(FL_END <= 8 * 12288, "zeroed region");
constexpr unsigned WS_SYNC = 8 * 12288;
constexpr unsigned WS_CQ = WS_SYNC + 4096;
constexpr unsigned WS_WO = WS_CQ + 1024 * 128;
constexpr unsigned WS_W13 = WS_WO + 512 * 1024;
constexpr unsigned WS_W2 = WS_W13 + 3584 * 1024;
constexpr unsigned WS_CS13 = WS_W2 + 1792 * 1024;
constexpr unsigned WS_X = WS_CS13 + 4 * 3584 * 4 + 1024;
constexpr int LS = RU ? 0 : 1;
constexpr unsigned WS_PST = WS_X + (RU ? 2 : 4) * 2097152;
constexpr unsigned WS_O = WS_PST + 4 * 262144;
constexpr unsigned WS_H = WS_O + (RU ? 1 : 2) * 2097152;
constexpr unsigned WS_KV = WS_H + (RU ? 1 : 2) * 7340032;
constexpr unsigned KV_STRIDE = 131072 + 1024 + 8192;

#define SBAR() __builtin_amdgcn_sched_barrier(0)

__device__ __forceinline__ unsigned pk2(float lo, float hi) {
    bf16x2 v = {(__bf16)lo, (__bf16)hi};
    return __builtin_bit_cast(unsigned, v);
}
__device__ __forceinline__ float bflo(unsigned u) { return __uint_as_float(u << 16); }
__device__ __forceinline__ float bfhi(unsigned u) { return __uint_as_float(u & 0xffff0000u); }
__device__ __forceinline__ float xsum(float v) {
    auto r = __builtin_amdgcn_permlane32_swap(__float_as_uint(v), __float_as_uint(v), false, false);
    return __uint_as_float(r[0]) + __uint_as_float(r[1]);
}
__device__ __forceinline__ float xmax(float v) {
    auto r = __builtin_amdgcn_permlane32_swap(__float_as_uint(v), __float_as_uint(v), false, false);
    return fmaxf(__uint_as_float(r[0]), __uint_as_float(r[1]));
}
__device__ __forceinline__ float max64(float v) {
    float t;
    asm volatile("s_nop 1\n\tv_max_f32_dpp %0, %0, %0 row_ror:1 row_mask:0xf bank_mask:0xf\n\ts_nop 1\n\t"
                 "v_max_f32_dpp %0, %0, %0 row_ror:2 row_mask:0xf bank_mask:0xf\n\ts_nop 1\n\t"
                 "v_max_f32_dpp %0, %0, %0 row_ror:4 row_mask:0xf bank_mask:0xf\n\ts_nop 1\n\t"
                 "v_max_f32_dpp %0, %0, %0 row_ror:8 row_mask:0xf bank_mask:0xf\n\tv_mov_b32 %1, %0\n\ts_nop 1\n\t"
                 "v_permlane16_swap_b32 %0, %1\n\ts_nop 0\n\tv_max_f32 %0, %0, %1\n\tv_mov_b32 %1, %0\n\ts_nop 1\n\t"
                 "v_permlane32_swap_b32 %0, %1\n\ts_nop 0\n\tv_max_f32 %0, %0, %1" : "+v"(v), "=&v"(t));
    return v;
}

#define GL1(o, P) "global_load_lds_dwordx4 %0, %1 offset:" #o P "\n\t"
#define GLDS_BODY(P) \
    if constexpr (N == 4) asm volatile("s_mov_b32 m0, %2\n\ts_nop 0\n\t" GL1(0, P) GL1(1024, P) GL1(2048, P) GL1(3072, P) :: "v"(voff), "s"(sbase), "s"(lds) : "memory"); \
    else if constexpr (N == 3) asm volatile("s_mov_b32 m0, %2\n\ts_nop 0\n\t" GL1(0, P) GL1(1024, P) GL1(2048, P) :: "v"(voff), "s"(sbase), "s"(lds) : "memory"); \
    else if constexpr (N == 2) asm volatile("s_mov_b32 m0, %2\n\ts_nop 0\n\t" GL1(0, P) GL1(1024, P) :: "v"(voff), "s"(sbase), "s"(lds) : "memory"); \
    else asm volatile("s_mov_b32 m0, %2\n\ts_nop 0\n\t" GL1(0, P) :: "v"(voff), "s"(sbase), "s"(lds) : "memory");
template <int N, bool SC1>
__device__ __forceinline__ void glds4(const void* sbase, unsigned voff, unsigned lds) {
    if constexpr (SC1) { GLDS_BODY(" sc1") } else { GLDS_BODY("") }
}
template <int N, bool SC1>
__device__ __forceinline__ void glds(const char* sbase, unsigned voff, unsigned lds) {
#pragma unroll
    for (int i = 0; i + 4 <= N; i += 4) glds4<4, SC1>(sbase + i * 1024, voff, lds + i * 1024);
    if constexpr (N % 4) glds4<N % 4, SC1>(sbase + (N / 4) * 4096, voff, lds + (N / 4) * 4096);
}
__device__ __forceinline__ void st16(const void* sbase, unsigned voff, u32x4 v) {
    asm volatile("global_store_dwordx4 %0, %1, %2 sc1\n\ts_nop 1" :: "v"(voff), "v"(v), "s"(sbase) : "memory");
}
__device__ __forceinline__ void st16x2(const void* sbase, unsigned voff, u32x4 a, u32x4 b) {
    asm volatile("global_store_dwordx4 %0, %1, %3 sc1\n\tglobal_store_dwordx4 %0, %2, %3 offset:1024 sc1\n\ts_nop 1" :: "v"(voff), "v"(a), "v"(b), "s"(sbase) : "memory");
}
__device__ __forceinline__ void st8(const void* sbase, unsigned voff, u32x2 v) {
    asm volatile("global_store_dwordx2 %0, %1, %2 sc1" :: "v"(voff), "v"(v), "s"(sbase) : "memory");
}
__device__ __forceinline__ void st4(const void* sbase, unsigned voff, unsigned v) {
    asm volatile("global_store_dword %0, %1, %2 sc1" :: "v"(voff), "v"(v), "s"(sbase) : "memory");
}
__device__ __forceinline__ void arrive_wave(const char* cnt) {
    unsigned a, b; uint64_t e;
    asm volatile("s_waitcnt vmcnt(0)\n\tv_mov_b32 %0, 0\n\tv_mov_b32 %1, 1\n\ts_mov_b64 %2, exec\n\ts_mov_b64 exec, 1\n\t"
                 "global_atomic_add %0, %1, %3 sc1\n\ts_mov_b64 exec, %2" : "=&v"(a), "=&v"(b), "=&s"(e) : "s"(cnt) : "memory");
}
__device__ __forceinline__ void arrive_block(const char* cnt, unsigned wid) {
    asm volatile("s_waitcnt vmcnt(0) lgkmcnt(0)\n\ts_barrier" ::: "memory");
    if (wid == 0) arrive_wave(cnt);
}
__device__ __forceinline__ void arrive_block4(const char* cnt, unsigned wid, unsigned lane) {
    asm volatile("s_waitcnt vmcnt(0) lgkmcnt(0)\n\ts_barrier" ::: "memory");
    if (wid == 0) {
        unsigned b; uint64_t e;
        asm volatile("v_mov_b32 %0, 1\n\ts_mov_b64 %1, exec\n\ts_mov_b64 exec, 15\n\t"
                     "global_atomic_add %2, %0, %3 sc1\n\ts_mov_b64 exec, %1" : "=&v"(b), "=&s"(e) : "v"(lane * 128), "s"(cnt) : "memory");
    }
}
__device__ __forceinline__ void wait_cnt(const char* cnt, unsigned voff, unsigned n, unsigned wid) {
    unsigned a, s0;
    asm volatile("s_cmp_lg_u32 %4, 0\n\ts_cbranch_scc1 2f\n\ts_mov_b32 %1, 0x100000\n"
                 "1:\n\tglobal_load_dword %0, %2, %3 sc1\n\ts_waitcnt vmcnt(0)\n\tv_cmp_gt_u32 vcc, %5, %0\n\ts_nop 4\n\t"
                 "s_cbranch_vccz 2f\n\ts_sub_u32 %1, %1, 1\n\ts_cbranch_scc0 1b\n2:\n\ts_waitcnt lgkmcnt(0)\n\ts_barrier"
                 : "=&v"(a), "=&s"(s0) : "v"(voff), "s"(cnt), "s"(wid), "s"(n) : "memory", "scc", "vcc");
}

template <class T> __device__ __forceinline__ T* uni(T* p) {
    const uint64_t u = (uint64_t)p;
    return (T*)(((uint64_t)(unsigned)__builtin_amdgcn_readfirstlane((unsigned)(u >> 32)) << 32) | (unsigned)__builtin_amdgcn_readfirstlane((unsigned)u));
}
struct Job { const char* a; const char* b; char* dst; float* cs; int ld2, kstride, w2k, is13; };
__device__ __forceinline__ Job job_of(int job, int tl, const uint16_t* wo, const uint16_t* w1, const uint16_t* w3, const uint16_t* w2, char* ws) {
    Job J; J.cs = nullptr; J.w2k = -1; J.is13 = 0; J.kstride = 1024;
    if (job < 112) {
        const int kq = job & 3, nb = job >> 2, tile = nb * 4 + tl, grp = tile / 7, tin = tile - grp * 7;
        J.is13 = 1; J.ld2 = F_ * 2;
        J.a = (const char*)(w1 + kq * 128 * F_ + nb * 64); J.b = (const char*)(w3 + kq * 128 * F_ + nb * 64);
        if (tin < 3) { J.dst = ws + WS_W13 + ((size_t)grp * 224 + kq * 24 + 2 * tin) * 1024; J.kstride = 6144; }
        else { J.dst = ws + WS_W13 + ((size_t)grp * 224 + (tin < 5 ? 90 + 2 * tin : 150 + 2 * tin) + kq * 16) * 1024; J.kstride = 4096; }
        J.cs = (float*)(ws + WS_CS13) + kq * 3584 + tile * 32;
    } else if (job < 168) {
        const int j = job - 112, kb = j >> 2, nb = j & 3;
        J.ld2 = D_ * 2; J.a = (const char*)(w2 + kb * 128 * D_ + nb * 128); J.b = J.a + 128;
        J.dst = ws + WS_W2 + (size_t)(nb * 4 + tl) * 112 * 1024; J.w2k = kb * 8;
    } else {
        const int j = (job - 168) & 15, kq = j >> 2, nb = j & 3;
        J.ld2 = D_ * 2; J.a = (const char*)(wo + kq * 128 * D_ + nb * 128); J.b = J.a + 128;
        J.dst = ws + WS_WO + ((size_t)(nb * 4 + tl) * 32 + kq * 8) * 1024;
    }
    return J;
}

__device__ __forceinline__ void ln_stats(float s1, float s2, float& mu, float& rstd) {
    mu = s1 * (1.f / D_);
    rstd = __builtin_amdgcn_rsqf(fmaxf(s2 * (1.f / D_) - mu * mu, 0.f) + LN_EPS);
}
__device__ __forceinline__ void k_epilogue(const float* acc, float mu, float rstd, const float* c, char* kv, unsigned tt, unsigned lane) {
    float k[8];
#pragma unroll
    for (int j = 0; j < 8; j++) k[j] = rstd * (acc[j] - mu * c[j]);
    u32x4 o = {pk2(k[0], k[1]), pk2(k[2], k[3]), pk2(k[4], k[5]), pk2(k[6], k[7])};
    const unsigned off = (tt * 64 + lane) * 16;
    st16(kv, off, o);
    float n2 = 0.f;
#pragma unroll
    for (int j = 0; j < 4; j++) { float a = bflo(o[j]), b = bfhi(o[j]); n2 += a * a + b * b; }
    n2 = max64(xsum(n2));
    if (lane == 0) st4(kv, 131072 + 1024 + tt * 128, __float_as_uint(n2));
}
__device__ __forceinline__ void v_epilogue(const float* acc, float mu, float rstd, const float* c, char* kv, unsigned tt, unsigned lane, unsigned scr) {
    const unsigned r = lane & 31, h = lane >> 5;
    float v[8], vm = 0.f;
#pragma unroll
    for (int j = 0; j < 8; j++) { v[j] = rstd * (acc[j] - mu * c[j]); vm = fmaxf(vm, fabsf(v[j])); }
    *(LAS u32x2*)(uintptr_t)(scr + r * 32 + h * 8) = u32x2{pk2(v[0], v[1]), pk2(v[2], v[3])};
    *(LAS u32x2*)(uintptr_t)(scr + r * 32 + h * 8 + 16) = u32x2{pk2(v[4], v[5]), pk2(v[6], v[7])};
    asm volatile("s_waitcnt lgkmcnt(0)" ::: "memory");
    const unsigned ta = scr + ((lane >> 4) * 8 + ((lane & 15) >> 2)) * 32 + (lane & 3) * 8;
    u32x2 lo = __builtin_bit_cast(u32x2, __builtin_amdgcn_ds_read_tr16_b64_v4i16((LAS s16x4*)(uintptr_t)ta));
    u32x2 hi = __builtin_bit_cast(u32x2, __builtin_amdgcn_ds_read_tr16_b64_v4i16((LAS s16x4*)(uintptr_t)(ta + 128)));
    u32x4 o = {lo[0], lo[1], hi[0], hi[1]};
    const unsigned off = 65536 + (tt * 64 + lane) * 16;
    st16(kv, off, o);
    vm = max64(vm);
    if (lane == 0) st4(kv, 131072 + 1024 + tt * 128 + 4, __float_as_uint(vm));
}

constexpr int L_K = 0, L_V = 65536, L_ONES = 129 * 1024, L_CS = 159 * 1024;

__device__ __forceinline__ void res_add(const float (&y)[16], unsigned (&xr)[8]) {
#pragma unroll
    for (int i = 0; i < 8; i++) {
        xr[i] = pk2(bflo(xr[i]) + y[2 * i], bfhi(xr[i]) + y[2 * i + 1]);
    }
    asm volatile("" : "+v"(xr[0]), "+v"(xr[1]), "+v"(xr[2]), "+v"(xr[3]), "+v"(xr[4]), "+v"(xr[5]), "+v"(xr[6]), "+v"(xr[7]));
}

#define KVW_BODY \
        "s_waitcnt vmcnt(16)\n" \
        "ds_read_b64_tr_b16 v[64:65], %[wa] offset:0\n" \
        "ds_read_b64_tr_b16 v[66:67], %[wb] offset:0\n" \
        "ds_read_b64_tr_b16 v[68:69], %[wa] offset:512\n" \
        "ds_read_b64_tr_b16 v[70:71], %[wb] offset:512\n" \
        "ds_read_b64_tr_b16 v[72:73], %[wa] offset:1024\n" \
        "ds_read_b64_tr_b16 v[74:75], %[wb] offset:1024\n" \
        "ds_read_b64_tr_b16 v[76:77], %[wa] offset:1536\n" \
        "ds_read_b64_tr_b16 v[78:79], %[wb] offset:1536\n" \
        "ds_read_b64_tr_b16 v[80:81], %[wa] offset:2048\n" \
        "ds_read_b64_tr_b16 v[82:83], %[wb] offset:2048\n" \
        "ds_read_b64_tr_b16 v[84:85], %[wa] offset:2560\n" \
        "ds_read_b64_tr_b16 v[86:87], %[wb] offset:2560\n" \
        "ds_read_b64_tr_b16 v[88:89], %[wa] offset:3072\n" \
        "ds_read_b64_tr_b16 v[90:91], %[wb] offset:3072\n" \
        "s_waitcnt lgkmcnt(12)\n" \
        "ds_read_b64_tr_b16 v[92:93], %[wa] offset:3584\n" \
        "ds_read_b64_tr_b16 v[94:95], %[wb] offset:3584\n" \
        "s_waitcnt lgkmcnt(14)\n" \
        "v_mfma_f32_32x32x16_bf16 v[144:159], v[64:67], %[ones], 0\n" \
        "s_waitcnt lgkmcnt(12)\n" \
        "v_mfma_f32_32x32x16_bf16 v[144:159], v[68:71], %[ones], v[144:159]\n" \
        "s_waitcnt lgkmcnt(10)\n" \
        "v_mfma_f32_32x32x16_bf16 v[144:159], v[72:75], %[ones], v[144:159]\n" \
        "s_waitcnt lgkmcnt(8)\n" \
        "v_mfma_f32_32x32x16_bf16 v[144:159], v[76:79], %[ones], v[144:159]\n" \
        "s_waitcnt lgkmcnt(6)\n" \
        "v_mfma_f32_32x32x16_bf16 v[144:159], v[80:83], %[ones], v[144:159]\n" \
        "s_waitcnt lgkmcnt(4)\n" \
        "v_mfma_f32_32x32x16_bf16 v[144:159], v[84:87], %[ones], v[144:159]\n" \
        "s_waitcnt lgkmcnt(2)\n" \
        "v_mfma_f32_32x32x16_bf16 v[144:159], v[88:91], %[ones], v[144:159]\n" \
        "s_waitcnt lgkmcnt(0)\n" \
        "v_mfma_f32_32x32x16_bf16 v[144:159], v[92:95], %[ones], v[144:159]\n" \
        "s_nop 7\n" \
        "s_nop 3\n" \
        "s_mov_b64 s[74:75], exec\n" \
        "s_mov_b32 exec_lo, 1\n" \
        "s_mov_b32 exec_hi, 1\n" \
        "ds_write_b128 %[pc], v[144:147] offset:0\n" \
        "ds_write_b128 %[pc], v[148:151] offset:32\n" \
        "ds_write_b128 %[pc], v[152:155] offset:64\n" \
        "ds_write_b128 %[pc], v[156:159] offset:96\n" \
        "s_mov_b64 exec, s[74:75]\n"

__global__ void __launch_bounds__(256, 1) k_main(char* ws0, uint16_t* out, const uint16_t* x, const uint16_t* wq, const uint16_t* wk,
                                                 const uint16_t* wv, const uint16_t* wo, const uint16_t* w1, const uint16_t* w3, const uint16_t* w2) {
    extern __shared__ __attribute__((aligned(16))) char smem[];
    asm volatile("s_getpc_b64 s[74:75]\n\ts_add_u32 s74, s74, " KNAME "@rel32@lo+4\n\ts_addc_u32 s75, s75, " KNAME "@rel32@hi+12\n\t"
                 "global_load_dwordx2 a[34:35], %0, s[74:75]" :: "v"(min((int)((blockIdx.x >> 3) * 2048 + threadIdx.x * 8), CODE_KB * 1024 - 8))
                 : "memory", "scc", "s74", "s75", "a34", "a35");
    const int wid0 = __builtin_amdgcn_readfirstlane(threadIdx.x >> 6);
    const unsigned lds0 = (unsigned)(uintptr_t)smem;
#define CTX() \
    char* ws = ws0; unsigned bid = blockIdx.x, wid = wid0, l = l0, lane; \
    asm volatile("" : "+s"(ws), "+s"(bid), "+s"(wid), "+s"(l)); \
    asm volatile("v_mbcnt_lo_u32_b32 %0, -1, 0\n\tv_mbcnt_hi_u32_b32 %0, -1, %0" : "=v"(lane)); \
    const unsigned tid = wid * 64 + lane, r = lane & 31, h = lane >> 5, xcd = bid & 7, idx = bid >> 3; \
    const unsigned T64 = xcd * 4 + (idx >> 3), c8 = idx & 7, G = T64 >> 1, mem16 = idx & 15; \
    const unsigned Gg = GU_MAP == 1 ? (xcd & 1) * 8 + (idx >> 2) : GU_MAP == 2 ? (xcd & 3) * 4 + (idx >> 3) : G; \
    const unsigned ng = GU_MAP == 1 ? (xcd >> 1) * 4 + (idx & 3) : GU_MAP == 2 ? (xcd >> 2) * 8 + (idx & 7) : mem16; \
    const unsigned tsub = wid & 1, nsub = wid >> 1, tt = T64 * 2 + tsub; \
    const unsigned voff = lane * 16, xoff = (tt * 32 + (c8 * 2 + nsub) * 2) * 1024; \
    const unsigned ot = c8 >> 2, on = (c8 & 3) * 4 + wid;       \
    const char* w13 = ws + (unsigned)(WS_W13 + ng * 224 * 1024); \
    const char* w2p = ws + (unsigned)(WS_W2 + ((c8 * 2) * 112 + wid * 28) * 1024); \
    (void)ot; (void)on; (void)tid; (void)r; (void)h; (void)G; (void)mem16; (void)Gg; (void)xoff; (void)w13; (void)w2p; (void)tt
    int l0 = 0;
    for (;;) {
        u32x32 woa, wob, woc, wod, w2a, w2b, w2c, w2d;
        unsigned xr[8];
        const char* jdst;
        const char* jcs;
        f32x2 st;
        u32x32 ja;
        u32x8 jo;
        {
            CTX();
            char* kv = ws + (unsigned)(WS_KV + l * KV_STRIDE);
            const int kvm = ((int)c8 - 2) >> 31;
            const bool kvcu = c8 < 2, kvfin = kvcu && tsub == c8;
            const unsigned vw = (lane >> 3) * 1024 + (((lane & 7) ^ (((lane >> 4) & 1) << 2)) * 16);
            const char* wqs = (const char*)wq + c8 * 128;
            const unsigned a1st = c8 & 1;
            unsigned one2;
            asm volatile("v_mov_b32 %0, 0x3f803f80" : "=v"(one2));
            const u32x4 ones = {one2, one2, one2, one2};
            const unsigned g = lane >> 4, q = (lane & 15) >> 2, p = lane & 3, krow = (l ? 4 : 8) * h + q, kd2 = l ? 8 : 4;
            u32x32 wf;
#define KVW(SKIP) do { const unsigned wa = lds0 + 131072 + wid * 8192 + (g & 1) * 4096 + krow * 32 + p * 8; SBAR(); \
                asm volatile("s_cmp_lg_u32 %[skip], 0\n\ts_cbranch_scc1 9f\n" KVW_BODY "9:" : "=&{v[64:95]}"(wf) \
                    : [ones] "v"(ones), [pc] "v"(lds0 + 131072 + wid * 8192 + 4096 + h * 16), [wa] "v"(wa), [wb] "v"(wa + kd2 * 32), [skip] "s"(SKIP) \
                    : "memory", "scc", "s72", "s73", "s74", "s75", "s76", "s77", "s78", "s79", "s80", "s81", "s82", "s83", "s84", "s85", "v144", "v145", "v146", "v147", "v148", "v149", "v150", "v151", "v152", "v153", "v154", "v155", "v156", "v157", "v158", "v159"); SBAR(); } while (0)
            u32x2 tag;
            asm volatile("v_mov_b32 v56, 0\n\tglobal_load_dwordx2 v[56:57], v56, %1 sc1" : "=&{v[56:57]}"(tag) : "s"(ws + (unsigned)(WS_SYNC)) : "memory");
            if (l == 0) {
                if (bid >= 248) {
#pragma unroll
                    for (int i = 0; i < 3; i++) st16(ws + (unsigned)(xcd * 12288 + i * 4096), tid * 16, u32x4{0, 0, 0, 0});
                }
                if (kvcu) {
                    glds<4, false>((const char*)wk + wid * 4096, voff, lds0 + 131072 + wid * 8192);
                    glds<4, false>((const char*)wv + wid * 4096, voff, lds0 + 131072 + wid * 8192 + 4096);
                }
                const unsigned rl = lane >> 4;
#pragma unroll
                for (int t = 0; t < 2; t++) {
                    const unsigned a = t ^ a1st;
                    const char* xs = (const char*)x + (T64 * 64 + a * 32) * 1024 + wid * 256;
#pragma unroll
                    for (int j = 0; j < 8; j++)
                        glds4<1, false>(xs + (j >> 2) * 16384, (4 * (j & 3) + rl) * 1024 + (((lane & 15) ^ (4 * (j & 3) + rl)) * 16), lds0 + a * 98304 + wid * 8192 + j * 1024);
                }
                KVW(c8 >> 1);
                if (bid >= 248) {
                    asm volatile("s_waitcnt vmcnt(16)\n\ts_barrier" : "+{v[56:57]}"(tag) :: "memory");
                    if (tid == 0) st8(ws + (unsigned)(WS_SYNC + 128 + xcd * 128), voff, u32x2{tag[0] + 1, tag[1]});
                }
                if (!kvcu) {
                    for (int j = wid * 16; j < wid * 16 + 16; j++) glds4<1, false>(wqs + j * 8192, vw, lds0 + 32768 + j * 1024);
                }
            } else {
                asm volatile("global_load_dwordx4 a[16:19], %0, %1\n\tglobal_load_dwordx4 a[20:23], %0, %1 offset:32\n\t"
                             "global_load_dwordx4 a[24:27], %0, %1 offset:64\n\tglobal_load_dwordx4 a[28:31], %0, %1 offset:96"
                             :: "v"(h * 16), "s"(ws + (unsigned)(WS_CQ + (bid * 4 + wid) * 128)) : "memory", "a16", "a17", "a18", "a19", "a20", "a21", "a22", "a23", "a24", "a25", "a26", "a27", "a28", "a29", "a30", "a31");
                KVW(c8 >> 1);
                wait_cnt(ws + (unsigned)(FL_X2 + T64 * 128), lane >> 6, 8u, wid);
                const char* xin = ws + (unsigned)(WS_X + (2 * LS) * 2097152 + (T64 * 64 + wid * 8) * 1024);
                glds<8, true>(xin + a1st * 32768, voff, lds0 + a1st * 98304 + wid * 8192);
                glds<8, true>(xin + (a1st ^ 1) * 32768, voff, lds0 + (a1st ^ 1) * 98304 + wid * 8192);
            }
            unsigned xo[8];
#pragma unroll
            for (int b = 0; b < 8; b++) xo[b] = lds0 + (l ? voff + b * 1024 : r * 256 + (((2 * b + h) ^ (r & 15)) * 16));
            float mu = 0.f, rstd = 0.f;
            if (kvcu) {
                const unsigned kb = c8 * 98304 + wid * 8192;
                SBAR();
#define KVX_OPS \
                    :: "{v[64:95]}"(wf), [x0] "v"(xo[0] + kb), [x1] "v"(xo[1] + kb), [x2] "v"(xo[2] + kb), [x3] "v"(xo[3] + kb), [x4] "v"(xo[4] + kb), \
                       [x5] "v"(xo[5] + kb), [x6] "v"(xo[6] + kb), [x7] "v"(xo[7] + kb), [ones] "v"(ones), [pk] "v"(lds0 + 131072 + wid * 8192 + voff), \
                       [ps] "v"(lds0 + 131072 + wid * 8192 + 4608 + lane * 8), [vwq] "v"(vw), [swq] "s"(wqs + wid * 131072), [mwq] "s"(lds0 + 32768 + wid * 16384) \
                    : "memory", "scc", "s72", "s73", "s74", "s75", "s76", "s77", "s78", "s79", "s80", "s81", "s82", "s83", "s84", "s85", "v96", "v97", "v98", "v99", "v100", "v101", "v102", "v103", "v104", "v105", "v106", "v107", "v108", "v109", "v110", "v111", "v112", "v113", "v114", "v115", "v116", "v117", "v118", "v119", "v120", "v121", "v122", "v123", "v124", "v125", "v126", "v127", "v128", "v129", "v130", "v131", "v132", "v133", "v134", "v135", "v136", "v137", "v138", "v139", "v140", "v141", "v142", "v143", "v144", "v145", "v146", "v147", "v148", "v149", "v150", "v151", "v152", "v153", "v154", "v155", "v156", "v157", "v158", "v159", "v160", "v161", "v162", "v163", "v164", "v165", "v166", "v167", "v168", "v169", "v170", "v171", "v172", "v173", "v174", "v175", "v176", "v177", "v178", "v179", "v180", "v181", "v182", "v183"
                if (l == 0) asm volatile(
        "v_mov_b32 v176, 0\n"
        "v_mov_b32 v177, 0\n"
        "v_mov_b32 v178, 0\n"
        "v_mov_b32 v179, 0\n"
        "s_waitcnt vmcnt(8)\n"
        "ds_read_b128 v[96:99], %[x0]\n"
        "ds_read_b128 v[100:103], %[x1]\n"
        "ds_read_b128 v[104:107], %[x2]\n"
        "ds_read_b128 v[108:111], %[x3]\n"
        "ds_read_b128 v[112:115], %[x4]\n"
        "ds_read_b128 v[116:119], %[x5]\n"
        "ds_read_b128 v[120:123], %[x6]\n"
        "ds_read_b128 v[124:127], %[x7]\n"
        "s_waitcnt lgkmcnt(7)\n"
        "v_mfma_f32_32x32x16_bf16 v[128:143], v[64:67], v[96:99], 0\n"
        "s_mov_b32 m0, %[mwq]\n"
        "s_mov_b64 s[74:75], %[swq]\n"
        "global_load_lds_dwordx4 %[vwq], s[74:75]\n"
        "v_lshlrev_b32 v180, 16, v96\n"
        "v_and_b32 v181, 0xffff0000, v96\n"
        "v_fmac_f32 v176, v180, v180\n"
        "v_fmac_f32 v177, v181, v181\n"
        "v_lshlrev_b32 v182, 16, v97\n"
        "v_and_b32 v183, 0xffff0000, v97\n"
        "v_fmac_f32 v178, v182, v182\n"
        "v_fmac_f32 v179, v183, v183\n"
        "v_mfma_f32_32x32x16_bf16 v[160:175], %[ones], v[96:99], 0\n"
        "v_lshlrev_b32 v180, 16, v98\n"
        "v_and_b32 v181, 0xffff0000, v98\n"
        "v_fmac_f32 v176, v180, v180\n"
        "v_fmac_f32 v177, v181, v181\n"
        "v_lshlrev_b32 v182, 16, v99\n"
        "v_and_b32 v183, 0xffff0000, v99\n"
        "v_fmac_f32 v178, v182, v182\n"
        "v_fmac_f32 v179, v183, v183\n"
        "s_waitcnt lgkmcnt(6)\n"
        "v_mfma_f32_32x32x16_bf16 v[128:143], v[68:71], v[100:103], v[128:143]\n"
        "s_add_u32 m0, m0, 1024\n"
        "s_add_u32 s74, s74, 8192\n"
        "s_addc_u32 s75, s75, 0\n"
        "global_load_lds_dwordx4 %[vwq], s[74:75]\n"
        "v_lshlrev_b32 v180, 16, v100\n"
        "v_and_b32 v181, 0xffff0000, v100\n"
        "v_fmac_f32 v176, v180, v180\n"
        "v_fmac_f32 v177, v181, v181\n"
        "v_lshlrev_b32 v182, 16, v101\n"
        "v_and_b32 v183, 0xffff0000, v101\n"
        "v_fmac_f32 v178, v182, v182\n"
        "v_fmac_f32 v179, v183, v183\n"
        "v_mfma_f32_32x32x16_bf16 v[160:175], %[ones], v[100:103], v[160:175]\n"
        "v_lshlrev_b32 v180, 16, v102\n"
        "v_and_b32 v181, 0xffff0000, v102\n"
        "v_fmac_f32 v176, v180, v180\n"
        "v_fmac_f32 v177, v181, v181\n"
        "v_lshlrev_b32 v182, 16, v103\n"
        "v_and_b32 v183, 0xffff0000, v103\n"
        "v_fmac_f32 v178, v182, v182\n"
        "v_fmac_f32 v179, v183, v183\n"
        "s_waitcnt lgkmcnt(5)\n"
        "v_mfma_f32_32x32x16_bf16 v[128:143], v[72:75], v[104:107], v[128:143]\n"
        "s_add_u32 m0, m0, 1024\n"
        "s_add_u32 s74, s74, 8192\n"
        "s_addc_u32 s75, s75, 0\n"
        "global_load_lds_dwordx4 %[vwq], s[74:75]\n"
        "v_lshlrev_b32 v180, 16, v104\n"
        "v_and_b32 v181, 0xffff0000, v104\n"
        "v_fmac_f32 v176, v180, v180\n"
        "v_fmac_f32 v177, v181, v181\n"
        "v_lshlrev_b32 v182, 16, v105\n"
        "v_and_b32 v183, 0xffff0000, v105\n"
        "v_fmac_f32 v178, v182, v182\n"
        "v_fmac_f32 v179, v183, v183\n"
        "v_mfma_f32_32x32x16_bf16 v[160:175], %[ones], v[104:107], v[160:175]\n"
        "v_lshlrev_b32 v180, 16, v106\n"
        "v_and_b32 v181, 0xffff0000, v106\n"
        "v_fmac_f32 v176, v180, v180\n"
        "v_fmac_f32 v177, v181, v181\n"
        "v_lshlrev_b32 v182, 16, v107\n"
        "v_and_b32 v183, 0xffff0000, v107\n"
        "v_fmac_f32 v178, v182, v182\n"
        "v_fmac_f32 v179, v183, v183\n"
        "s_waitcnt lgkmcnt(4)\n"
        "v_mfma_f32_32x32x16_bf16 v[128:143], v[76:79], v[108:111], v[128:143]\n"
        "s_add_u32 m0, m0, 1024\n"
        "s_add_u32 s74, s74, 8192\n"
        "s_addc_u32 s75, s75, 0\n"
        "global_load_lds_dwordx4 %[vwq], s[74:75]\n"
        "v_lshlrev_b32 v180, 16, v108\n"
        "v_and_b32 v181, 0xffff0000, v108\n"
        "v_fmac_f32 v176, v180, v180\n"
        "v_fmac_f32 v177, v181, v181\n"
        "v_lshlrev_b32 v182, 16, v109\n"
        "v_and_b32 v183, 0xffff0000, v109\n"
        "v_fmac_f32 v178, v182, v182\n"
        "v_fmac_f32 v179, v183, v183\n"
        "v_mfma_f32_32x32x16_bf16 v[160:175], %[ones], v[108:111], v[160:175]\n"
        "v_lshlrev_b32 v180, 16, v110\n"
        "v_and_b32 v181, 0xffff0000, v110\n"
        "v_fmac_f32 v176, v180, v180\n"
        "v_fmac_f32 v177, v181, v181\n"
        "v_lshlrev_b32 v182, 16, v111\n"
        "v_and_b32 v183, 0xffff0000, v111\n"
        "v_fmac_f32 v178, v182, v182\n"
        "v_fmac_f32 v179, v183, v183\n"
        "s_waitcnt lgkmcnt(3)\n"
        "v_mfma_f32_32x32x16_bf16 v[128:143], v[80:83], v[112:115], v[128:143]\n"
        "s_add_u32 m0, m0, 1024\n"
        "s_add_u32 s74, s74, 8192\n"
        "s_addc_u32 s75, s75, 0\n"
        "global_load_lds_dwordx4 %[vwq], s[74:75]\n"
        "v_lshlrev_b32 v180, 16, v112\n"
        "v_and_b32 v181, 0xffff0000, v112\n"
        "v_fmac_f32 v176, v180, v180\n"
        "v_fmac_f32 v177, v181, v181\n"
        "v_lshlrev_b32 v182, 16, v113\n"
        "v_and_b32 v183, 0xffff0000, v113\n"
        "v_fmac_f32 v178, v182, v182\n"
        "v_fmac_f32 v179, v183, v183\n"
        "v_mfma_f32_32x32x16_bf16 v[160:175], %[ones], v[112:115], v[160:175]\n"
        "v_lshlrev_b32 v180, 16, v114\n"
        "v_and_b32 v181, 0xffff0000, v114\n"
        "v_fmac_f32 v176, v180, v180\n"
        "v_fmac_f32 v177, v181, v181\n"
        "v_lshlrev_b32 v182, 16, v115\n"
        "v_and_b32 v183, 0xffff0000, v115\n"
        "v_fmac_f32 v178, v182, v182\n"
        "v_fmac_f32 v179, v183, v183\n"
        "s_waitcnt lgkmcnt(2)\n"
        "v_mfma_f32_32x32x16_bf16 v[128:143], v[84:87], v[116:119], v[128:143]\n"
        "s_add_u32 m0, m0, 1024\n"
        "s_add_u32 s74, s74, 8192\n"
        "s_addc_u32 s75, s75, 0\n"
        "global_load_lds_dwordx4 %[vwq], s[74:75]\n"
        "v_lshlrev_b32 v180, 16, v116\n"
        "v_and_b32 v181, 0xffff0000, v116\n"
        "v_fmac_f32 v176, v180, v180\n"
        "v_fmac_f32 v177, v181, v181\n"
        "v_lshlrev_b32 v182, 16, v117\n"
        "v_and_b32 v183, 0xffff0000, v117\n"
        "v_fmac_f32 v178, v182, v182\n"
        "v_fmac_f32 v179, v183, v183\n"
        "v_mfma_f32_32x32x16_bf16 v[160:175], %[ones], v[116:119], v[160:175]\n"
        "v_lshlrev_b32 v180, 16, v118\n"
        "v_and_b32 v181, 0xffff0000, v118\n"
        "v_fmac_f32 v176, v180, v180\n"
        "v_fmac_f32 v177, v181, v181\n"
        "v_lshlrev_b32 v182, 16, v119\n"
        "v_and_b32 v183, 0xffff0000, v119\n"
        "v_fmac_f32 v178, v182, v182\n"
        "v_fmac_f32 v179, v183, v183\n"
        "s_waitcnt lgkmcnt(1)\n"
        "v_mfma_f32_32x32x16_bf16 v[128:143], v[88:91], v[120:123], v[128:143]\n"
        "s_add_u32 m0, m0, 1024\n"
        "s_add_u32 s74, s74, 8192\n"
        "s_addc_u32 s75, s75, 0\n"
        "global_load_lds_dwordx4 %[vwq], s[74:75]\n"
        "v_lshlrev_b32 v180, 16, v120\n"
        "v_and_b32 v181, 0xffff0000, v120\n"
        "v_fmac_f32 v176, v180, v180\n"
        "v_fmac_f32 v177, v181, v181\n"
        "v_lshlrev_b32 v182, 16, v121\n"
        "v_and_b32 v183, 0xffff0000, v121\n"
        "v_fmac_f32 v178, v182, v182\n"
        "v_fmac_f32 v179, v183, v183\n"
        "v_mfma_f32_32x32x16_bf16 v[160:175], %[ones], v[120:123], v[160:175]\n"
        "v_lshlrev_b32 v180, 16, v122\n"
        "v_and_b32 v181, 0xffff0000, v122\n"
        "v_fmac_f32 v176, v180, v180\n"
        "v_fmac_f32 v177, v181, v181\n"
        "v_lshlrev_b32 v182, 16, v123\n"
        "v_and_b32 v183, 0xffff0000, v123\n"
        "v_fmac_f32 v178, v182, v182\n"
        "v_fmac_f32 v179, v183, v183\n"
        "s_waitcnt lgkmcnt(0)\n"
        "v_mfma_f32_32x32x16_bf16 v[128:143], v[92:95], v[124:127], v[128:143]\n"
        "s_add_u32 m0, m0, 1024\n"
        "s_add_u32 s74, s74, 8192\n"
        "s_addc_u32 s75, s75, 0\n"
        "global_load_lds_dwordx4 %[vwq], s[74:75]\n"
        "v_lshlrev_b32 v180, 16, v124\n"
        "v_and_b32 v181, 0xffff0000, v124\n"
        "v_fmac_f32 v176, v180, v180\n"
        "v_fmac_f32 v177, v181, v181\n"
        "v_lshlrev_b32 v182, 16, v125\n"
        "v_and_b32 v183, 0xffff0000, v125\n"
        "v_fmac_f32 v178, v182, v182\n"
        "v_fmac_f32 v179, v183, v183\n"
        "v_mfma_f32_32x32x16_bf16 v[160:175], %[ones], v[124:127], v[160:175]\n"
        "v_lshlrev_b32 v180, 16, v126\n"
        "v_and_b32 v181, 0xffff0000, v126\n"
        "v_fmac_f32 v176, v180, v180\n"
        "v_fmac_f32 v177, v181, v181\n"
        "v_lshlrev_b32 v182, 16, v127\n"
        "v_and_b32 v183, 0xffff0000, v127\n"
        "v_fmac_f32 v178, v182, v182\n"
        "v_fmac_f32 v179, v183, v183\n"
        "v_add_f32 v176, v176, v177\n"
        "v_add_f32 v178, v178, v179\n"
        "v_add_f32 v177, v176, v178\n"
        "ds_write_b128 %[pk], v[128:131] offset:0\n"
        "ds_write_b128 %[pk], v[132:135] offset:1024\n"
        "ds_write_b128 %[pk], v[136:139] offset:2048\n"
        "ds_write_b128 %[pk], v[140:143] offset:3072\n"
        "v_mov_b32 v176, v160\n"
        "ds_write_b64 %[ps], v[176:177]\n"
        "s_waitcnt lgkmcnt(0)\n"
        "s_barrier\n"
                    KVX_OPS);
                else asm volatile(
        "v_mov_b32 v176, 0\n"
        "v_mov_b32 v177, 0\n"
        "v_mov_b32 v178, 0\n"
        "v_mov_b32 v179, 0\n"
        "s_waitcnt vmcnt(8)\n"
        "ds_read_b128 v[96:99], %[x0]\n"
        "ds_read_b128 v[100:103], %[x1]\n"
        "ds_read_b128 v[104:107], %[x2]\n"
        "ds_read_b128 v[108:111], %[x3]\n"
        "ds_read_b128 v[112:115], %[x4]\n"
        "ds_read_b128 v[116:119], %[x5]\n"
        "ds_read_b128 v[120:123], %[x6]\n"
        "ds_read_b128 v[124:127], %[x7]\n"
        "s_waitcnt lgkmcnt(7)\n"
        "v_mfma_f32_32x32x16_bf16 v[128:143], v[64:67], v[96:99], 0\n"
        "v_lshlrev_b32 v180, 16, v96\n"
        "v_and_b32 v181, 0xffff0000, v96\n"
        "v_fmac_f32 v176, v180, v180\n"
        "v_fmac_f32 v177, v181, v181\n"
        "v_lshlrev_b32 v182, 16, v97\n"
        "v_and_b32 v183, 0xffff0000, v97\n"
        "v_fmac_f32 v178, v182, v182\n"
        "v_fmac_f32 v179, v183, v183\n"
        "v_mfma_f32_32x32x16_bf16 v[160:175], %[ones], v[96:99], 0\n"
        "v_lshlrev_b32 v180, 16, v98\n"
        "v_and_b32 v181, 0xffff0000, v98\n"
        "v_fmac_f32 v176, v180, v180\n"
        "v_fmac_f32 v177, v181, v181\n"
        "v_lshlrev_b32 v182, 16, v99\n"
        "v_and_b32 v183, 0xffff0000, v99\n"
        "v_fmac_f32 v178, v182, v182\n"
        "v_fmac_f32 v179, v183, v183\n"
        "s_waitcnt lgkmcnt(6)\n"
        "v_mfma_f32_32x32x16_bf16 v[128:143], v[68:71], v[100:103], v[128:143]\n"
        "v_lshlrev_b32 v180, 16, v100\n"
        "v_and_b32 v181, 0xffff0000, v100\n"
        "v_fmac_f32 v176, v180, v180\n"
        "v_fmac_f32 v177, v181, v181\n"
        "v_lshlrev_b32 v182, 16, v101\n"
        "v_and_b32 v183, 0xffff0000, v101\n"
        "v_fmac_f32 v178, v182, v182\n"
        "v_fmac_f32 v179, v183, v183\n"
        "v_mfma_f32_32x32x16_bf16 v[160:175], %[ones], v[100:103], v[160:175]\n"
        "v_lshlrev_b32 v180, 16, v102\n"
        "v_and_b32 v181, 0xffff0000, v102\n"
        "v_fmac_f32 v176, v180, v180\n"
        "v_fmac_f32 v177, v181, v181\n"
        "v_lshlrev_b32 v182, 16, v103\n"
        "v_and_b32 v183, 0xffff0000, v103\n"
        "v_fmac_f32 v178, v182, v182\n"
        "v_fmac_f32 v179, v183, v183\n"
        "s_waitcnt lgkmcnt(5)\n"
        "v_mfma_f32_32x32x16_bf16 v[128:143], v[72:75], v[104:107], v[128:143]\n"
        "v_lshlrev_b32 v180, 16, v104\n"
        "v_and_b32 v181, 0xffff0000, v104\n"
        "v_fmac_f32 v176, v180, v180\n"
        "v_fmac_f32 v177, v181, v181\n"
        "v_lshlrev_b32 v182, 16, v105\n"
        "v_and_b32 v183, 0xffff0000, v105\n"
        "v_fmac_f32 v178, v182, v182\n"
        "v_fmac_f32 v179, v183, v183\n"
        "v_mfma_f32_32x32x16_bf16 v[160:175], %[ones], v[104:107], v[160:175]\n"
        "v_lshlrev_b32 v180, 16, v106\n"
        "v_and_b32 v181, 0xffff0000, v106\n"
        "v_fmac_f32 v176, v180, v180\n"
        "v_fmac_f32 v177, v181, v181\n"
        "v_lshlrev_b32 v182, 16, v107\n"
        "v_and_b32 v183, 0xffff0000, v107\n"
        "v_fmac_f32 v178, v182, v182\n"
        "v_fmac_f32 v179, v183, v183\n"
        "s_waitcnt lgkmcnt(4)\n"
        "v_mfma_f32_32x32x16_bf16 v[128:143], v[76:79], v[108:111], v[128:143]\n"
        "v_lshlrev_b32 v180, 16, v108\n"
        "v_and_b32 v181, 0xffff0000, v108\n"
        "v_fmac_f32 v176, v180, v180\n"
        "v_fmac_f32 v177, v181, v181\n"
        "v_lshlrev_b32 v182, 16, v109\n"
        "v_and_b32 v183, 0xffff0000, v109\n"
        "v_fmac_f32 v178, v182, v182\n"
        "v_fmac_f32 v179, v183, v183\n"
        "v_mfma_f32_32x32x16_bf16 v[160:175], %[ones], v[108:111], v[160:175]\n"
        "v_lshlrev_b32 v180, 16, v110\n"
        "v_and_b32 v181, 0xffff0000, v110\n"
        "v_fmac_f32 v176, v180, v180\n"
        "v_fmac_f32 v177, v181, v181\n"
        "v_lshlrev_b32 v182, 16, v111\n"
        "v_and_b32 v183, 0xffff0000, v111\n"
        "v_fmac_f32 v178, v182, v182\n"
        "v_fmac_f32 v179, v183, v183\n"
        "s_waitcnt lgkmcnt(3)\n"
        "v_mfma_f32_32x32x16_bf16 v[128:143], v[80:83], v[112:115], v[128:143]\n"
        "v_lshlrev_b32 v180, 16, v112\n"
        "v_and_b32 v181, 0xffff0000, v112\n"
        "v_fmac_f32 v176, v180, v180\n"
        "v_fmac_f32 v177, v181, v181\n"
        "v_lshlrev_b32 v182, 16, v113\n"
        "v_and_b32 v183, 0xffff0000, v113\n"
        "v_fmac_f32 v178, v182, v182\n"
        "v_fmac_f32 v179, v183, v183\n"
        "v_mfma_f32_32x32x16_bf16 v[160:175], %[ones], v[112:115], v[160:175]\n"
        "v_lshlrev_b32 v180, 16, v114\n"
        "v_and_b32 v181, 0xffff0000, v114\n"
        "v_fmac_f32 v176, v180, v180\n"
        "v_fmac_f32 v177, v181, v181\n"
        "v_lshlrev_b32 v182, 16, v115\n"
        "v_and_b32 v183, 0xffff0000, v115\n"
        "v_fmac_f32 v178, v182, v182\n"
        "v_fmac_f32 v179, v183, v183\n"
        "s_waitcnt lgkmcnt(2)\n"
        "v_mfma_f32_32x32x16_bf16 v[128:143], v[84:87], v[116:119], v[128:143]\n"
        "v_lshlrev_b32 v180, 16, v116\n"
        "v_and_b32 v181, 0xffff0000, v116\n"
        "v_fmac_f32 v176, v180, v180\n"
        "v_fmac_f32 v177, v181, v181\n"
        "v_lshlrev_b32 v182, 16, v117\n"
        "v_and_b32 v183, 0xffff0000, v117\n"
        "v_fmac_f32 v178, v182, v182\n"
        "v_fmac_f32 v179, v183, v183\n"
        "v_mfma_f32_32x32x16_bf16 v[160:175], %[ones], v[116:119], v[160:175]\n"
        "v_lshlrev_b32 v180, 16, v118\n"
        "v_and_b32 v181, 0xffff0000, v118\n"
        "v_fmac_f32 v176, v180, v180\n"
        "v_fmac_f32 v177, v181, v181\n"
        "v_lshlrev_b32 v182, 16, v119\n"
        "v_and_b32 v183, 0xffff0000, v119\n"
        "v_fmac_f32 v178, v182, v182\n"
        "v_fmac_f32 v179, v183, v183\n"
        "s_waitcnt lgkmcnt(1)\n"
        "v_mfma_f32_32x32x16_bf16 v[128:143], v[88:91], v[120:123], v[128:143]\n"
        "v_lshlrev_b32 v180, 16, v120\n"
        "v_and_b32 v181, 0xffff0000, v120\n"
        "v_fmac_f32 v176, v180, v180\n"
        "v_fmac_f32 v177, v181, v181\n"
        "v_lshlrev_b32 v182, 16, v121\n"
        "v_and_b32 v183, 0xffff0000, v121\n"
        "v_fmac_f32 v178, v182, v182\n"
        "v_fmac_f32 v179, v183, v183\n"
        "v_mfma_f32_32x32x16_bf16 v[160:175], %[ones], v[120:123], v[160:175]\n"
        "v_lshlrev_b32 v180, 16, v122\n"
        "v_and_b32 v181, 0xffff0000, v122\n"
        "v_fmac_f32 v176, v180, v180\n"
        "v_fmac_f32 v177, v181, v181\n"
        "v_lshlrev_b32 v182, 16, v123\n"
        "v_and_b32 v183, 0xffff0000, v123\n"
        "v_fmac_f32 v178, v182, v182\n"
        "v_fmac_f32 v179, v183, v183\n"
        "s_waitcnt lgkmcnt(0)\n"
        "v_mfma_f32_32x32x16_bf16 v[128:143], v[92:95], v[124:127], v[128:143]\n"
        "v_lshlrev_b32 v180, 16, v124\n"
        "v_and_b32 v181, 0xffff0000, v124\n"
        "v_fmac_f32 v176, v180, v180\n"
        "v_fmac_f32 v177, v181, v181\n"
        "v_lshlrev_b32 v182, 16, v125\n"
        "v_and_b32 v183, 0xffff0000, v125\n"
        "v_fmac_f32 v178, v182, v182\n"
        "v_fmac_f32 v179, v183, v183\n"
        "v_mfma_f32_32x32x16_bf16 v[160:175], %[ones], v[124:127], v[160:175]\n"
        "v_lshlrev_b32 v180, 16, v126\n"
        "v_and_b32 v181, 0xffff0000, v126\n"
        "v_fmac_f32 v176, v180, v180\n"
        "v_fmac_f32 v177, v181, v181\n"
        "v_lshlrev_b32 v182, 16, v127\n"
        "v_and_b32 v183, 0xffff0000, v127\n"
        "v_fmac_f32 v178, v182, v182\n"
        "v_fmac_f32 v179, v183, v183\n"
        "v_add_f32 v176, v176, v177\n"
        "v_add_f32 v178, v178, v179\n"
        "v_add_f32 v177, v176, v178\n"
        "ds_write_b128 %[pk], v[128:131] offset:0\n"
        "ds_write_b128 %[pk], v[132:135] offset:1024\n"
        "ds_write_b128 %[pk], v[136:139] offset:2048\n"
        "ds_write_b128 %[pk], v[140:143] offset:3072\n"
        "v_mov_b32 v176, v160\n"
        "ds_write_b64 %[ps], v[176:177]\n"
        "s_waitcnt lgkmcnt(0)\n"
        "s_barrier\n"
                    KVX_OPS);
                SBAR();
                if (kvfin) {
                    const LAS f32x2* ps = (const LAS f32x2*)(uintptr_t)(lds0 + 131072 + 4608 + lane * 8);
                    const LAS f32x4* pc = (const LAS f32x4*)(uintptr_t)(lds0 + 131072 + 4096 + nsub * 64 + h * 16);
                    const LAS f32x4* pp = (const LAS f32x4*)(uintptr_t)(lds0 + 131072 + nsub * 2048 + voff);
                    f32x2 t4[4]; f32x4 c4[8], p4[8];
#pragma unroll
                    for (int w = 0; w < 4; w++) t4[w] = ps[w * 1024];
#pragma unroll
                    for (int w = 0; w < 4; w++) { p4[w] = pp[w * 512]; p4[4 + w] = pp[w * 512 + 64]; }
#pragma unroll
                    for (int w = 0; w < 4; w++) { c4[w] = pc[w * 512]; c4[4 + w] = pc[w * 512 + 2]; }
                    SBAR();
                    const f32x2 t = (t4[0] + t4[1]) + (t4[2] + t4[3]);
                    ln_stats(t[0], xsum(t[1]), mu, rstd);
                    const f32x4 a = (p4[0] + p4[1]) + (p4[2] + p4[3]), b = (p4[4] + p4[5]) + (p4[6] + p4[7]);
                    const f32x4 ca = (c4[0] + c4[1]) + (c4[2] + c4[3]), cb = (c4[4] + c4[5]) + (c4[6] + c4[7]);
                    const float c[8] = {ca[0], ca[1], ca[2], ca[3], cb[0], cb[1], cb[2], cb[3]};
                    const float av[8] = {a[0], a[1], a[2], a[3], b[0], b[1], b[2], b[3]};
                    if (nsub == 0) k_epilogue(av, mu, rstd, c, kv, tt, lane);
                    else v_epilogue(av, mu, rstd, c, kv, tt, lane, lds0 + 131072 + 6144);
                } else if (l == 0) {
                    for (int j = 0; j < 16; j++) { const int jj = nsub * 32 + (j >> 3) * 16 + 8 + (j & 7); glds4<1, false>(wqs + jj * 8192, vw, lds0 + 32768 + jj * 1024); }
                }
            }
            const int job = (int)(T64 * 6 + c8) - 2, nojob = (l | kvm | ((183 - job) >> 31)) & 1;
            const char *jpa = nullptr, *jpb = nullptr; int jld = 0; unsigned jrd = 0, jrd1 = 0, kdo = 0;
            jdst = nullptr; jcs = nullptr;
            if (!nojob) {
                const Job J = job_of(job, wid, wo, w1, w3, w2, ws);
                jpa = J.a; jpb = J.b; jld = J.ld2; jdst = J.dst; jcs = (const char*)J.cs;
                jrd = lds0 + (J.is13 ? (16 * h + 4 * (g & 1) + q) * 272 + (wid * 16 + 4 * p) * 2 : (4 * h + q) * 272 + (wid * 32 + (g & 1) * 16 + 4 * p) * 2);
                jrd1 = jrd + (J.is13 ? 128 : 16 * 272);
                const int k = J.w2k + (lane & 7), w = (k * 147) >> 12, rr = k - w * 28, g7 = (rr * 37) >> 8, tin = rr - g7 * 7;
                kdo = J.is13 ? ((lane & 7) >> 1) * J.kstride + (lane & 1) * 1024
                             : (J.w2k >= 0 ? w * 28 + (tin < 3 ? g7 * 3 + tin : 9 + g7 * 4 + tin) : (lane & 7)) * 1024;
            }
            jpa = uni(jpa); jpb = uni(jpb); jdst = uni(jdst); jcs = uni(jcs);
            asm volatile("" : "+s"(jpa), "+s"(jpb), "+s"(jdst), "+s"(jcs), "+v"(jrd), "+v"(jrd1), "+v"(kdo));
            {
                unsigned s0_;
                asm volatile("s_cmp_lg_u32 %3, 0\n\ts_cbranch_scc1 2f\n\ts_mov_b32 %0, 0x100000\n"
                             "1:\n\tglobal_load_dwordx2 v[58:59], %5, %2 sc1\n\ts_waitcnt vmcnt(0)\n\t"
                             "v_add_u32 v61, 1, v56\n\tv_xor_b32 v61, v61, v58\n\tv_xor_b32 v62, v57, v59\n\tv_or_b32 v61, v61, v62\n\t"
                             "v_cmp_ne_u32 vcc, 0, v61\n\ts_nop 4\n\ts_cbranch_vccz 2f\n\ts_sub_u32 %0, %0, 1\n\ts_cbranch_scc0 1b\n"
                             "2:\n\ts_waitcnt vmcnt(2) lgkmcnt(0)\n\ts_cmp_lg_u32 %4, 0\n\ts_cbranch_scc1 3f\n\ts_waitcnt vmcnt(0)\n3:\n\ts_barrier"
                             : "=&s"(s0_)
                             : "{v[56:57]}"(tag), "s"(ws + (unsigned)(WS_SYNC + 128)), "s"(l | (kvm & (wid ^ c8 ^ 1)) | (~kvm & wid)),
                               "s"(kvm & (1 ^ tsub ^ c8) & 1), "v"((lane & 7) * 128)
                             : "memory", "scc", "vcc", "v58", "v59", "v61", "v62");
            }
            asm volatile("" : "+{v[56:57]}"(tag));
            if (l == 1 && bid == 255 && tid == 0) st4(ws + (unsigned)(WS_SYNC), voff, tag[0] + 1);
            if (kvfin && nsub == 0 && h == 0) st8(ws + (unsigned)(WS_ST + l * 16384 + tt * 256), r * 8, u32x2{__float_as_uint(mu), __float_as_uint(rstd) | 1u});
            {
                const unsigned qb_ = tsub * 98304;
                const unsigned wa = lds0 + 32768 + krow * 128 + (((4 * nsub + 2 * (g & 1) + (p >> 1)) ^ (((q >> 1) & 1) << 2)) * 16) + (p & 1) * 8;
#define MMQ_OPS \
                    : "=&{v[192:223]}"(ja) : [vo] "v"((tid >> 3) * jld + (tid & 7) * 16), [sa] "s"(jpa), [sb] "s"(jpb), \
                       [st] "s"(__builtin_amdgcn_readfirstlane(jld * 32)), [skip] "s"(nojob), [x0] "v"(xo[0] + qb_), [x1] "v"(xo[1] + qb_), [x2] "v"(xo[2] + qb_), [x3] "v"(xo[3] + qb_), [x4] "v"(xo[4] + qb_), [x5] "v"(xo[5] + qb_), \
                       [x6] "v"(xo[6] + qb_), [x7] "v"(xo[7] + qb_), [ones] "v"(ones), [wa] "v"(wa), [wb] "v"(wa + kd2 * 128), \
                       [cnt] "s"(ws + (unsigned)(FL_KC + l * 1024 + xcd * 128)), [vgo] "v"(lane * 1024), [fin] "s"(kvm & (1 ^ tsub ^ c8) & 1), [go] "s"(ws + (unsigned)(FL_KV + l * 8192 + xcd * 128)), \
                       [vcq] "v"(h * 16), [scq] "s"(ws + (unsigned)(WS_CQ + (bid * 4 + wid) * 128)) \
                    : "memory", "scc", "s72", "s73", "s74", "s75", "s76", "s77", "s78", "s79", "s80", "s81", "s82", "s83", "s84", "s85", "v61", "v62", "v63", "v64", "v65", "v66", "v67", "v68", "v69", "v70", "v71", "v72", "v73", "v74", "v75", "v76", "v77", "v78", "v79", "v80", "v81", "v82", "v83", "v84", "v85", "v86", "v87", "v88", "v89", "v90", "v91", "v92", "v93", "v94", "v95", "v96", "v97", "v98", "v99", "v100", "v101", "v102", "v103", "v104", "v105", "v106", "v107", "v108", "v109", "v110", "v111", "v112", "v113", "v114", "v115", "v116", "v117", "v118", "v119", "v120", "v121", "v122", "v123", "v124", "v125", "v126", "v127"
                SBAR();
                if (l == 0) asm volatile(
        "ds_read_b64_tr_b16 v[64:65], %[wa] offset:0\n"
        "ds_read_b64_tr_b16 v[66:67], %[wb] offset:0\n"
        "ds_read_b128 v[68:71], %[x0] offset:0\n"
        "ds_read_b64_tr_b16 v[72:73], %[wa] offset:2048\n"
        "ds_read_b64_tr_b16 v[74:75], %[wb] offset:2048\n"
        "ds_read_b128 v[76:79], %[x1] offset:0\n"
        "ds_read_b64_tr_b16 v[80:81], %[wa] offset:4096\n"
        "ds_read_b64_tr_b16 v[82:83], %[wb] offset:4096\n"
        "ds_read_b128 v[84:87], %[x2] offset:0\n"
        "ds_read_b64_tr_b16 v[88:89], %[wa] offset:6144\n"
        "ds_read_b64_tr_b16 v[90:91], %[wb] offset:6144\n"
        "ds_read_b128 v[92:95], %[x3] offset:0\n"
        "s_waitcnt lgkmcnt(9)\n"
        "v_mfma_f32_32x32x16_bf16 a[0:15], v[64:67], v[68:71], 0\n"
        "ds_read_b64_tr_b16 v[96:97], %[wa] offset:8192\n"
        "ds_read_b64_tr_b16 v[98:99], %[wb] offset:8192\n"
        "ds_read_b128 v[100:103], %[x4] offset:0\n"
        "v_mfma_f32_32x32x16_bf16 a[16:31], v[64:67], %[ones], 0\n"
        "s_waitcnt lgkmcnt(9)\n"
        "v_mfma_f32_32x32x16_bf16 a[0:15], v[72:75], v[76:79], a[0:15]\n"
        "ds_read_b64_tr_b16 v[64:65], %[wa] offset:10240\n"
        "ds_read_b64_tr_b16 v[66:67], %[wb] offset:10240\n"
        "ds_read_b128 v[68:71], %[x5] offset:0\n"
        "v_mfma_f32_32x32x16_bf16 a[16:31], v[72:75], %[ones], a[16:31]\n"
        "s_cmp_lg_u32 %[skip], 0\n"
        "s_cbranch_scc1 2f\n"
        "s_mov_b64 s[76:77], %[sa]\n"
        "global_load_dwordx4 v[192:195], %[vo], s[76:77]\n"
        "s_add_u32 s76, s76, %[st]\n"
        "s_addc_u32 s77, s77, 0\n"
        "global_load_dwordx4 v[196:199], %[vo], s[76:77]\n"
        "s_add_u32 s76, s76, %[st]\n"
        "s_addc_u32 s77, s77, 0\n"
        "global_load_dwordx4 v[200:203], %[vo], s[76:77]\n"
        "s_add_u32 s76, s76, %[st]\n"
        "s_addc_u32 s77, s77, 0\n"
        "global_load_dwordx4 v[204:207], %[vo], s[76:77]\n"
        "2:\n"
        "s_waitcnt lgkmcnt(9)\n"
        "v_mfma_f32_32x32x16_bf16 a[0:15], v[80:83], v[84:87], a[0:15]\n"
        "ds_read_b64_tr_b16 v[72:73], %[wa] offset:12288\n"
        "ds_read_b64_tr_b16 v[74:75], %[wb] offset:12288\n"
        "ds_read_b128 v[76:79], %[x6] offset:0\n"
        "v_mfma_f32_32x32x16_bf16 a[16:31], v[80:83], %[ones], a[16:31]\n"
        "s_waitcnt lgkmcnt(9)\n"
        "v_mfma_f32_32x32x16_bf16 a[0:15], v[88:91], v[92:95], a[0:15]\n"
        "ds_read_b64_tr_b16 v[80:81], %[wa] offset:14336\n"
        "ds_read_b64_tr_b16 v[82:83], %[wb] offset:14336\n"
        "ds_read_b128 v[84:87], %[x7] offset:0\n"
        "v_mfma_f32_32x32x16_bf16 a[16:31], v[88:91], %[ones], a[16:31]\n"
        "s_cmp_lg_u32 %[skip], 0\n"
        "s_cbranch_scc1 2f\n"
        "s_mov_b64 s[78:79], %[sb]\n"
        "global_load_dwordx4 v[208:211], %[vo], s[78:79]\n"
        "s_add_u32 s78, s78, %[st]\n"
        "s_addc_u32 s79, s79, 0\n"
        "global_load_dwordx4 v[212:215], %[vo], s[78:79]\n"
        "s_add_u32 s78, s78, %[st]\n"
        "s_addc_u32 s79, s79, 0\n"
        "global_load_dwordx4 v[216:219], %[vo], s[78:79]\n"
        "s_add_u32 s78, s78, %[st]\n"
        "s_addc_u32 s79, s79, 0\n"
        "global_load_dwordx4 v[220:223], %[vo], s[78:79]\n"
        "2:\n"
        "s_waitcnt lgkmcnt(9)\n"
        "v_mfma_f32_32x32x16_bf16 a[0:15], v[96:99], v[100:103], a[0:15]\n"
        "ds_read_b64_tr_b16 v[88:89], %[wa] offset:16384\n"
        "ds_read_b64_tr_b16 v[90:91], %[wb] offset:16384\n"
        "ds_read_b128 v[92:95], %[x0] offset:8192\n"
        "v_mfma_f32_32x32x16_bf16 a[16:31], v[96:99], %[ones], a[16:31]\n"
        "s_waitcnt lgkmcnt(9)\n"
        "v_mfma_f32_32x32x16_bf16 a[0:15], v[64:67], v[68:71], a[0:15]\n"
        "ds_read_b64_tr_b16 v[96:97], %[wa] offset:18432\n"
        "ds_read_b64_tr_b16 v[98:99], %[wb] offset:18432\n"
        "ds_read_b128 v[100:103], %[x1] offset:8192\n"
        "v_mfma_f32_32x32x16_bf16 a[16:31], v[64:67], %[ones], a[16:31]\n"
        "s_waitcnt lgkmcnt(9)\n"
        "v_mfma_f32_32x32x16_bf16 a[0:15], v[72:75], v[76:79], a[0:15]\n"
        "ds_read_b64_tr_b16 v[64:65], %[wa] offset:20480\n"
        "ds_read_b64_tr_b16 v[66:67], %[wb] offset:20480\n"
        "ds_read_b128 v[68:71], %[x2] offset:8192\n"
        "v_mfma_f32_32x32x16_bf16 a[16:31], v[72:75], %[ones], a[16:31]\n"
        "s_cmp_eq_u32 %[fin], 0\n"
        "s_cbranch_scc1 1f\n"
        "s_waitcnt vmcnt(0)\n"
        "v_mov_b32 v63, 1\n"
        "v_mov_b32 v62, 0\n"
        "s_mov_b64 s[74:75], exec\n"
        "s_mov_b64 exec, 1\n"
        "global_atomic_add v61, v62, v63, %[cnt] sc0 sc1\n"
        "s_mov_b64 exec, s[74:75]\n"
        "1:\n"
        "s_waitcnt lgkmcnt(9)\n"
        "v_mfma_f32_32x32x16_bf16 a[0:15], v[80:83], v[84:87], a[0:15]\n"
        "ds_read_b64_tr_b16 v[72:73], %[wa] offset:22528\n"
        "ds_read_b64_tr_b16 v[74:75], %[wb] offset:22528\n"
        "ds_read_b128 v[76:79], %[x3] offset:8192\n"
        "v_mfma_f32_32x32x16_bf16 a[16:31], v[80:83], %[ones], a[16:31]\n"
        "s_waitcnt lgkmcnt(9)\n"
        "v_mfma_f32_32x32x16_bf16 a[0:15], v[88:91], v[92:95], a[0:15]\n"
        "ds_read_b64_tr_b16 v[80:81], %[wa] offset:24576\n"
        "ds_read_b64_tr_b16 v[82:83], %[wb] offset:24576\n"
        "ds_read_b128 v[84:87], %[x4] offset:8192\n"
        "v_mfma_f32_32x32x16_bf16 a[16:31], v[88:91], %[ones], a[16:31]\n"
        "s_waitcnt lgkmcnt(9)\n"
        "v_mfma_f32_32x32x16_bf16 a[0:15], v[96:99], v[100:103], a[0:15]\n"
        "ds_read_b64_tr_b16 v[88:89], %[wa] offset:26624\n"
        "ds_read_b64_tr_b16 v[90:91], %[wb] offset:26624\n"
        "ds_read_b128 v[92:95], %[x5] offset:8192\n"
        "v_mfma_f32_32x32x16_bf16 a[16:31], v[96:99], %[ones], a[16:31]\n"
        "s_waitcnt lgkmcnt(9)\n"
        "v_mfma_f32_32x32x16_bf16 a[0:15], v[64:67], v[68:71], a[0:15]\n"
        "ds_read_b64_tr_b16 v[96:97], %[wa] offset:28672\n"
        "ds_read_b64_tr_b16 v[98:99], %[wb] offset:28672\n"
        "ds_read_b128 v[100:103], %[x6] offset:8192\n"
        "v_mfma_f32_32x32x16_bf16 a[16:31], v[64:67], %[ones], a[16:31]\n"
        "s_waitcnt lgkmcnt(9)\n"
        "v_mfma_f32_32x32x16_bf16 a[0:15], v[72:75], v[76:79], a[0:15]\n"
        "ds_read_b64_tr_b16 v[64:65], %[wa] offset:30720\n"
        "ds_read_b64_tr_b16 v[66:67], %[wb] offset:30720\n"
        "ds_read_b128 v[68:71], %[x7] offset:8192\n"
        "v_mfma_f32_32x32x16_bf16 a[16:31], v[72:75], %[ones], a[16:31]\n"
        "s_waitcnt lgkmcnt(9)\n"
        "v_mfma_f32_32x32x16_bf16 a[0:15], v[80:83], v[84:87], a[0:15]\n"
        "ds_read_b64_tr_b16 v[72:73], %[wa] offset:32768\n"
        "ds_read_b64_tr_b16 v[74:75], %[wb] offset:32768\n"
        "ds_read_b128 v[76:79], %[x0] offset:16384\n"
        "v_mfma_f32_32x32x16_bf16 a[16:31], v[80:83], %[ones], a[16:31]\n"
        "s_waitcnt lgkmcnt(9)\n"
        "v_mfma_f32_32x32x16_bf16 a[0:15], v[88:91], v[92:95], a[0:15]\n"
        "ds_read_b64_tr_b16 v[80:81], %[wa] offset:34816\n"
        "ds_read_b64_tr_b16 v[82:83], %[wb] offset:34816\n"
        "ds_read_b128 v[84:87], %[x1] offset:16384\n"
        "v_mfma_f32_32x32x16_bf16 a[16:31], v[88:91], %[ones], a[16:31]\n"
        "s_waitcnt lgkmcnt(9)\n"
        "v_mfma_f32_32x32x16_bf16 a[0:15], v[96:99], v[100:103], a[0:15]\n"
        "ds_read_b64_tr_b16 v[88:89], %[wa] offset:36864\n"
        "ds_read_b64_tr_b16 v[90:91], %[wb] offset:36864\n"
        "ds_read_b128 v[92:95], %[x2] offset:16384\n"
        "v_mfma_f32_32x32x16_bf16 a[16:31], v[96:99], %[ones], a[16:31]\n"
        "s_waitcnt lgkmcnt(9)\n"
        "v_mfma_f32_32x32x16_bf16 a[0:15], v[64:67], v[68:71], a[0:15]\n"
        "ds_read_b64_tr_b16 v[96:97], %[wa] offset:38912\n"
        "ds_read_b64_tr_b16 v[98:99], %[wb] offset:38912\n"
        "ds_read_b128 v[100:103], %[x3] offset:16384\n"
        "v_mfma_f32_32x32x16_bf16 a[16:31], v[64:67], %[ones], a[16:31]\n"
        "s_waitcnt lgkmcnt(9)\n"
        "v_mfma_f32_32x32x16_bf16 a[0:15], v[72:75], v[76:79], a[0:15]\n"
        "ds_read_b64_tr_b16 v[64:65], %[wa] offset:40960\n"
        "ds_read_b64_tr_b16 v[66:67], %[wb] offset:40960\n"
        "ds_read_b128 v[68:71], %[x4] offset:16384\n"
        "v_mfma_f32_32x32x16_bf16 a[16:31], v[72:75], %[ones], a[16:31]\n"
        "s_waitcnt lgkmcnt(9)\n"
        "v_mfma_f32_32x32x16_bf16 a[0:15], v[80:83], v[84:87], a[0:15]\n"
        "ds_read_b64_tr_b16 v[72:73], %[wa] offset:43008\n"
        "ds_read_b64_tr_b16 v[74:75], %[wb] offset:43008\n"
        "ds_read_b128 v[76:79], %[x5] offset:16384\n"
        "v_mfma_f32_32x32x16_bf16 a[16:31], v[80:83], %[ones], a[16:31]\n"
        "s_cmp_eq_u32 %[fin], 0\n"
        "s_cbranch_scc1 1f\n"
        "s_waitcnt vmcnt(0)\n"
        "v_readfirstlane_b32 s73, v61\n"
        "s_cmp_lg_u32 s73, 15\n"
        "s_cbranch_scc1 1f\n"
        "v_mov_b32 v63, 1\n"
        "s_mov_b64 s[74:75], exec\n"
        "s_mov_b64 exec, 0xff\n"
        "global_store_dword %[vgo], v63, %[go] sc1\n"
        "s_mov_b64 exec, s[74:75]\n"
        "1:\n"
        "s_waitcnt lgkmcnt(9)\n"
        "v_mfma_f32_32x32x16_bf16 a[0:15], v[88:91], v[92:95], a[0:15]\n"
        "ds_read_b64_tr_b16 v[80:81], %[wa] offset:45056\n"
        "ds_read_b64_tr_b16 v[82:83], %[wb] offset:45056\n"
        "ds_read_b128 v[84:87], %[x6] offset:16384\n"
        "v_mfma_f32_32x32x16_bf16 a[16:31], v[88:91], %[ones], a[16:31]\n"
        "s_waitcnt lgkmcnt(9)\n"
        "v_mfma_f32_32x32x16_bf16 a[0:15], v[96:99], v[100:103], a[0:15]\n"
        "ds_read_b64_tr_b16 v[88:89], %[wa] offset:47104\n"
        "ds_read_b64_tr_b16 v[90:91], %[wb] offset:47104\n"
        "ds_read_b128 v[92:95], %[x7] offset:16384\n"
        "v_mfma_f32_32x32x16_bf16 a[16:31], v[96:99], %[ones], a[16:31]\n"
        "s_waitcnt lgkmcnt(9)\n"
        "v_mfma_f32_32x32x16_bf16 a[0:15], v[64:67], v[68:71], a[0:15]\n"
        "ds_read_b64_tr_b16 v[96:97], %[wa] offset:49152\n"
        "ds_read_b64_tr_b16 v[98:99], %[wb] offset:49152\n"
        "ds_read_b128 v[100:103], %[x0] offset:24576\n"
        "v_mfma_f32_32x32x16_bf16 a[16:31], v[64:67], %[ones], a[16:31]\n"
        "s_waitcnt lgkmcnt(9)\n"
        "v_mfma_f32_32x32x16_bf16 a[0:15], v[72:75], v[76:79], a[0:15]\n"
        "ds_read_b64_tr_b16 v[64:65], %[wa] offset:51200\n"
        "ds_read_b64_tr_b16 v[66:67], %[wb] offset:51200\n"
        "ds_read_b128 v[68:71], %[x1] offset:24576\n"
        "v_mfma_f32_32x32x16_bf16 a[16:31], v[72:75], %[ones], a[16:31]\n"
        "s_waitcnt lgkmcnt(9)\n"
        "v_mfma_f32_32x32x16_bf16 a[0:15], v[80:83], v[84:87], a[0:15]\n"
        "ds_read_b64_tr_b16 v[72:73], %[wa] offset:53248\n"
        "ds_read_b64_tr_b16 v[74:75], %[wb] offset:53248\n"
        "ds_read_b128 v[76:79], %[x2] offset:24576\n"
        "v_mfma_f32_32x32x16_bf16 a[16:31], v[80:83], %[ones], a[16:31]\n"
        "s_waitcnt lgkmcnt(9)\n"
        "v_mfma_f32_32x32x16_bf16 a[0:15], v[88:91], v[92:95], a[0:15]\n"
        "ds_read_b64_tr_b16 v[80:81], %[wa] offset:55296\n"
        "ds_read_b64_tr_b16 v[82:83], %[wb] offset:55296\n"
        "ds_read_b128 v[84:87], %[x3] offset:24576\n"
        "v_mfma_f32_32x32x16_bf16 a[16:31], v[88:91], %[ones], a[16:31]\n"
        "s_waitcnt lgkmcnt(9)\n"
        "v_mfma_f32_32x32x16_bf16 a[0:15], v[96:99], v[100:103], a[0:15]\n"
        "ds_read_b64_tr_b16 v[88:89], %[wa] offset:57344\n"
        "ds_read_b64_tr_b16 v[90:91], %[wb] offset:57344\n"
        "ds_read_b128 v[92:95], %[x4] offset:24576\n"
        "v_mfma_f32_32x32x16_bf16 a[16:31], v[96:99], %[ones], a[16:31]\n"
        "s_waitcnt lgkmcnt(9)\n"
        "v_mfma_f32_32x32x16_bf16 a[0:15], v[64:67], v[68:71], a[0:15]\n"
        "ds_read_b64_tr_b16 v[96:97], %[wa] offset:59392\n"
        "ds_read_b64_tr_b16 v[98:99], %[wb] offset:59392\n"
        "ds_read_b128 v[100:103], %[x5] offset:24576\n"
        "v_mfma_f32_32x32x16_bf16 a[16:31], v[64:67], %[ones], a[16:31]\n"
        "s_waitcnt lgkmcnt(9)\n"
        "v_mfma_f32_32x32x16_bf16 a[0:15], v[72:75], v[76:79], a[0:15]\n"
        "ds_read_b64_tr_b16 v[64:65], %[wa] offset:61440\n"
        "ds_read_b64_tr_b16 v[66:67], %[wb] offset:61440\n"
        "ds_read_b128 v[68:71], %[x6] offset:24576\n"
        "v_mfma_f32_32x32x16_bf16 a[16:31], v[72:75], %[ones], a[16:31]\n"
        "s_waitcnt lgkmcnt(9)\n"
        "v_mfma_f32_32x32x16_bf16 a[0:15], v[80:83], v[84:87], a[0:15]\n"
        "ds_read_b64_tr_b16 v[72:73], %[wa] offset:63488\n"
        "ds_read_b64_tr_b16 v[74:75], %[wb] offset:63488\n"
        "ds_read_b128 v[76:79], %[x7] offset:24576\n"
        "v_mfma_f32_32x32x16_bf16 a[16:31], v[80:83], %[ones], a[16:31]\n"
        "s_waitcnt lgkmcnt(9)\n"
        "v_mfma_f32_32x32x16_bf16 a[0:15], v[88:91], v[92:95], a[0:15]\n"
        "v_mfma_f32_32x32x16_bf16 a[16:31], v[88:91], %[ones], a[16:31]\n"
        "s_waitcnt lgkmcnt(6)\n"
        "v_mfma_f32_32x32x16_bf16 a[0:15], v[96:99], v[100:103], a[0:15]\n"
        "v_mfma_f32_32x32x16_bf16 a[16:31], v[96:99], %[ones], a[16:31]\n"
        "s_waitcnt lgkmcnt(3)\n"
        "v_mfma_f32_32x32x16_bf16 a[0:15], v[64:67], v[68:71], a[0:15]\n"
        "v_mfma_f32_32x32x16_bf16 a[16:31], v[64:67], %[ones], a[16:31]\n"
        "s_waitcnt lgkmcnt(0)\n"
        "v_mfma_f32_32x32x16_bf16 a[0:15], v[72:75], v[76:79], a[0:15]\n"
        "v_mfma_f32_32x32x16_bf16 a[16:31], v[72:75], %[ones], a[16:31]\n"
        "s_nop 7\n"
        "s_nop 7\n"
        "s_mov_b64 s[74:75], exec\n"
        "s_mov_b32 exec_lo, 1\n"
        "s_mov_b32 exec_hi, 1\n"
        "global_store_dwordx4 %[vcq], a[16:19], %[scq] offset:0\n"
        "global_store_dwordx4 %[vcq], a[20:23], %[scq] offset:32\n"
        "global_store_dwordx4 %[vcq], a[24:27], %[scq] offset:64\n"
        "global_store_dwordx4 %[vcq], a[28:31], %[scq] offset:96\n"
        "s_mov_b64 exec, s[74:75]\n"
                    MMQ_OPS, "a0", "a1", "a2", "a3", "a4", "a5", "a6", "a7", "a8", "a9", "a10", "a11", "a12", "a13", "a14", "a15", "a16", "a17", "a18", "a19", "a20", "a21", "a22", "a23", "a24", "a25", "a26", "a27", "a28", "a29", "a30", "a31");
                else asm volatile(
        "ds_read_b64_tr_b16 v[64:65], %[wa] offset:0\n"
        "ds_read_b64_tr_b16 v[66:67], %[wb] offset:0\n"
        "ds_read_b128 v[68:71], %[x0] offset:0\n"
        "ds_read_b64_tr_b16 v[72:73], %[wa] offset:2048\n"
        "ds_read_b64_tr_b16 v[74:75], %[wb] offset:2048\n"
        "ds_read_b128 v[76:79], %[x1] offset:0\n"
        "ds_read_b64_tr_b16 v[80:81], %[wa] offset:4096\n"
        "ds_read_b64_tr_b16 v[82:83], %[wb] offset:4096\n"
        "ds_read_b128 v[84:87], %[x2] offset:0\n"
        "ds_read_b64_tr_b16 v[88:89], %[wa] offset:6144\n"
        "ds_read_b64_tr_b16 v[90:91], %[wb] offset:6144\n"
        "ds_read_b128 v[92:95], %[x3] offset:0\n"
        "s_waitcnt lgkmcnt(9)\n"
        "v_mfma_f32_32x32x16_bf16 a[0:15], v[64:67], v[68:71], 0\n"
        "ds_read_b64_tr_b16 v[96:97], %[wa] offset:8192\n"
        "ds_read_b64_tr_b16 v[98:99], %[wb] offset:8192\n"
        "ds_read_b128 v[100:103], %[x4] offset:0\n"
        "s_waitcnt lgkmcnt(9)\n"
        "v_mfma_f32_32x32x16_bf16 a[0:15], v[72:75], v[76:79], a[0:15]\n"
        "ds_read_b64_tr_b16 v[64:65], %[wa] offset:10240\n"
        "ds_read_b64_tr_b16 v[66:67], %[wb] offset:10240\n"
        "ds_read_b128 v[68:71], %[x5] offset:0\n"
        "s_waitcnt lgkmcnt(9)\n"
        "v_mfma_f32_32x32x16_bf16 a[0:15], v[80:83], v[84:87], a[0:15]\n"
        "ds_read_b64_tr_b16 v[72:73], %[wa] offset:12288\n"
        "ds_read_b64_tr_b16 v[74:75], %[wb] offset:12288\n"
        "ds_read_b128 v[76:79], %[x6] offset:0\n"
        "s_waitcnt lgkmcnt(9)\n"
        "v_mfma_f32_32x32x16_bf16 a[0:15], v[88:91], v[92:95], a[0:15]\n"
        "ds_read_b64_tr_b16 v[80:81], %[wa] offset:14336\n"
        "ds_read_b64_tr_b16 v[82:83], %[wb] offset:14336\n"
        "ds_read_b128 v[84:87], %[x7] offset:0\n"
        "s_waitcnt lgkmcnt(9)\n"
        "v_mfma_f32_32x32x16_bf16 a[0:15], v[96:99], v[100:103], a[0:15]\n"
        "ds_read_b64_tr_b16 v[88:89], %[wa] offset:16384\n"
        "ds_read_b64_tr_b16 v[90:91], %[wb] offset:16384\n"
        "ds_read_b128 v[92:95], %[x0] offset:8192\n"
        "s_waitcnt lgkmcnt(9)\n"
        "v_mfma_f32_32x32x16_bf16 a[0:15], v[64:67], v[68:71], a[0:15]\n"
        "ds_read_b64_tr_b16 v[96:97], %[wa] offset:18432\n"
        "ds_read_b64_tr_b16 v[98:99], %[wb] offset:18432\n"
        "ds_read_b128 v[100:103], %[x1] offset:8192\n"
        "s_waitcnt lgkmcnt(9)\n"
        "v_mfma_f32_32x32x16_bf16 a[0:15], v[72:75], v[76:79], a[0:15]\n"
        "ds_read_b64_tr_b16 v[64:65], %[wa] offset:20480\n"
        "ds_read_b64_tr_b16 v[66:67], %[wb] offset:20480\n"
        "ds_read_b128 v[68:71], %[x2] offset:8192\n"
        "s_waitcnt lgkmcnt(9)\n"
        "v_mfma_f32_32x32x16_bf16 a[0:15], v[80:83], v[84:87], a[0:15]\n"
        "ds_read_b64_tr_b16 v[72:73], %[wa] offset:22528\n"
        "ds_read_b64_tr_b16 v[74:75], %[wb] offset:22528\n"
        "ds_read_b128 v[76:79], %[x3] offset:8192\n"
        "s_waitcnt lgkmcnt(9)\n"
        "v_mfma_f32_32x32x16_bf16 a[0:15], v[88:91], v[92:95], a[0:15]\n"
        "ds_read_b64_tr_b16 v[80:81], %[wa] offset:24576\n"
        "ds_read_b64_tr_b16 v[82:83], %[wb] offset:24576\n"
        "ds_read_b128 v[84:87], %[x4] offset:8192\n"
        "s_waitcnt lgkmcnt(9)\n"
        "v_mfma_f32_32x32x16_bf16 a[0:15], v[96:99], v[100:103], a[0:15]\n"
        "ds_read_b64_tr_b16 v[88:89], %[wa] offset:26624\n"
        "ds_read_b64_tr_b16 v[90:91], %[wb] offset:26624\n"
        "ds_read_b128 v[92:95], %[x5] offset:8192\n"
        "s_waitcnt lgkmcnt(9)\n"
        "v_mfma_f32_32x32x16_bf16 a[0:15], v[64:67], v[68:71], a[0:15]\n"
        "ds_read_b64_tr_b16 v[96:97], %[wa] offset:28672\n"
        "ds_read_b64_tr_b16 v[98:99], %[wb] offset:28672\n"
        "ds_read_b128 v[100:103], %[x6] offset:8192\n"
        "s_waitcnt lgkmcnt(9)\n"
        "v_mfma_f32_32x32x16_bf16 a[0:15], v[72:75], v[76:79], a[0:15]\n"
        "ds_read_b64_tr_b16 v[64:65], %[wa] offset:30720\n"
        "ds_read_b64_tr_b16 v[66:67], %[wb] offset:30720\n"
        "ds_read_b128 v[68:71], %[x7] offset:8192\n"
        "s_waitcnt lgkmcnt(9)\n"
        "v_mfma_f32_32x32x16_bf16 a[0:15], v[80:83], v[84:87], a[0:15]\n"
        "ds_read_b64_tr_b16 v[72:73], %[wa] offset:32768\n"
        "ds_read_b64_tr_b16 v[74:75], %[wb] offset:32768\n"
        "ds_read_b128 v[76:79], %[x0] offset:16384\n"
        "s_cmp_eq_u32 %[fin], 0\n"
        "s_cbranch_scc1 1f\n"
        "s_waitcnt vmcnt(0)\n"
        "v_mov_b32 v63, 1\n"
        "v_mov_b32 v62, 0\n"
        "s_mov_b64 s[74:75], exec\n"
        "s_mov_b64 exec, 1\n"
        "global_atomic_add v61, v62, v63, %[cnt] sc0 sc1\n"
        "s_mov_b64 exec, s[74:75]\n"
        "1:\n"
        "s_waitcnt lgkmcnt(9)\n"
        "v_mfma_f32_32x32x16_bf16 a[0:15], v[88:91], v[92:95], a[0:15]\n"
        "ds_read_b64_tr_b16 v[80:81], %[wa] offset:34816\n"
        "ds_read_b64_tr_b16 v[82:83], %[wb] offset:34816\n"
        "ds_read_b128 v[84:87], %[x1] offset:16384\n"
        "s_waitcnt lgkmcnt(9)\n"
        "v_mfma_f32_32x32x16_bf16 a[0:15], v[96:99], v[100:103], a[0:15]\n"
        "ds_read_b64_tr_b16 v[88:89], %[wa] offset:36864\n"
        "ds_read_b64_tr_b16 v[90:91], %[wb] offset:36864\n"
        "ds_read_b128 v[92:95], %[x2] offset:16384\n"
        "s_waitcnt lgkmcnt(9)\n"
        "v_mfma_f32_32x32x16_bf16 a[0:15], v[64:67], v[68:71], a[0:15]\n"
        "ds_read_b64_tr_b16 v[96:97], %[wa] offset:38912\n"
        "ds_read_b64_tr_b16 v[98:99], %[wb] offset:38912\n"
        "ds_read_b128 v[100:103], %[x3] offset:16384\n"
        "s_waitcnt lgkmcnt(9)\n"
        "v_mfma_f32_32x32x16_bf16 a[0:15], v[72:75], v[76:79], a[0:15]\n"
        "ds_read_b64_tr_b16 v[64:65], %[wa] offset:40960\n"
        "ds_read_b64_tr_b16 v[66:67], %[wb] offset:40960\n"
        "ds_read_b128 v[68:71], %[x4] offset:16384\n"
        "s_waitcnt lgkmcnt(9)\n"
        "v_mfma_f32_32x32x16_bf16 a[0:15], v[80:83], v[84:87], a[0:15]\n"
        "ds_read_b64_tr_b16 v[72:73], %[wa] offset:43008\n"
        "ds_read_b64_tr_b16 v[74:75], %[wb] offset:43008\n"
        "ds_read_b128 v[76:79], %[x5] offset:16384\n"
        "s_waitcnt lgkmcnt(9)\n"
        "v_mfma_f32_32x32x16_bf16 a[0:15], v[88:91], v[92:95], a[0:15]\n"
        "ds_read_b64_tr_b16 v[80:81], %[wa] offset:45056\n"
        "ds_read_b64_tr_b16 v[82:83], %[wb] offset:45056\n"
        "ds_read_b128 v[84:87], %[x6] offset:16384\n"
        "s_waitcnt lgkmcnt(9)\n"
        "v_mfma_f32_32x32x16_bf16 a[0:15], v[96:99], v[100:103], a[0:15]\n"
        "ds_read_b64_tr_b16 v[88:89], %[wa] offset:47104\n"
        "ds_read_b64_tr_b16 v[90:91], %[wb] offset:47104\n"
        "ds_read_b128 v[92:95], %[x7] offset:16384\n"
        "s_waitcnt lgkmcnt(9)\n"
        "v_mfma_f32_32x32x16_bf16 a[0:15], v[64:67], v[68:71], a[0:15]\n"
        "ds_read_b64_tr_b16 v[96:97], %[wa] offset:49152\n"
        "ds_read_b64_tr_b16 v[98:99], %[wb] offset:49152\n"
        "ds_read_b128 v[100:103], %[x0] offset:24576\n"
        "s_waitcnt lgkmcnt(9)\n"
        "v_mfma_f32_32x32x16_bf16 a[0:15], v[72:75], v[76:79], a[0:15]\n"
        "ds_read_b64_tr_b16 v[64:65], %[wa] offset:51200\n"
        "ds_read_b64_tr_b16 v[66:67], %[wb] offset:51200\n"
        "ds_read_b128 v[68:71], %[x1] offset:24576\n"
        "s_waitcnt lgkmcnt(9)\n"
        "v_mfma_f32_32x32x16_bf16 a[0:15], v[80:83], v[84:87], a[0:15]\n"
        "ds_read_b64_tr_b16 v[72:73], %[wa] offset:53248\n"
        "ds_read_b64_tr_b16 v[74:75], %[wb] offset:53248\n"
        "ds_read_b128 v[76:79], %[x2] offset:24576\n"
        "s_waitcnt lgkmcnt(9)\n"
        "v_mfma_f32_32x32x16_bf16 a[0:15], v[88:91], v[92:95], a[0:15]\n"
        "ds_read_b64_tr_b16 v[80:81], %[wa] offset:55296\n"
        "ds_read_b64_tr_b16 v[82:83], %[wb] offset:55296\n"
        "ds_read_b128 v[84:87], %[x3] offset:24576\n"
        "s_waitcnt lgkmcnt(9)\n"
        "v_mfma_f32_32x32x16_bf16 a[0:15], v[96:99], v[100:103], a[0:15]\n"
        "ds_read_b64_tr_b16 v[88:89], %[wa] offset:57344\n"
        "ds_read_b64_tr_b16 v[90:91], %[wb] offset:57344\n"
        "ds_read_b128 v[92:95], %[x4] offset:24576\n"
        "s_waitcnt lgkmcnt(9)\n"
        "v_mfma_f32_32x32x16_bf16 a[0:15], v[64:67], v[68:71], a[0:15]\n"
        "ds_read_b64_tr_b16 v[96:97], %[wa] offset:59392\n"
        "ds_read_b64_tr_b16 v[98:99], %[wb] offset:59392\n"
        "ds_read_b128 v[100:103], %[x5] offset:24576\n"
        "s_waitcnt lgkmcnt(9)\n"
        "v_mfma_f32_32x32x16_bf16 a[0:15], v[72:75], v[76:79], a[0:15]\n"
        "ds_read_b64_tr_b16 v[64:65], %[wa] offset:61440\n"
        "ds_read_b64_tr_b16 v[66:67], %[wb] offset:61440\n"
        "ds_read_b128 v[68:71], %[x6] offset:24576\n"
        "s_waitcnt lgkmcnt(9)\n"
        "v_mfma_f32_32x32x16_bf16 a[0:15], v[80:83], v[84:87], a[0:15]\n"
        "ds_read_b64_tr_b16 v[72:73], %[wa] offset:63488\n"
        "ds_read_b64_tr_b16 v[74:75], %[wb] offset:63488\n"
        "ds_read_b128 v[76:79], %[x7] offset:24576\n"
        "s_waitcnt lgkmcnt(9)\n"
        "v_mfma_f32_32x32x16_bf16 a[0:15], v[88:91], v[92:95], a[0:15]\n"
        "s_waitcnt lgkmcnt(6)\n"
        "v_mfma_f32_32x32x16_bf16 a[0:15], v[96:99], v[100:103], a[0:15]\n"
        "s_waitcnt lgkmcnt(3)\n"
        "v_mfma_f32_32x32x16_bf16 a[0:15], v[64:67], v[68:71], a[0:15]\n"
        "s_cmp_eq_u32 %[fin], 0\n"
        "s_cbranch_scc1 1f\n"
        "s_waitcnt vmcnt(0)\n"
        "v_readfirstlane_b32 s73, v61\n"
        "s_cmp_lg_u32 s73, 15\n"
        "s_cbranch_scc1 1f\n"
        "v_mov_b32 v63, 1\n"
        "s_mov_b64 s[74:75], exec\n"
        "s_mov_b64 exec, 0xff\n"
        "global_store_dword %[vgo], v63, %[go] sc1\n"
        "s_mov_b64 exec, s[74:75]\n"
        "1:\n"
        "s_waitcnt lgkmcnt(0)\n"
        "v_mfma_f32_32x32x16_bf16 a[0:15], v[72:75], v[76:79], a[0:15]\n"
        "s_nop 7\n"
        "s_nop 7\n"
                    MMQ_OPS, "a0", "a1", "a2", "a3", "a4", "a5", "a6", "a7", "a8", "a9", "a10", "a11", "a12", "a13", "a14", "a15");
                SBAR();
            }
            {
                const unsigned k2 = on * 2;
                if (l == 0) {
                    const unsigned rb = lds0 + ot * 98304 + (k2 >> 3) * 8192 + r * 256 + h * 8;
#pragma unroll
                    for (int i = 0; i < 4; i++) {
                        const u32x2 v2 = *(const LAS u32x2*)(uintptr_t)(rb + ((((k2 & 7) * 2 + i) ^ (r & 15)) * 16));
                        xr[2 * i] = v2[0]; xr[2 * i + 1] = v2[1];
                    }
                } else {
                    const LAS u32x4* rb = (const LAS u32x4*)(uintptr_t)(lds0 + ot * 98304 + k2 * 1024 + voff);
                    const u32x4 a = rb[0], b = rb[64];
#pragma unroll
                    for (int i = 0; i < 4; i++) { xr[i] = a[i]; xr[4 + i] = b[i]; }
                }
                asm volatile("" : "+v"(xr[0]), "+v"(xr[1]), "+v"(xr[2]), "+v"(xr[3]), "+v"(xr[4]), "+v"(xr[5]), "+v"(xr[6]), "+v"(xr[7]));
            }
            asm volatile("global_load_dwordx2 v[58:59], %1, %2 sc1" : "=&{v[58:59]}"(st) : "v"(r * 8), "s"(ws + (unsigned)(WS_ST + l * 16384 + tt * 256)) : "memory");
            if (!nojob) {
                SBAR();
                asm volatile(
        "s_waitcnt vmcnt(5) lgkmcnt(0)\n"
        "s_barrier\n"
        "ds_write_b128 %[wr], v[192:195] offset:0\n"
        "ds_write_b128 %[wr], v[196:199] offset:8704\n"
        "ds_write_b128 %[wr], v[200:203] offset:17408\n"
        "ds_write_b128 %[wr], v[204:207] offset:26112\n"
        "ds_write_b128 %[wr], v[208:211] offset:128\n"
        "ds_write_b128 %[wr], v[212:215] offset:8832\n"
        "ds_write_b128 %[wr], v[216:219] offset:17536\n"
        "ds_write_b128 %[wr], v[220:223] offset:26240\n"
        "v_readlane_b32 s72, %[kdo], 0\n"
        "v_readlane_b32 s73, %[kdo], 1\n"
        "v_readlane_b32 s74, %[kdo], 2\n"
        "v_readlane_b32 s75, %[kdo], 3\n"
        "v_readlane_b32 s76, %[kdo], 4\n"
        "v_readlane_b32 s77, %[kdo], 5\n"
        "v_readlane_b32 s78, %[kdo], 6\n"
        "v_readlane_b32 s79, %[kdo], 7\n"
        "s_waitcnt lgkmcnt(0)\n"
        "s_barrier\n"
        "ds_read_b64_tr_b16 v[192:193], %[rd0] offset:0\n"
        "ds_read_b64_tr_b16 v[194:195], %[rd0] offset:2176\n"
        "ds_read_b64_tr_b16 v[196:197], %[rd1] offset:0\n"
        "ds_read_b64_tr_b16 v[198:199], %[rd1] offset:2176\n"
        "ds_read_b64_tr_b16 v[200:201], %[rd0] offset:8704\n"
        "ds_read_b64_tr_b16 v[202:203], %[rd0] offset:10880\n"
        "ds_read_b64_tr_b16 v[204:205], %[rd1] offset:8704\n"
        "ds_read_b64_tr_b16 v[206:207], %[rd1] offset:10880\n"
        "ds_read_b64_tr_b16 v[208:209], %[rd0] offset:17408\n"
        "ds_read_b64_tr_b16 v[210:211], %[rd0] offset:19584\n"
        "ds_read_b64_tr_b16 v[212:213], %[rd1] offset:17408\n"
        "ds_read_b64_tr_b16 v[214:215], %[rd1] offset:19584\n"
        "ds_read_b64_tr_b16 v[216:217], %[rd0] offset:26112\n"
        "ds_read_b64_tr_b16 v[218:219], %[rd0] offset:28288\n"
        "s_waitcnt lgkmcnt(12)\n"
        "ds_read_b64_tr_b16 v[220:221], %[rd1] offset:26112\n"
        "ds_read_b64_tr_b16 v[222:223], %[rd1] offset:28288\n"
        "v_add_u32 v224, s72, %[voff]\n"
        "v_add_u32 v225, s73, %[voff]\n"
        "v_add_u32 v226, s74, %[voff]\n"
        "v_add_u32 v227, s75, %[voff]\n"
        "v_add_u32 v228, s76, %[voff]\n"
        "v_add_u32 v229, s77, %[voff]\n"
        "v_add_u32 v230, s78, %[voff]\n"
        "v_add_u32 v231, s79, %[voff]\n"
        "s_waitcnt lgkmcnt(0)\n"
                    : "+{v[192:223]}"(ja), "=&{v[224:231]}"(jo) : [wr] "v"(lds0 + (tid >> 3) * 272 + (tid & 7) * 16), [rd0] "v"(jrd), [rd1] "v"(jrd1), [kdo] "v"(kdo), [voff] "v"(voff)
                    : "memory", "s72", "s73", "s74", "s75", "s76", "s77", "s78", "s79", "s80", "s81", "s82", "s83", "s84", "s85");
                SBAR();
            } else {
                asm volatile("" : "={v[224:231]}"(jo));
            }
        }
        const char *o_st, *o_fl, *o_src, *o_own, *x1_st, *x1_ps, *x1_fl, *cs_src;
        unsigned o_lds, o_rd, o_ln, voff1, lane1, w13l, w13h, m13;
        unsigned wid1;
        {
        CTX();
        char* kv = ws + (unsigned)(WS_KV + l * KV_STRIDE);
        {
            unsigned o1;
            asm volatile("v_mov_b32 %0, 0x3f803f80" : "=v"(o1));
            o1 = (tid & 11) == 0 ? o1 : 0u;
#pragma unroll
            for (int i = 0; i < 3; i++) *(LAS u32x4*)(uintptr_t)(lds0 + L_ONES + tid * 16 + i * 4096) = u32x4{o1, o1, o1, o1};
        }
        f32x16 oa, ob;
        unsigned ka = lds0 + L_K + voff;
        unsigned va = r < 16 ? lds0 + L_V + (r + 16 * h) * 16 : lds0 + L_ONES + (r & 15) * 16;
        unsigned vinc = r < 16 ? 8192u : 0u, sidx = 0xDDDDu, vwr = (lane & 7) * 128, vkm = lane * 128, mkv = lds0 + wid * 2048;
        const uint64_t wou = (uint64_t)(ws + (unsigned)(WS_WO + (on * 32 + 4) * 1024));
        const uint64_t w2au = (uint64_t)(w2p + nsub * 114688 + 4096), w2bu = (uint64_t)(w2p + (nsub ^ 1) * 114688 + 4096);
        unsigned wol = (unsigned)wou, woh = (unsigned)(wou >> 32), w2al = (unsigned)w2au, w2ah = (unsigned)(w2au >> 32), w2bl = (unsigned)w2bu, w2bh = (unsigned)(w2bu >> 32);
        const char *skv = kv + wid * 2048, *skv0 = kv + tsub * 65536, *skm = kv + 131072 + 1024, *swr = ws + (unsigned)(FL_W), *swa = ws + (unsigned)(FL_W + xcd * 128);
        const uint64_t w13u = (uint64_t)(w13 + wid * 8192);
        w13l = (unsigned)w13u; w13h = (unsigned)(w13u >> 32); m13 = lds0 + wid * 8192;
        const char* sgo = ws + (unsigned)(FL_KV + l * 8192 + xcd * 1024);
        o_st = ws + (unsigned)(WS_O + (l * LS) * 2097152 + (tt * 32 + c8 * 4 + nsub * 2) * 1024);
        o_fl = ws + (unsigned)(FL_O + l * 4096 + T64 * 128);
        asm volatile("" : "+v"(ka), "+v"(va), "+v"(vinc), "+v"(sidx), "+v"(vwr), "+v"(vkm));
        asm volatile("" : "+s"(wol), "+s"(woh), "+s"(w2al), "+s"(w2ah), "+s"(w2bl), "+s"(w2bh), "+s"(skv), "+s"(skv0), "+s"(skm),
                          "+s"(swr), "+s"(swa), "+s"(sgo), "+s"(o_st), "+s"(o_fl), "+s"(mkv), "+s"(w13l), "+s"(w13h), "+s"(m13));
        u32x4 qa, qb;
        float qn2;
        {
            f32x16 qv, cv;
            unsigned s0_;
            asm volatile("s_mov_b32 %3, 0x100000\n" "v_accvgpr_read_b32 v64, a0\nv_accvgpr_read_b32 v65, a1\nv_accvgpr_read_b32 v66, a2\nv_accvgpr_read_b32 v67, a3\nv_accvgpr_read_b32 v68, a4\nv_accvgpr_read_b32 v69, a5\nv_accvgpr_read_b32 v70, a6\nv_accvgpr_read_b32 v71, a7\nv_accvgpr_read_b32 v72, a8\nv_accvgpr_read_b32 v73, a9\nv_accvgpr_read_b32 v74, a10\nv_accvgpr_read_b32 v75, a11\nv_accvgpr_read_b32 v76, a12\nv_accvgpr_read_b32 v77, a13\nv_accvgpr_read_b32 v78, a14\nv_accvgpr_read_b32 v79, a15\nv_accvgpr_read_b32 v80, a16\nv_accvgpr_read_b32 v81, a17\nv_accvgpr_read_b32 v82, a18\nv_accvgpr_read_b32 v83, a19\nv_accvgpr_read_b32 v84, a20\nv_accvgpr_read_b32 v85, a21\nv_accvgpr_read_b32 v86, a22\nv_accvgpr_read_b32 v87, a23\nv_accvgpr_read_b32 v88, a24\nv_accvgpr_read_b32 v89, a25\nv_accvgpr_read_b32 v90, a26\nv_accvgpr_read_b32 v91, a27\nv_accvgpr_read_b32 v92, a28\nv_accvgpr_read_b32 v93, a29\nv_accvgpr_read_b32 v94, a30\nv_accvgpr_read_b32 v95, a31" "\n1:\n\ts_waitcnt vmcnt(0)\n\tv_cmp_eq_u32 vcc, 0, v59\n\ts_nop 4\n\ts_cbranch_vccz 2f\n\t"
                         "global_load_dwordx2 v[58:59], %4, %5 sc1\n\ts_sub_u32 %3, %3, 1\n\ts_cbranch_scc0 1b\n\ts_waitcnt vmcnt(0)\n2:"
                         : "=&{v[64:79]}"(qv), "=&{v[80:95]}"(cv), "+{v[58:59]}"(st), "=&s"(s0_)
                         : "v"(r * 8), "s"(ws + (unsigned)(WS_ST + l * 16384 + tt * 256)) : "memory", "scc", "vcc");
            const float sa = st[1] * QSCALE, sb = -st[0] * sa;
            float q[16];
#pragma unroll
            for (int i = 0; i < 16; i++) q[i] = sa * qv[i] + sb * cv[i];
            qa = u32x4{pk2(q[0], q[1]), pk2(q[2], q[3]), pk2(q[4], q[5]), pk2(q[6], q[7])};
            qb = u32x4{pk2(q[8], q[9]), pk2(q[10], q[11]), pk2(q[12], q[13]), pk2(q[14], q[15])};
            float na = 0.f, nb = 0.f;
#pragma unroll
            for (int j = 0; j < 4; j++) { float x_ = bflo(qa[j]), y = bfhi(qa[j]), z = bflo(qb[j]), w = bfhi(qb[j]); na += x_ * x_ + y * y; nb += z * z + w * w; }
            qn2 = fmaxf(xsum(na), xsum(nb));
            asm volatile("" : "+v"(qa), "+v"(qb), "+v"(qn2));
        }
        wait_cnt(sgo, vwr, 1u, wid);
        f32x2 km;
        asm volatile(
        "global_load_dwordx2 %[km], %[vkm], %[skm] sc1\n"
        "s_mov_b32 m0, %[m0]\n"
        "s_mov_b64 s[74:75], %[skv]\n"
        "global_load_lds_dwordx4 %[voff], %[skv0] sc1\n"
        "s_add_u32 s76, s74, 0\n"
        "s_addc_u32 s77, s75, 0\n"
        "s_add_u32 m0, %[mkv], 0\n"
        "s_nop 0\n"
        "global_load_lds_dwordx4 %[voff], s[76:77] offset:1024 sc1\n"
        "global_load_lds_dwordx4 %[voff], s[76:77] offset:2048 sc1\n"
        "s_add_u32 s76, s74, 65536\n"
        "s_addc_u32 s77, s75, 0\n"
        "s_add_u32 m0, %[mkv], 65536\n"
        "s_nop 0\n"
        "global_load_lds_dwordx4 %[voff], s[76:77] offset:1024 sc1\n"
        "global_load_lds_dwordx4 %[voff], s[76:77] offset:2048 sc1\n"
        "s_add_u32 s76, s74, 8192\n"
        "s_addc_u32 s77, s75, 0\n"
        "s_add_u32 m0, %[mkv], 8192\n"
        "s_nop 0\n"
        "global_load_lds_dwordx4 %[voff], s[76:77] offset:1024 sc1\n"
        "global_load_lds_dwordx4 %[voff], s[76:77] offset:2048 sc1\n"
        "s_add_u32 s76, s74, 73728\n"
        "s_addc_u32 s77, s75, 0\n"
        "s_add_u32 m0, %[mkv], 73728\n"
        "s_nop 0\n"
        "global_load_lds_dwordx4 %[voff], s[76:77] offset:1024 sc1\n"
        "global_load_lds_dwordx4 %[voff], s[76:77] offset:2048 sc1\n"
            : [km] "=&v"(km) : [vkm] "v"(vkm), [skm] "s"(skm), [voff] "v"(voff), [skv0] "s"(skv0), [m0] "s"(lds0 + tsub * 65536), [skv] "s"(skv), [mkv] "s"(mkv)
            : "memory", "scc", "s72", "s73", "s74", "s75", "s76", "s77", "s78", "s79", "s80", "s81", "s82", "s83", "s84", "s85");
        asm volatile(
        "s_cmp_eq_u64 %[scs], 0\n"
        "s_cbranch_scc1 1f\n"
        "v_mov_b32 v60, 0x3f803f80\n"
        "v_mov_b32 v61, 0x3f803f80\n"
        "v_mov_b32 v62, 0x3f803f80\n"
        "v_mov_b32 v63, 0x3f803f80\n"
        "s_nop 1\n"
        "v_mfma_f32_16x16x32_bf16 v[64:67], v[192:195], v[60:63], 0\n"
        "v_mfma_f32_16x16x32_bf16 v[68:71], v[196:199], v[60:63], 0\n"
        "v_mfma_f32_16x16x32_bf16 v[64:67], v[200:203], v[60:63], v[64:67]\n"
        "v_mfma_f32_16x16x32_bf16 v[68:71], v[204:207], v[60:63], v[68:71]\n"
        "v_mfma_f32_16x16x32_bf16 v[64:67], v[208:211], v[60:63], v[64:67]\n"
        "v_mfma_f32_16x16x32_bf16 v[68:71], v[212:215], v[60:63], v[68:71]\n"
        "v_mfma_f32_16x16x32_bf16 v[64:67], v[216:219], v[60:63], v[64:67]\n"
        "v_mfma_f32_16x16x32_bf16 v[68:71], v[220:223], v[60:63], v[68:71]\n"
        "s_nop 7\n"
        "s_nop 1\n"
        "s_mov_b64 s[74:75], exec\n"
        "s_mov_b32 exec_lo, 0x10001\n"
        "s_mov_b32 exec_hi, 0x10001\n"
        "global_store_dwordx4 %[vcs], v[64:67], %[scs] offset:0 sc1\n"
        "global_store_dwordx4 %[vcs], v[68:71], %[scs] offset:64 sc1\n"
        "s_mov_b64 exec, s[74:75]\n"
        "1:\n"
            :: "{v[192:223]}"(ja), [vcs] "v"(lane & 48), [scs] "s"(jcs) : "memory", "scc", "s72", "s73", "s74", "s75", "s76", "s77", "s78", "s79", "s80", "s81", "s82", "s83", "s84", "s85", "v60", "v61", "v62", "v63", "v64", "v65", "v66", "v67", "v68", "v69", "v70", "v71", "v72", "v73", "v74", "v75", "v76", "v77", "v78", "v79");
        asm volatile("s_waitcnt vmcnt(11)\n\ts_cmp_lg_u64 %1, 0\n\ts_cbranch_scc1 1f\n\ts_waitcnt vmcnt(9)\n1:" : "+v"(km) : "s"(jcs) : "memory", "scc");
        const float kmax2 = max64(km[0]), vmax = max64(km[1]);
        const bool safe = (qn2 * kmax2 <= 3600.f) && (vmax <= 1.0e12f) && (vmax >= 1.0e-9f);
        const bool allsafe = __all(safe);
        asm volatile("s_waitcnt vmcnt(6) lgkmcnt(0)\n\ts_cmp_lg_u64 %0, 0\n\ts_cbranch_scc1 1f\n\ts_waitcnt vmcnt(4)\n1:\n\ts_barrier" :: "s"(jcs) : "memory", "scc");
        {
            u32x4 k0, k1, v00, v01, v10, v11;
#define ATT_OUT \
                : [oa] "=&{v[128:143]}"(oa), [ob] "=&{v[144:159]}"(ob), [k0] "=&v"(k0), [k1] "=&v"(k1), [v00] "=&v"(v00), [v01] "=&v"(v01), [v10] "=&v"(v10), [v11] "=&v"(v11), \
                  "={a[0:31]}"(woa), "={a[32:63]}"(wob), "={a[64:95]}"(woc), "={a[96:127]}"(wod), \
                  "={a[128:159]}"(w2a), "={a[160:191]}"(w2b), "={a[192:223]}"(w2c), "={a[224:255]}"(w2d)
#define ATT_IN \
                  [ka0] "v"(ka), [va0] "v"(va), [qa] "v"(qa), [qb] "v"(qb), [idx] "v"(sidx), [vinc] "v"(vinc), [voff] "v"(voff), [skv] "s"(skv), [mkv] "s"(mkv), \
                  [wol] "s"(wol), [woh] "s"(woh), [swr] "s"(swr), [vwr] "v"(vwr), [swa] "s"(swa), [jd] "s"(jdst), \
                  [wid] "s"(wid), "{v[192:223]}"(ja), "{v[224:231]}"(jo), [w2al] "s"(w2al), [w2ah] "s"(w2ah), [w2bl] "s"(w2bl), [w2bh] "s"(w2bh), \
                  [w13l] "s"(w13l), [w13h] "s"(w13h), [m13] "s"(m13), [lay1] "s"(l) \
                : "memory", "scc", "vcc", "s72", "s73", "s74", "s75", "s76", "s77", "s78", "s79", "s80", "s81", "s82", "s83", "s84", "s85", "s86", "s87", "s88", "s89", "s90", "s91", "s92", "s93", "s94", "s95", "s96", "s97", "s98", "s99", "v64", "v65", "v66", "v67", "v68", "v69", "v70", "v71", "v72", "v73", "v74", "v75", "v76", "v77", "v78", "v79", "v80", "v81", "v82", "v83", "v84", "v85", "v86", "v87", "v88", "v89", "v90", "v91", "v92", "v93", "v94", "v95", "v96", "v97", "v98", "v99", "v100"
            if (__builtin_expect(allsafe, 1)) {
                SBAR();
                asm volatile(
        "v_mov_b32 v99, %[ka0]\n"
        "v_mov_b32 v100, %[va0]\n"
        "ds_read_b128 %[k0], v99\n"
        "ds_read_b128 %[v00], v100\n"
        "ds_read_b128 %[v01], v100 offset:512\n"
        "ds_read_b128 %[v11], v100 offset:512\n"
        "v_mov_b64 v[80:81], 0\n"
        "v_mov_b64 v[82:83], 0\n"
        "v_mov_b64 v[84:85], 0\n"
        "v_mov_b64 v[86:87], 0\n"
        "v_mov_b64 v[88:89], 0\n"
        "v_mov_b64 v[90:91], 0\n"
        "v_mov_b64 v[92:93], 0\n"
        "v_mov_b64 v[94:95], 0\n"
        "v_mov_b64 v[128:129], 0\n"
        "v_mov_b64 v[130:131], 0\n"
        "v_mov_b64 v[132:133], 0\n"
        "v_mov_b64 v[134:135], 0\n"
        "v_mov_b64 v[136:137], 0\n"
        "v_mov_b64 v[138:139], 0\n"
        "v_mov_b64 v[140:141], 0\n"
        "v_mov_b64 v[142:143], 0\n"
        "v_mov_b64 v[144:145], 0\n"
        "v_mov_b64 v[146:147], 0\n"
        "v_mov_b64 v[148:149], 0\n"
        "v_mov_b64 v[150:151], 0\n"
        "v_mov_b64 v[152:153], 0\n"
        "v_mov_b64 v[154:155], 0\n"
        "v_mov_b64 v[156:157], 0\n"
        "v_mov_b64 v[158:159], 0\n"
        "s_mov_b32 s72, 8\n"
        "s_mov_b64 s[74:75], %[skv]\n"
        "s_add_u32 s73, %[mkv], 17408\n"
        "s_add_u32 s74, s74, 17408\n"
        "s_addc_u32 s75, s75, 0\n"
        "s_add_u32 s84, %[wol], 0\n"
        "s_addc_u32 s85, %[woh], 0\n"
        "s_add_u32 s86, %[wol], 8192\n"
        "s_addc_u32 s87, %[woh], 0\n"
        "s_add_u32 s88, %[wol], 16384\n"
        "s_addc_u32 s89, %[woh], 0\n"
        "s_add_u32 s90, %[wol], 24576\n"
        "s_addc_u32 s91, %[woh], 0\n"
        "s_add_u32 s92, %[w2al], 0\n"
        "s_addc_u32 s93, %[w2ah], 0\n"
        "s_add_u32 s94, %[w2al], 8192\n"
        "s_addc_u32 s95, %[w2ah], 0\n"
        "s_add_u32 s96, %[w2bl], 0\n"
        "s_addc_u32 s97, %[w2bh], 0\n"
        "s_add_u32 s98, %[w2bl], 8192\n"
        "s_addc_u32 s99, %[w2bh], 0\n"
        "s_waitcnt lgkmcnt(0)\n"
        "v_mfma_f32_32x32x16_bf16 v[64:79], %[k0], %[qa], 0\n"
        "s_nop 7\n"
        "s_nop 3\n"
        "v_mov_b32 v97, 0\n"
        "v_mov_b32 v98, 1\n"
        "s_or_b32 s80, %[wid], %[lay1]\n"
        "s_cselect_b32 s80, 99, 7\n"
        "s_sub_u32 s79, 4, %[lay1]\n"
        "s_cmp_eq_u64 %[jd], 0\n"
        "s_cbranch_scc1 1f\n"
        "v_exp_f32 v64, v64\n"
        "v_exp_f32 v65, v65\n"
        "v_exp_f32 v66, v66\n"
        "v_smfmac_f32_32x32x32_bf16 %[ob], %[v11], v[88:95], %[idx]\n"
        "ds_read_b128 %[k1], v99 offset:1024\n"
        "v_exp_f32 v67, v67\n"
        "v_exp_f32 v68, v68\n"
        "v_exp_f32 v69, v69\n"
        "v_exp_f32 v70, v70\n"
        "v_mfma_f32_32x32x16_bf16 v[80:95], %[k0], %[qb], 0\n"
        "ds_read_b128 %[v10], v100 offset:1024\n"
        "v_exp_f32 v71, v71\n"
        "v_exp_f32 v72, v72\n"
        "v_exp_f32 v73, v73\n"
        "v_exp_f32 v74, v74\n"
        "v_smfmac_f32_32x32x32_bf16 %[oa], %[v00], v[64:71], %[idx]\n"
        "ds_read_b128 %[v11], v100 offset:1536\n"
        "global_store_dwordx4 v224, v[192:195], %[jd] sc1\n"
        "v_exp_f32 v75, v75\n"
        "v_exp_f32 v76, v76\n"
        "v_exp_f32 v77, v77\n"
        "v_exp_f32 v78, v78\n"
        "v_exp_f32 v79, v79\n"
        "v_exp_f32 v80, v80\n"
        "v_exp_f32 v81, v81\n"
        "v_exp_f32 v82, v82\n"
        "v_smfmac_f32_32x32x32_bf16 %[oa], %[v01], v[72:79], %[idx]\n"
        "s_waitcnt lgkmcnt(2)\n"
        "s_add_u32 s76, s74, 65536\n"
        "v_exp_f32 v83, v83\n"
        "v_exp_f32 v84, v84\n"
        "v_exp_f32 v85, v85\n"
        "v_exp_f32 v86, v86\n"
        "v_mfma_f32_32x32x16_bf16 v[64:79], %[k1], %[qa], 0\n"
        "s_addc_u32 s77, s75, 0\n"
        "v_exp_f32 v87, v87\n"
        "v_exp_f32 v88, v88\n"
        "v_exp_f32 v89, v89\n"
        "v_exp_f32 v90, v90\n"
        "v_smfmac_f32_32x32x32_bf16 %[ob], %[v00], v[80:87], %[idx]\n"
        "s_waitcnt lgkmcnt(0)\n"
        "v_exp_f32 v91, v91\n"
        "v_exp_f32 v92, v92\n"
        "v_exp_f32 v93, v93\n"
        "v_exp_f32 v94, v94\n"
        "v_exp_f32 v95, v95\n"
        "v_exp_f32 v64, v64\n"
        "v_exp_f32 v65, v65\n"
        "v_exp_f32 v66, v66\n"
        "v_smfmac_f32_32x32x32_bf16 %[ob], %[v01], v[88:95], %[idx]\n"
        "ds_read_b128 %[k0], v99 offset:2048\n"
        "v_exp_f32 v67, v67\n"
        "v_exp_f32 v68, v68\n"
        "v_exp_f32 v69, v69\n"
        "v_exp_f32 v70, v70\n"
        "v_mfma_f32_32x32x16_bf16 v[80:95], %[k1], %[qb], 0\n"
        "ds_read_b128 %[v00], v100 offset:2048\n"
        "v_exp_f32 v71, v71\n"
        "v_exp_f32 v72, v72\n"
        "v_exp_f32 v73, v73\n"
        "v_exp_f32 v74, v74\n"
        "v_smfmac_f32_32x32x32_bf16 %[oa], %[v10], v[64:71], %[idx]\n"
        "ds_read_b128 %[v01], v100 offset:2560\n"
        "v_exp_f32 v75, v75\n"
        "v_exp_f32 v76, v76\n"
        "v_exp_f32 v77, v77\n"
        "v_exp_f32 v78, v78\n"
        "v_exp_f32 v79, v79\n"
        "v_exp_f32 v80, v80\n"
        "v_exp_f32 v81, v81\n"
        "v_exp_f32 v82, v82\n"
        "v_smfmac_f32_32x32x32_bf16 %[oa], %[v11], v[72:79], %[idx]\n"
        "s_waitcnt lgkmcnt(2)\n"
        "global_store_dwordx4 v225, v[196:199], %[jd] sc1\n"
        "v_exp_f32 v83, v83\n"
        "v_exp_f32 v84, v84\n"
        "v_exp_f32 v85, v85\n"
        "v_exp_f32 v86, v86\n"
        "v_mfma_f32_32x32x16_bf16 v[64:79], %[k0], %[qa], 0\n"
        "v_exp_f32 v87, v87\n"
        "v_exp_f32 v88, v88\n"
        "v_exp_f32 v89, v89\n"
        "v_exp_f32 v90, v90\n"
        "v_smfmac_f32_32x32x32_bf16 %[ob], %[v10], v[80:87], %[idx]\n"
        "s_waitcnt lgkmcnt(0)\n"
        "s_nop 0\n"
        "v_exp_f32 v91, v91\n"
        "v_exp_f32 v92, v92\n"
        "v_exp_f32 v93, v93\n"
        "v_exp_f32 v94, v94\n"
        "v_exp_f32 v95, v95\n"
        "v_exp_f32 v64, v64\n"
        "v_exp_f32 v65, v65\n"
        "v_exp_f32 v66, v66\n"
        "v_smfmac_f32_32x32x32_bf16 %[ob], %[v11], v[88:95], %[idx]\n"
        "ds_read_b128 %[k1], v99 offset:3072\n"
        "v_exp_f32 v67, v67\n"
        "v_exp_f32 v68, v68\n"
        "v_exp_f32 v69, v69\n"
        "v_exp_f32 v70, v70\n"
        "v_mfma_f32_32x32x16_bf16 v[80:95], %[k0], %[qb], 0\n"
        "ds_read_b128 %[v10], v100 offset:3072\n"
        "v_exp_f32 v71, v71\n"
        "v_exp_f32 v72, v72\n"
        "v_exp_f32 v73, v73\n"
        "v_exp_f32 v74, v74\n"
        "v_smfmac_f32_32x32x32_bf16 %[oa], %[v00], v[64:71], %[idx]\n"
        "ds_read_b128 %[v11], v100 offset:3584\n"
        "v_exp_f32 v75, v75\n"
        "v_exp_f32 v76, v76\n"
        "v_exp_f32 v77, v77\n"
        "v_exp_f32 v78, v78\n"
        "v_exp_f32 v79, v79\n"
        "v_exp_f32 v80, v80\n"
        "v_exp_f32 v81, v81\n"
        "v_exp_f32 v82, v82\n"
        "v_smfmac_f32_32x32x32_bf16 %[oa], %[v01], v[72:79], %[idx]\n"
        "s_waitcnt lgkmcnt(2)\n"
        "global_store_dwordx4 v226, v[200:203], %[jd] sc1\n"
        "v_exp_f32 v83, v83\n"
        "v_exp_f32 v84, v84\n"
        "v_exp_f32 v85, v85\n"
        "v_exp_f32 v86, v86\n"
        "v_mfma_f32_32x32x16_bf16 v[64:79], %[k1], %[qa], 0\n"
        "v_exp_f32 v87, v87\n"
        "v_exp_f32 v88, v88\n"
        "v_exp_f32 v89, v89\n"
        "v_exp_f32 v90, v90\n"
        "v_smfmac_f32_32x32x32_bf16 %[ob], %[v00], v[80:87], %[idx]\n"
        "s_waitcnt lgkmcnt(0)\n"
        "s_nop 0\n"
        "v_exp_f32 v91, v91\n"
        "v_exp_f32 v92, v92\n"
        "v_exp_f32 v93, v93\n"
        "v_exp_f32 v94, v94\n"
        "v_exp_f32 v95, v95\n"
        "v_exp_f32 v64, v64\n"
        "v_exp_f32 v65, v65\n"
        "v_exp_f32 v66, v66\n"
        "v_smfmac_f32_32x32x32_bf16 %[ob], %[v01], v[88:95], %[idx]\n"
        "ds_read_b128 %[k0], v99 offset:4096\n"
        "v_exp_f32 v67, v67\n"
        "v_exp_f32 v68, v68\n"
        "v_exp_f32 v69, v69\n"
        "v_exp_f32 v70, v70\n"
        "v_mfma_f32_32x32x16_bf16 v[80:95], %[k1], %[qb], 0\n"
        "ds_read_b128 %[v00], v100 offset:4096\n"
        "v_exp_f32 v71, v71\n"
        "v_exp_f32 v72, v72\n"
        "v_exp_f32 v73, v73\n"
        "v_exp_f32 v74, v74\n"
        "v_smfmac_f32_32x32x32_bf16 %[oa], %[v10], v[64:71], %[idx]\n"
        "ds_read_b128 %[v01], v100 offset:4608\n"
        "v_exp_f32 v75, v75\n"
        "v_exp_f32 v76, v76\n"
        "v_exp_f32 v77, v77\n"
        "v_exp_f32 v78, v78\n"
        "v_exp_f32 v79, v79\n"
        "v_exp_f32 v80, v80\n"
        "v_exp_f32 v81, v81\n"
        "v_exp_f32 v82, v82\n"
        "v_smfmac_f32_32x32x32_bf16 %[oa], %[v11], v[72:79], %[idx]\n"
        "s_waitcnt lgkmcnt(2)\n"
        "global_store_dwordx4 v227, v[204:207], %[jd] sc1\n"
        "v_exp_f32 v83, v83\n"
        "v_exp_f32 v84, v84\n"
        "v_exp_f32 v85, v85\n"
        "v_exp_f32 v86, v86\n"
        "v_mfma_f32_32x32x16_bf16 v[64:79], %[k0], %[qa], 0\n"
        "s_waitcnt vmcnt(4)\n"
        "v_exp_f32 v87, v87\n"
        "v_exp_f32 v88, v88\n"
        "v_exp_f32 v89, v89\n"
        "v_exp_f32 v90, v90\n"
        "v_smfmac_f32_32x32x32_bf16 %[ob], %[v10], v[80:87], %[idx]\n"
        "s_waitcnt lgkmcnt(0)\n"
        "s_barrier\n"
        "v_exp_f32 v91, v91\n"
        "v_exp_f32 v92, v92\n"
        "v_exp_f32 v93, v93\n"
        "v_exp_f32 v94, v94\n"
        "v_exp_f32 v95, v95\n"
        "v_exp_f32 v64, v64\n"
        "v_exp_f32 v65, v65\n"
        "v_exp_f32 v66, v66\n"
        "v_smfmac_f32_32x32x32_bf16 %[ob], %[v11], v[88:95], %[idx]\n"
        "ds_read_b128 %[k1], v99 offset:5120\n"
        "s_mov_b32 m0, s73\n"
        "v_exp_f32 v67, v67\n"
        "v_exp_f32 v68, v68\n"
        "v_exp_f32 v69, v69\n"
        "v_exp_f32 v70, v70\n"
        "v_mfma_f32_32x32x16_bf16 v[80:95], %[k0], %[qb], 0\n"
        "ds_read_b128 %[v10], v100 offset:5120\n"
        "v_exp_f32 v71, v71\n"
        "v_exp_f32 v72, v72\n"
        "v_exp_f32 v73, v73\n"
        "v_exp_f32 v74, v74\n"
        "v_smfmac_f32_32x32x32_bf16 %[oa], %[v00], v[64:71], %[idx]\n"
        "ds_read_b128 %[v11], v100 offset:5632\n"
        "global_load_lds_dwordx4 %[voff], s[74:75] sc1\n"
        "v_exp_f32 v75, v75\n"
        "v_exp_f32 v76, v76\n"
        "v_exp_f32 v77, v77\n"
        "v_exp_f32 v78, v78\n"
        "v_exp_f32 v79, v79\n"
        "v_exp_f32 v80, v80\n"
        "v_exp_f32 v81, v81\n"
        "v_exp_f32 v82, v82\n"
        "v_smfmac_f32_32x32x32_bf16 %[oa], %[v01], v[72:79], %[idx]\n"
        "s_waitcnt lgkmcnt(2)\n"
        "global_store_dwordx4 v228, v[208:211], %[jd] sc1\n"
        "v_exp_f32 v83, v83\n"
        "v_exp_f32 v84, v84\n"
        "v_exp_f32 v85, v85\n"
        "v_exp_f32 v86, v86\n"
        "v_mfma_f32_32x32x16_bf16 v[64:79], %[k1], %[qa], 0\n"
        "v_exp_f32 v87, v87\n"
        "v_exp_f32 v88, v88\n"
        "v_exp_f32 v89, v89\n"
        "v_exp_f32 v90, v90\n"
        "v_smfmac_f32_32x32x32_bf16 %[ob], %[v00], v[80:87], %[idx]\n"
        "s_waitcnt lgkmcnt(0)\n"
        "global_load_lds_dwordx4 %[voff], s[74:75] offset:1024 sc1\n"
        "v_exp_f32 v91, v91\n"
        "v_exp_f32 v92, v92\n"
        "v_exp_f32 v93, v93\n"
        "v_exp_f32 v94, v94\n"
        "v_exp_f32 v95, v95\n"
        "v_exp_f32 v64, v64\n"
        "v_exp_f32 v65, v65\n"
        "v_exp_f32 v66, v66\n"
        "v_smfmac_f32_32x32x32_bf16 %[ob], %[v01], v[88:95], %[idx]\n"
        "ds_read_b128 %[k0], v99 offset:6144\n"
        "s_add_u32 m0, s73, 65536\n"
        "v_exp_f32 v67, v67\n"
        "v_exp_f32 v68, v68\n"
        "v_exp_f32 v69, v69\n"
        "v_exp_f32 v70, v70\n"
        "v_mfma_f32_32x32x16_bf16 v[80:95], %[k1], %[qb], 0\n"
        "ds_read_b128 %[v00], v100 offset:6144\n"
        "v_exp_f32 v71, v71\n"
        "v_exp_f32 v72, v72\n"
        "v_exp_f32 v73, v73\n"
        "v_exp_f32 v74, v74\n"
        "v_smfmac_f32_32x32x32_bf16 %[oa], %[v10], v[64:71], %[idx]\n"
        "ds_read_b128 %[v01], v100 offset:6656\n"
        "global_load_lds_dwordx4 %[voff], s[76:77] sc1\n"
        "v_exp_f32 v75, v75\n"
        "v_exp_f32 v76, v76\n"
        "v_exp_f32 v77, v77\n"
        "v_exp_f32 v78, v78\n"
        "v_exp_f32 v79, v79\n"
        "v_exp_f32 v80, v80\n"
        "v_exp_f32 v81, v81\n"
        "v_exp_f32 v82, v82\n"
        "v_smfmac_f32_32x32x32_bf16 %[oa], %[v11], v[72:79], %[idx]\n"
        "s_waitcnt lgkmcnt(2)\n"
        "global_store_dwordx4 v229, v[212:215], %[jd] sc1\n"
        "v_exp_f32 v83, v83\n"
        "v_exp_f32 v84, v84\n"
        "v_exp_f32 v85, v85\n"
        "v_exp_f32 v86, v86\n"
        "v_mfma_f32_32x32x16_bf16 v[64:79], %[k0], %[qa], 0\n"
        "v_exp_f32 v87, v87\n"
        "v_exp_f32 v88, v88\n"
        "v_exp_f32 v89, v89\n"
        "v_exp_f32 v90, v90\n"
        "v_smfmac_f32_32x32x32_bf16 %[ob], %[v10], v[80:87], %[idx]\n"
        "s_waitcnt lgkmcnt(0)\n"
        "global_load_lds_dwordx4 %[voff], s[76:77] offset:1024 sc1\n"
        "v_exp_f32 v91, v91\n"
        "v_exp_f32 v92, v92\n"
        "v_exp_f32 v93, v93\n"
        "v_exp_f32 v94, v94\n"
        "v_exp_f32 v95, v95\n"
        "v_exp_f32 v64, v64\n"
        "v_exp_f32 v65, v65\n"
        "v_exp_f32 v66, v66\n"
        "v_smfmac_f32_32x32x32_bf16 %[ob], %[v11], v[88:95], %[idx]\n"
        "ds_read_b128 %[k1], v99 offset:7168\n"
        "s_add_u32 s73, s73, 8192\n"
        "v_exp_f32 v67, v67\n"
        "v_exp_f32 v68, v68\n"
        "v_exp_f32 v69, v69\n"
        "v_exp_f32 v70, v70\n"
        "v_mfma_f32_32x32x16_bf16 v[80:95], %[k0], %[qb], 0\n"
        "ds_read_b128 %[v10], v100 offset:7168\n"
        "v_exp_f32 v71, v71\n"
        "v_exp_f32 v72, v72\n"
        "v_exp_f32 v73, v73\n"
        "v_exp_f32 v74, v74\n"
        "v_smfmac_f32_32x32x32_bf16 %[oa], %[v00], v[64:71], %[idx]\n"
        "ds_read_b128 %[v11], v100 offset:7680\n"
        "s_add_u32 s74, s74, 8192\n"
        "v_exp_f32 v75, v75\n"
        "v_exp_f32 v76, v76\n"
        "v_exp_f32 v77, v77\n"
        "v_exp_f32 v78, v78\n"
        "v_exp_f32 v79, v79\n"
        "v_exp_f32 v80, v80\n"
        "v_exp_f32 v81, v81\n"
        "v_exp_f32 v82, v82\n"
        "v_smfmac_f32_32x32x32_bf16 %[oa], %[v01], v[72:79], %[idx]\n"
        "s_waitcnt lgkmcnt(2)\n"
        "s_addc_u32 s75, s75, 0\n"
        "v_exp_f32 v83, v83\n"
        "v_exp_f32 v84, v84\n"
        "v_exp_f32 v85, v85\n"
        "v_exp_f32 v86, v86\n"
        "v_mfma_f32_32x32x16_bf16 v[64:79], %[k1], %[qa], 0\n"
        "global_store_dwordx4 v230, v[216:219], %[jd] sc1\n"
        "v_exp_f32 v87, v87\n"
        "v_exp_f32 v88, v88\n"
        "v_exp_f32 v89, v89\n"
        "v_exp_f32 v90, v90\n"
        "v_smfmac_f32_32x32x32_bf16 %[ob], %[v00], v[80:87], %[idx]\n"
        "s_waitcnt lgkmcnt(0)\n"
        "v_exp_f32 v91, v91\n"
        "v_exp_f32 v92, v92\n"
        "v_exp_f32 v93, v93\n"
        "v_exp_f32 v94, v94\n"
        "v_exp_f32 v95, v95\n"
        "v_exp_f32 v64, v64\n"
        "v_exp_f32 v65, v65\n"
        "v_exp_f32 v66, v66\n"
        "v_smfmac_f32_32x32x32_bf16 %[ob], %[v01], v[88:95], %[idx]\n"
        "ds_read_b128 %[k0], v99 offset:8192\n"
        "global_store_dwordx4 v231, v[220:223], %[jd] sc1\n"
        "v_exp_f32 v67, v67\n"
        "v_exp_f32 v68, v68\n"
        "v_exp_f32 v69, v69\n"
        "v_exp_f32 v70, v70\n"
        "v_mfma_f32_32x32x16_bf16 v[80:95], %[k1], %[qb], 0\n"
        "ds_read_b128 %[v00], v100 offset:8192\n"
        "v_exp_f32 v71, v71\n"
        "v_exp_f32 v72, v72\n"
        "v_exp_f32 v73, v73\n"
        "v_exp_f32 v74, v74\n"
        "v_smfmac_f32_32x32x32_bf16 %[oa], %[v10], v[64:71], %[idx]\n"
        "ds_read_b128 %[v01], v100 offset:8704\n"
        "v_exp_f32 v75, v75\n"
        "v_exp_f32 v76, v76\n"
        "v_exp_f32 v77, v77\n"
        "v_exp_f32 v78, v78\n"
        "v_exp_f32 v79, v79\n"
        "v_exp_f32 v80, v80\n"
        "v_exp_f32 v81, v81\n"
        "v_exp_f32 v82, v82\n"
        "v_smfmac_f32_32x32x32_bf16 %[oa], %[v11], v[72:79], %[idx]\n"
        "s_waitcnt lgkmcnt(2)\n"
        "s_sub_u32 s72, s72, 1\n"
        "v_exp_f32 v83, v83\n"
        "v_exp_f32 v84, v84\n"
        "v_exp_f32 v85, v85\n"
        "v_exp_f32 v86, v86\n"
        "v_mfma_f32_32x32x16_bf16 v[64:79], %[k0], %[qa], 0\n"
        "s_cmp_lg_u32 s72, s79\n"
        "v_exp_f32 v87, v87\n"
        "v_exp_f32 v88, v88\n"
        "v_exp_f32 v89, v89\n"
        "v_exp_f32 v90, v90\n"
        "v_smfmac_f32_32x32x32_bf16 %[ob], %[v10], v[80:87], %[idx]\n"
        "s_waitcnt lgkmcnt(0)\n"
        "v_add_u32 v99, 8192, v99\n"
        "v_add_u32 v100, %[vinc], v100\n"
        "v_exp_f32 v91, v91\n"
        "v_exp_f32 v92, v92\n"
        "v_exp_f32 v93, v93\n"
        "v_exp_f32 v94, v94\n"
        "v_exp_f32 v95, v95\n"
        "1:\n"
        "v_exp_f32 v64, v64\n"
        "v_exp_f32 v65, v65\n"
        "v_exp_f32 v66, v66\n"
        "v_smfmac_f32_32x32x32_bf16 %[ob], %[v11], v[88:95], %[idx]\n"
        "ds_read_b128 %[k1], v99 offset:1024\n"
        "v_exp_f32 v67, v67\n"
        "v_exp_f32 v68, v68\n"
        "v_exp_f32 v69, v69\n"
        "v_exp_f32 v70, v70\n"
        "v_mfma_f32_32x32x16_bf16 v[80:95], %[k0], %[qb], 0\n"
        "ds_read_b128 %[v10], v100 offset:1024\n"
        "v_exp_f32 v71, v71\n"
        "v_exp_f32 v72, v72\n"
        "v_exp_f32 v73, v73\n"
        "v_exp_f32 v74, v74\n"
        "v_smfmac_f32_32x32x32_bf16 %[oa], %[v00], v[64:71], %[idx]\n"
        "ds_read_b128 %[v11], v100 offset:1536\n"
        "v_exp_f32 v75, v75\n"
        "v_exp_f32 v76, v76\n"
        "v_exp_f32 v77, v77\n"
        "v_exp_f32 v78, v78\n"
        "v_exp_f32 v79, v79\n"
        "v_exp_f32 v80, v80\n"
        "v_exp_f32 v81, v81\n"
        "v_exp_f32 v82, v82\n"
        "v_smfmac_f32_32x32x32_bf16 %[oa], %[v01], v[72:79], %[idx]\n"
        "s_waitcnt lgkmcnt(2)\n"
        "s_add_u32 s76, s74, 65536\n"
        "v_exp_f32 v83, v83\n"
        "v_exp_f32 v84, v84\n"
        "v_exp_f32 v85, v85\n"
        "v_exp_f32 v86, v86\n"
        "v_mfma_f32_32x32x16_bf16 v[64:79], %[k1], %[qa], 0\n"
        "s_addc_u32 s77, s75, 0\n"
        "v_exp_f32 v87, v87\n"
        "v_exp_f32 v88, v88\n"
        "v_exp_f32 v89, v89\n"
        "v_exp_f32 v90, v90\n"
        "v_smfmac_f32_32x32x32_bf16 %[ob], %[v00], v[80:87], %[idx]\n"
        "s_waitcnt lgkmcnt(0)\n"
        "v_exp_f32 v91, v91\n"
        "v_exp_f32 v92, v92\n"
        "v_exp_f32 v93, v93\n"
        "v_exp_f32 v94, v94\n"
        "v_exp_f32 v95, v95\n"
        "v_exp_f32 v64, v64\n"
        "v_exp_f32 v65, v65\n"
        "v_exp_f32 v66, v66\n"
        "v_smfmac_f32_32x32x32_bf16 %[ob], %[v01], v[88:95], %[idx]\n"
        "ds_read_b128 %[k0], v99 offset:2048\n"
        "s_cmp_eq_u32 s72, 5\n"
        "v_exp_f32 v67, v67\n"
        "v_exp_f32 v68, v68\n"
        "v_exp_f32 v69, v69\n"
        "v_exp_f32 v70, v70\n"
        "v_mfma_f32_32x32x16_bf16 v[80:95], %[k1], %[qb], 0\n"
        "ds_read_b128 %[v00], v100 offset:2048\n"
        "v_exp_f32 v71, v71\n"
        "v_exp_f32 v72, v72\n"
        "v_exp_f32 v73, v73\n"
        "v_exp_f32 v74, v74\n"
        "v_smfmac_f32_32x32x32_bf16 %[oa], %[v10], v[64:71], %[idx]\n"
        "ds_read_b128 %[v01], v100 offset:2560\n"
        "s_cselect_b64 exec, -1, 0\n"
        "global_load_dword v96, %[vwr], %[swr] sc1\n"
        "s_mov_b64 exec, -1\n"
        "v_exp_f32 v75, v75\n"
        "v_exp_f32 v76, v76\n"
        "v_exp_f32 v77, v77\n"
        "v_exp_f32 v78, v78\n"
        "v_exp_f32 v79, v79\n"
        "v_exp_f32 v80, v80\n"
        "v_exp_f32 v81, v81\n"
        "v_exp_f32 v82, v82\n"
        "v_smfmac_f32_32x32x32_bf16 %[oa], %[v11], v[72:79], %[idx]\n"
        "s_waitcnt lgkmcnt(2)\n"
        "v_exp_f32 v83, v83\n"
        "v_exp_f32 v84, v84\n"
        "v_exp_f32 v85, v85\n"
        "v_exp_f32 v86, v86\n"
        "v_mfma_f32_32x32x16_bf16 v[64:79], %[k0], %[qa], 0\n"
        "v_exp_f32 v87, v87\n"
        "v_exp_f32 v88, v88\n"
        "v_exp_f32 v89, v89\n"
        "v_exp_f32 v90, v90\n"
        "v_smfmac_f32_32x32x32_bf16 %[ob], %[v10], v[80:87], %[idx]\n"
        "s_waitcnt lgkmcnt(0)\n"
        "s_nop 0\n"
        "v_exp_f32 v91, v91\n"
        "v_exp_f32 v92, v92\n"
        "v_exp_f32 v93, v93\n"
        "v_exp_f32 v94, v94\n"
        "v_exp_f32 v95, v95\n"
        "v_exp_f32 v64, v64\n"
        "v_exp_f32 v65, v65\n"
        "v_exp_f32 v66, v66\n"
        "v_smfmac_f32_32x32x32_bf16 %[ob], %[v11], v[88:95], %[idx]\n"
        "ds_read_b128 %[k1], v99 offset:3072\n"
        "v_exp_f32 v67, v67\n"
        "v_exp_f32 v68, v68\n"
        "v_exp_f32 v69, v69\n"
        "v_exp_f32 v70, v70\n"
        "v_mfma_f32_32x32x16_bf16 v[80:95], %[k0], %[qb], 0\n"
        "ds_read_b128 %[v10], v100 offset:3072\n"
        "v_exp_f32 v71, v71\n"
        "v_exp_f32 v72, v72\n"
        "v_exp_f32 v73, v73\n"
        "v_exp_f32 v74, v74\n"
        "v_smfmac_f32_32x32x32_bf16 %[oa], %[v00], v[64:71], %[idx]\n"
        "ds_read_b128 %[v11], v100 offset:3584\n"
        "v_exp_f32 v75, v75\n"
        "v_exp_f32 v76, v76\n"
        "v_exp_f32 v77, v77\n"
        "v_exp_f32 v78, v78\n"
        "v_exp_f32 v79, v79\n"
        "v_exp_f32 v80, v80\n"
        "v_exp_f32 v81, v81\n"
        "v_exp_f32 v82, v82\n"
        "v_smfmac_f32_32x32x32_bf16 %[oa], %[v01], v[72:79], %[idx]\n"
        "s_waitcnt lgkmcnt(2)\n"
        "v_exp_f32 v83, v83\n"
        "v_exp_f32 v84, v84\n"
        "v_exp_f32 v85, v85\n"
        "v_exp_f32 v86, v86\n"
        "v_mfma_f32_32x32x16_bf16 v[64:79], %[k1], %[qa], 0\n"
        "v_exp_f32 v87, v87\n"
        "v_exp_f32 v88, v88\n"
        "v_exp_f32 v89, v89\n"
        "v_exp_f32 v90, v90\n"
        "v_smfmac_f32_32x32x32_bf16 %[ob], %[v00], v[80:87], %[idx]\n"
        "s_waitcnt lgkmcnt(0)\n"
        "s_nop 0\n"
        "v_exp_f32 v91, v91\n"
        "v_exp_f32 v92, v92\n"
        "v_exp_f32 v93, v93\n"
        "v_exp_f32 v94, v94\n"
        "v_exp_f32 v95, v95\n"
        "v_exp_f32 v64, v64\n"
        "v_exp_f32 v65, v65\n"
        "v_exp_f32 v66, v66\n"
        "v_smfmac_f32_32x32x32_bf16 %[ob], %[v01], v[88:95], %[idx]\n"
        "ds_read_b128 %[k0], v99 offset:4096\n"
        "v_exp_f32 v67, v67\n"
        "v_exp_f32 v68, v68\n"
        "v_exp_f32 v69, v69\n"
        "v_exp_f32 v70, v70\n"
        "v_mfma_f32_32x32x16_bf16 v[80:95], %[k1], %[qb], 0\n"
        "ds_read_b128 %[v00], v100 offset:4096\n"
        "v_exp_f32 v71, v71\n"
        "v_exp_f32 v72, v72\n"
        "v_exp_f32 v73, v73\n"
        "v_exp_f32 v74, v74\n"
        "v_smfmac_f32_32x32x32_bf16 %[oa], %[v10], v[64:71], %[idx]\n"
        "ds_read_b128 %[v01], v100 offset:4608\n"
        "v_exp_f32 v75, v75\n"
        "v_exp_f32 v76, v76\n"
        "v_exp_f32 v77, v77\n"
        "v_exp_f32 v78, v78\n"
        "v_exp_f32 v79, v79\n"
        "v_exp_f32 v80, v80\n"
        "v_exp_f32 v81, v81\n"
        "v_exp_f32 v82, v82\n"
        "v_smfmac_f32_32x32x32_bf16 %[oa], %[v11], v[72:79], %[idx]\n"
        "s_waitcnt lgkmcnt(2)\n"
        "v_exp_f32 v83, v83\n"
        "v_exp_f32 v84, v84\n"
        "v_exp_f32 v85, v85\n"
        "v_exp_f32 v86, v86\n"
        "v_mfma_f32_32x32x16_bf16 v[64:79], %[k0], %[qa], 0\n"
        "s_waitcnt vmcnt(0)\n"
        "v_exp_f32 v87, v87\n"
        "v_exp_f32 v88, v88\n"
        "v_exp_f32 v89, v89\n"
        "v_exp_f32 v90, v90\n"
        "v_smfmac_f32_32x32x32_bf16 %[ob], %[v10], v[80:87], %[idx]\n"
        "s_waitcnt lgkmcnt(0)\n"
        "s_barrier\n"
        "v_exp_f32 v91, v91\n"
        "v_exp_f32 v92, v92\n"
        "v_exp_f32 v93, v93\n"
        "v_exp_f32 v94, v94\n"
        "v_exp_f32 v95, v95\n"
        "v_exp_f32 v64, v64\n"
        "v_exp_f32 v65, v65\n"
        "v_exp_f32 v66, v66\n"
        "v_smfmac_f32_32x32x32_bf16 %[ob], %[v11], v[88:95], %[idx]\n"
        "ds_read_b128 %[k1], v99 offset:5120\n"
        "s_mov_b32 m0, s73\n"
        "v_exp_f32 v67, v67\n"
        "v_exp_f32 v68, v68\n"
        "v_exp_f32 v69, v69\n"
        "v_exp_f32 v70, v70\n"
        "v_mfma_f32_32x32x16_bf16 v[80:95], %[k0], %[qb], 0\n"
        "ds_read_b128 %[v10], v100 offset:5120\n"
        "v_exp_f32 v71, v71\n"
        "v_exp_f32 v72, v72\n"
        "v_exp_f32 v73, v73\n"
        "v_exp_f32 v74, v74\n"
        "v_smfmac_f32_32x32x32_bf16 %[oa], %[v00], v[64:71], %[idx]\n"
        "ds_read_b128 %[v11], v100 offset:5632\n"
        "global_load_lds_dwordx4 %[voff], s[74:75] sc1\n"
        "v_exp_f32 v75, v75\n"
        "v_exp_f32 v76, v76\n"
        "v_exp_f32 v77, v77\n"
        "v_exp_f32 v78, v78\n"
        "v_exp_f32 v79, v79\n"
        "v_exp_f32 v80, v80\n"
        "v_exp_f32 v81, v81\n"
        "v_exp_f32 v82, v82\n"
        "v_smfmac_f32_32x32x32_bf16 %[oa], %[v01], v[72:79], %[idx]\n"
        "s_waitcnt lgkmcnt(2)\n"
        "v_exp_f32 v83, v83\n"
        "v_exp_f32 v84, v84\n"
        "v_exp_f32 v85, v85\n"
        "v_exp_f32 v86, v86\n"
        "v_mfma_f32_32x32x16_bf16 v[64:79], %[k1], %[qa], 0\n"
        "v_exp_f32 v87, v87\n"
        "v_exp_f32 v88, v88\n"
        "v_exp_f32 v89, v89\n"
        "v_exp_f32 v90, v90\n"
        "v_smfmac_f32_32x32x32_bf16 %[ob], %[v00], v[80:87], %[idx]\n"
        "s_waitcnt lgkmcnt(0)\n"
        "global_load_lds_dwordx4 %[voff], s[74:75] offset:1024 sc1\n"
        "v_exp_f32 v91, v91\n"
        "v_exp_f32 v92, v92\n"
        "v_exp_f32 v93, v93\n"
        "v_exp_f32 v94, v94\n"
        "v_exp_f32 v95, v95\n"
        "v_exp_f32 v64, v64\n"
        "v_exp_f32 v65, v65\n"
        "v_exp_f32 v66, v66\n"
        "v_smfmac_f32_32x32x32_bf16 %[ob], %[v01], v[88:95], %[idx]\n"
        "ds_read_b128 %[k0], v99 offset:6144\n"
        "s_add_u32 m0, s73, 65536\n"
        "v_exp_f32 v67, v67\n"
        "v_exp_f32 v68, v68\n"
        "v_exp_f32 v69, v69\n"
        "v_exp_f32 v70, v70\n"
        "v_mfma_f32_32x32x16_bf16 v[80:95], %[k1], %[qb], 0\n"
        "ds_read_b128 %[v00], v100 offset:6144\n"
        "v_exp_f32 v71, v71\n"
        "v_exp_f32 v72, v72\n"
        "v_exp_f32 v73, v73\n"
        "v_exp_f32 v74, v74\n"
        "v_smfmac_f32_32x32x32_bf16 %[oa], %[v10], v[64:71], %[idx]\n"
        "ds_read_b128 %[v01], v100 offset:6656\n"
        "global_load_lds_dwordx4 %[voff], s[76:77] sc1\n"
        "v_exp_f32 v75, v75\n"
        "v_exp_f32 v76, v76\n"
        "v_exp_f32 v77, v77\n"
        "v_exp_f32 v78, v78\n"
        "v_exp_f32 v79, v79\n"
        "v_exp_f32 v80, v80\n"
        "v_exp_f32 v81, v81\n"
        "v_exp_f32 v82, v82\n"
        "v_smfmac_f32_32x32x32_bf16 %[oa], %[v11], v[72:79], %[idx]\n"
        "s_waitcnt lgkmcnt(2)\n"
        "v_exp_f32 v83, v83\n"
        "v_exp_f32 v84, v84\n"
        "v_exp_f32 v85, v85\n"
        "v_exp_f32 v86, v86\n"
        "v_mfma_f32_32x32x16_bf16 v[64:79], %[k0], %[qa], 0\n"
        "v_exp_f32 v87, v87\n"
        "v_exp_f32 v88, v88\n"
        "v_exp_f32 v89, v89\n"
        "v_exp_f32 v90, v90\n"
        "v_smfmac_f32_32x32x32_bf16 %[ob], %[v10], v[80:87], %[idx]\n"
        "s_waitcnt lgkmcnt(0)\n"
        "global_load_lds_dwordx4 %[voff], s[76:77] offset:1024 sc1\n"
        "v_exp_f32 v91, v91\n"
        "v_exp_f32 v92, v92\n"
        "v_exp_f32 v93, v93\n"
        "v_exp_f32 v94, v94\n"
        "v_exp_f32 v95, v95\n"
        "v_exp_f32 v64, v64\n"
        "v_exp_f32 v65, v65\n"
        "v_exp_f32 v66, v66\n"
        "v_smfmac_f32_32x32x32_bf16 %[ob], %[v11], v[88:95], %[idx]\n"
        "ds_read_b128 %[k1], v99 offset:7168\n"
        "s_add_u32 s73, s73, 8192\n"
        "v_exp_f32 v67, v67\n"
        "v_exp_f32 v68, v68\n"
        "v_exp_f32 v69, v69\n"
        "v_exp_f32 v70, v70\n"
        "v_mfma_f32_32x32x16_bf16 v[80:95], %[k0], %[qb], 0\n"
        "ds_read_b128 %[v10], v100 offset:7168\n"
        "v_exp_f32 v71, v71\n"
        "v_exp_f32 v72, v72\n"
        "v_exp_f32 v73, v73\n"
        "v_exp_f32 v74, v74\n"
        "v_smfmac_f32_32x32x32_bf16 %[oa], %[v00], v[64:71], %[idx]\n"
        "ds_read_b128 %[v11], v100 offset:7680\n"
        "s_add_u32 s74, s74, 8192\n"
        "v_exp_f32 v75, v75\n"
        "v_exp_f32 v76, v76\n"
        "v_exp_f32 v77, v77\n"
        "v_exp_f32 v78, v78\n"
        "v_exp_f32 v79, v79\n"
        "v_exp_f32 v80, v80\n"
        "v_exp_f32 v81, v81\n"
        "v_exp_f32 v82, v82\n"
        "v_smfmac_f32_32x32x32_bf16 %[oa], %[v01], v[72:79], %[idx]\n"
        "s_waitcnt lgkmcnt(2)\n"
        "s_addc_u32 s75, s75, 0\n"
        "v_exp_f32 v83, v83\n"
        "v_exp_f32 v84, v84\n"
        "v_exp_f32 v85, v85\n"
        "v_exp_f32 v86, v86\n"
        "v_mfma_f32_32x32x16_bf16 v[64:79], %[k1], %[qa], 0\n"
        "s_cmp_eq_u32 s72, s80\n"
        "v_exp_f32 v87, v87\n"
        "v_exp_f32 v88, v88\n"
        "v_exp_f32 v89, v89\n"
        "v_exp_f32 v90, v90\n"
        "v_smfmac_f32_32x32x32_bf16 %[ob], %[v00], v[80:87], %[idx]\n"
        "s_waitcnt lgkmcnt(0)\n"
        "s_cselect_b64 exec, 1, 0\n"
        "global_atomic_add v97, v98, %[swa] sc1\n"
        "s_mov_b64 exec, -1\n"
        "v_exp_f32 v91, v91\n"
        "v_exp_f32 v92, v92\n"
        "v_exp_f32 v93, v93\n"
        "v_exp_f32 v94, v94\n"
        "v_exp_f32 v95, v95\n"
        "v_exp_f32 v64, v64\n"
        "v_exp_f32 v65, v65\n"
        "v_exp_f32 v66, v66\n"
        "v_smfmac_f32_32x32x32_bf16 %[ob], %[v01], v[88:95], %[idx]\n"
        "ds_read_b128 %[k0], v99 offset:8192\n"
        "v_exp_f32 v67, v67\n"
        "v_exp_f32 v68, v68\n"
        "v_exp_f32 v69, v69\n"
        "v_exp_f32 v70, v70\n"
        "v_mfma_f32_32x32x16_bf16 v[80:95], %[k1], %[qb], 0\n"
        "ds_read_b128 %[v00], v100 offset:8192\n"
        "v_exp_f32 v71, v71\n"
        "v_exp_f32 v72, v72\n"
        "v_exp_f32 v73, v73\n"
        "v_exp_f32 v74, v74\n"
        "v_smfmac_f32_32x32x32_bf16 %[oa], %[v10], v[64:71], %[idx]\n"
        "ds_read_b128 %[v01], v100 offset:8704\n"
        "v_exp_f32 v75, v75\n"
        "v_exp_f32 v76, v76\n"
        "v_exp_f32 v77, v77\n"
        "v_exp_f32 v78, v78\n"
        "v_exp_f32 v79, v79\n"
        "v_exp_f32 v80, v80\n"
        "v_exp_f32 v81, v81\n"
        "v_exp_f32 v82, v82\n"
        "v_smfmac_f32_32x32x32_bf16 %[oa], %[v11], v[72:79], %[idx]\n"
        "s_waitcnt lgkmcnt(2)\n"
        "s_sub_u32 s72, s72, 1\n"
        "v_exp_f32 v83, v83\n"
        "v_exp_f32 v84, v84\n"
        "v_exp_f32 v85, v85\n"
        "v_exp_f32 v86, v86\n"
        "v_mfma_f32_32x32x16_bf16 v[64:79], %[k0], %[qa], 0\n"
        "s_cmp_lg_u32 s72, s79\n"
        "v_exp_f32 v87, v87\n"
        "v_exp_f32 v88, v88\n"
        "v_exp_f32 v89, v89\n"
        "v_exp_f32 v90, v90\n"
        "v_smfmac_f32_32x32x32_bf16 %[ob], %[v10], v[80:87], %[idx]\n"
        "s_waitcnt lgkmcnt(0)\n"
        "v_add_u32 v99, 8192, v99\n"
        "v_add_u32 v100, %[vinc], v100\n"
        "v_exp_f32 v91, v91\n"
        "v_exp_f32 v92, v92\n"
        "v_exp_f32 v93, v93\n"
        "v_exp_f32 v94, v94\n"
        "v_exp_f32 v95, v95\n"
        "s_cbranch_scc1 1b\n"
        "s_mov_b32 s81, 0x100000\n"
        "2:\n"
        "v_cmp_gt_u32 vcc, 32, v96\n"
        "s_nop 4\n"
        "s_cbranch_vccz 3f\n"
        "global_load_dword v96, %[vwr], %[swr] sc1\n"
        "s_waitcnt vmcnt(0)\n"
        "s_sub_u32 s81, s81, 1\n"
        "s_cbranch_scc0 2b\n"
        "3:\n"
        "s_cmp_lg_u32 %[lay1], 0\n"
        "s_cbranch_scc1 8f\n"
        "v_exp_f32 v64, v64\n"
        "v_exp_f32 v65, v65\n"
        "v_exp_f32 v66, v66\n"
        "v_smfmac_f32_32x32x32_bf16 %[ob], %[v11], v[88:95], %[idx]\n"
        "ds_read_b128 %[k1], v99 offset:1024\n"
        "global_load_dwordx4 a[128:131], %[voff], s[92:93] offset:-4096 sc1\n"
        "v_exp_f32 v67, v67\n"
        "v_exp_f32 v68, v68\n"
        "v_exp_f32 v69, v69\n"
        "v_exp_f32 v70, v70\n"
        "v_mfma_f32_32x32x16_bf16 v[80:95], %[k0], %[qb], 0\n"
        "ds_read_b128 %[v10], v100 offset:1024\n"
        "global_load_dwordx4 a[152:155], %[voff], s[92:93] offset:-1024 sc1\n"
        "v_exp_f32 v71, v71\n"
        "v_exp_f32 v72, v72\n"
        "v_exp_f32 v73, v73\n"
        "v_exp_f32 v74, v74\n"
        "v_smfmac_f32_32x32x32_bf16 %[oa], %[v00], v[64:71], %[idx]\n"
        "ds_read_b128 %[v11], v100 offset:1536\n"
        "global_load_dwordx4 a[136:139], %[voff], s[92:93] offset:-3072 sc1\n"
        "v_exp_f32 v75, v75\n"
        "v_exp_f32 v76, v76\n"
        "v_exp_f32 v77, v77\n"
        "v_exp_f32 v78, v78\n"
        "v_exp_f32 v79, v79\n"
        "v_exp_f32 v80, v80\n"
        "v_exp_f32 v81, v81\n"
        "v_exp_f32 v82, v82\n"
        "v_smfmac_f32_32x32x32_bf16 %[oa], %[v01], v[72:79], %[idx]\n"
        "s_waitcnt lgkmcnt(2)\n"
        "s_add_u32 s76, s74, 65536\n"
        "v_exp_f32 v83, v83\n"
        "v_exp_f32 v84, v84\n"
        "v_exp_f32 v85, v85\n"
        "v_exp_f32 v86, v86\n"
        "v_mfma_f32_32x32x16_bf16 v[64:79], %[k1], %[qa], 0\n"
        "s_addc_u32 s77, s75, 0\n"
        "v_exp_f32 v87, v87\n"
        "v_exp_f32 v88, v88\n"
        "v_exp_f32 v89, v89\n"
        "v_exp_f32 v90, v90\n"
        "v_smfmac_f32_32x32x32_bf16 %[ob], %[v00], v[80:87], %[idx]\n"
        "s_waitcnt lgkmcnt(0)\n"
        "global_load_dwordx4 a[144:147], %[voff], s[92:93] offset:-2048 sc1\n"
        "v_exp_f32 v91, v91\n"
        "v_exp_f32 v92, v92\n"
        "v_exp_f32 v93, v93\n"
        "v_exp_f32 v94, v94\n"
        "v_exp_f32 v95, v95\n"
        "v_exp_f32 v64, v64\n"
        "v_exp_f32 v65, v65\n"
        "v_exp_f32 v66, v66\n"
        "v_smfmac_f32_32x32x32_bf16 %[ob], %[v01], v[88:95], %[idx]\n"
        "ds_read_b128 %[k0], v99 offset:2048\n"
        "global_load_dwordx4 a[160:163], %[voff], s[92:93] offset:0 sc1\n"
        "v_exp_f32 v67, v67\n"
        "v_exp_f32 v68, v68\n"
        "v_exp_f32 v69, v69\n"
        "v_exp_f32 v70, v70\n"
        "v_mfma_f32_32x32x16_bf16 v[80:95], %[k1], %[qb], 0\n"
        "ds_read_b128 %[v00], v100 offset:2048\n"
        "global_load_dwordx4 a[140:143], %[voff], s[96:97] offset:-3072 sc1\n"
        "v_exp_f32 v71, v71\n"
        "v_exp_f32 v72, v72\n"
        "v_exp_f32 v73, v73\n"
        "v_exp_f32 v74, v74\n"
        "v_smfmac_f32_32x32x32_bf16 %[oa], %[v10], v[64:71], %[idx]\n"
        "ds_read_b128 %[v01], v100 offset:2560\n"
        "global_load_dwordx4 a[168:171], %[voff], s[92:93] offset:1024 sc1\n"
        "v_exp_f32 v75, v75\n"
        "v_exp_f32 v76, v76\n"
        "v_exp_f32 v77, v77\n"
        "v_exp_f32 v78, v78\n"
        "v_exp_f32 v79, v79\n"
        "v_exp_f32 v80, v80\n"
        "v_exp_f32 v81, v81\n"
        "v_exp_f32 v82, v82\n"
        "v_smfmac_f32_32x32x32_bf16 %[oa], %[v11], v[72:79], %[idx]\n"
        "s_waitcnt lgkmcnt(2)\n"
        "global_load_dwordx4 a[176:179], %[voff], s[92:93] offset:2048 sc1\n"
        "v_exp_f32 v83, v83\n"
        "v_exp_f32 v84, v84\n"
        "v_exp_f32 v85, v85\n"
        "v_exp_f32 v86, v86\n"
        "v_mfma_f32_32x32x16_bf16 v[64:79], %[k0], %[qa], 0\n"
        "global_load_dwordx4 a[184:187], %[voff], s[92:93] offset:3072 sc1\n"
        "v_exp_f32 v87, v87\n"
        "v_exp_f32 v88, v88\n"
        "v_exp_f32 v89, v89\n"
        "v_exp_f32 v90, v90\n"
        "v_smfmac_f32_32x32x32_bf16 %[ob], %[v10], v[80:87], %[idx]\n"
        "s_waitcnt lgkmcnt(0)\n"
        "global_load_dwordx4 a[132:135], %[voff], s[96:97] offset:-4096 sc1\n"
        "v_exp_f32 v91, v91\n"
        "v_exp_f32 v92, v92\n"
        "v_exp_f32 v93, v93\n"
        "v_exp_f32 v94, v94\n"
        "v_exp_f32 v95, v95\n"
        "v_exp_f32 v64, v64\n"
        "v_exp_f32 v65, v65\n"
        "v_exp_f32 v66, v66\n"
        "v_smfmac_f32_32x32x32_bf16 %[ob], %[v11], v[88:95], %[idx]\n"
        "ds_read_b128 %[k1], v99 offset:3072\n"
        "global_load_dwordx4 a[148:151], %[voff], s[96:97] offset:-2048 sc1\n"
        "v_exp_f32 v67, v67\n"
        "v_exp_f32 v68, v68\n"
        "v_exp_f32 v69, v69\n"
        "v_exp_f32 v70, v70\n"
        "v_mfma_f32_32x32x16_bf16 v[80:95], %[k0], %[qb], 0\n"
        "ds_read_b128 %[v10], v100 offset:3072\n"
        "global_load_dwordx4 a[188:191], %[voff], s[96:97] offset:3072 sc1\n"
        "v_exp_f32 v71, v71\n"
        "v_exp_f32 v72, v72\n"
        "v_exp_f32 v73, v73\n"
        "v_exp_f32 v74, v74\n"
        "v_smfmac_f32_32x32x32_bf16 %[oa], %[v00], v[64:71], %[idx]\n"
        "ds_read_b128 %[v11], v100 offset:3584\n"
        "global_load_dwordx4 a[156:159], %[voff], s[96:97] offset:-1024 sc1\n"
        "v_exp_f32 v75, v75\n"
        "v_exp_f32 v76, v76\n"
        "v_exp_f32 v77, v77\n"
        "v_exp_f32 v78, v78\n"
        "v_exp_f32 v79, v79\n"
        "v_exp_f32 v80, v80\n"
        "v_exp_f32 v81, v81\n"
        "v_exp_f32 v82, v82\n"
        "v_smfmac_f32_32x32x32_bf16 %[oa], %[v01], v[72:79], %[idx]\n"
        "s_waitcnt lgkmcnt(2)\n"
        "global_load_dwordx4 a[164:167], %[voff], s[96:97] offset:0 sc1\n"
        "v_exp_f32 v83, v83\n"
        "v_exp_f32 v84, v84\n"
        "v_exp_f32 v85, v85\n"
        "v_exp_f32 v86, v86\n"
        "v_mfma_f32_32x32x16_bf16 v[64:79], %[k1], %[qa], 0\n"
        "global_load_dwordx4 a[172:175], %[voff], s[96:97] offset:1024 sc1\n"
        "v_exp_f32 v87, v87\n"
        "v_exp_f32 v88, v88\n"
        "v_exp_f32 v89, v89\n"
        "v_exp_f32 v90, v90\n"
        "v_smfmac_f32_32x32x32_bf16 %[ob], %[v00], v[80:87], %[idx]\n"
        "s_waitcnt lgkmcnt(0)\n"
        "global_load_dwordx4 a[180:183], %[voff], s[96:97] offset:2048 sc1\n"
        "v_exp_f32 v91, v91\n"
        "v_exp_f32 v92, v92\n"
        "v_exp_f32 v93, v93\n"
        "v_exp_f32 v94, v94\n"
        "v_exp_f32 v95, v95\n"
        "v_exp_f32 v64, v64\n"
        "v_exp_f32 v65, v65\n"
        "v_exp_f32 v66, v66\n"
        "v_smfmac_f32_32x32x32_bf16 %[ob], %[v01], v[88:95], %[idx]\n"
        "ds_read_b128 %[k0], v99 offset:4096\n"
        "global_load_dwordx4 a[192:195], %[voff], s[94:95] offset:-4096 sc1\n"
        "v_exp_f32 v67, v67\n"
        "v_exp_f32 v68, v68\n"
        "v_exp_f32 v69, v69\n"
        "v_exp_f32 v70, v70\n"
        "v_mfma_f32_32x32x16_bf16 v[80:95], %[k1], %[qb], 0\n"
        "ds_read_b128 %[v00], v100 offset:4096\n"
        "global_load_dwordx4 a[216:219], %[voff], s[94:95] offset:-1024 sc1\n"
        "v_exp_f32 v71, v71\n"
        "v_exp_f32 v72, v72\n"
        "v_exp_f32 v73, v73\n"
        "v_exp_f32 v74, v74\n"
        "v_smfmac_f32_32x32x32_bf16 %[oa], %[v10], v[64:71], %[idx]\n"
        "ds_read_b128 %[v01], v100 offset:4608\n"
        "global_load_dwordx4 a[200:203], %[voff], s[94:95] offset:-3072 sc1\n"
        "v_exp_f32 v75, v75\n"
        "v_exp_f32 v76, v76\n"
        "v_exp_f32 v77, v77\n"
        "v_exp_f32 v78, v78\n"
        "v_exp_f32 v79, v79\n"
        "v_exp_f32 v80, v80\n"
        "v_exp_f32 v81, v81\n"
        "v_exp_f32 v82, v82\n"
        "v_smfmac_f32_32x32x32_bf16 %[oa], %[v11], v[72:79], %[idx]\n"
        "s_waitcnt lgkmcnt(2)\n"
        "global_load_dwordx4 a[208:211], %[voff], s[94:95] offset:-2048 sc1\n"
        "v_exp_f32 v83, v83\n"
        "v_exp_f32 v84, v84\n"
        "v_exp_f32 v85, v85\n"
        "v_exp_f32 v86, v86\n"
        "v_mfma_f32_32x32x16_bf16 v[64:79], %[k0], %[qa], 0\n"
        "s_waitcnt vmcnt(20)\n"
        "v_exp_f32 v87, v87\n"
        "v_exp_f32 v88, v88\n"
        "v_exp_f32 v89, v89\n"
        "v_exp_f32 v90, v90\n"
        "v_smfmac_f32_32x32x32_bf16 %[ob], %[v10], v[80:87], %[idx]\n"
        "s_waitcnt lgkmcnt(0)\n"
        "s_barrier\n"
        "v_exp_f32 v91, v91\n"
        "v_exp_f32 v92, v92\n"
        "v_exp_f32 v93, v93\n"
        "v_exp_f32 v94, v94\n"
        "v_exp_f32 v95, v95\n"
        "v_exp_f32 v64, v64\n"
        "v_exp_f32 v65, v65\n"
        "v_exp_f32 v66, v66\n"
        "v_smfmac_f32_32x32x32_bf16 %[ob], %[v11], v[88:95], %[idx]\n"
        "ds_read_b128 %[k1], v99 offset:5120\n"
        "s_mov_b32 m0, s73\n"
        "v_exp_f32 v67, v67\n"
        "v_exp_f32 v68, v68\n"
        "v_exp_f32 v69, v69\n"
        "v_exp_f32 v70, v70\n"
        "v_mfma_f32_32x32x16_bf16 v[80:95], %[k0], %[qb], 0\n"
        "ds_read_b128 %[v10], v100 offset:5120\n"
        "global_load_dwordx4 a[240:243], %[voff], s[94:95] offset:2048 sc1\n"
        "v_exp_f32 v71, v71\n"
        "v_exp_f32 v72, v72\n"
        "v_exp_f32 v73, v73\n"
        "v_exp_f32 v74, v74\n"
        "v_smfmac_f32_32x32x32_bf16 %[oa], %[v00], v[64:71], %[idx]\n"
        "ds_read_b128 %[v11], v100 offset:5632\n"
        "global_load_lds_dwordx4 %[voff], s[74:75] sc1\n"
        "v_exp_f32 v75, v75\n"
        "v_exp_f32 v76, v76\n"
        "v_exp_f32 v77, v77\n"
        "v_exp_f32 v78, v78\n"
        "v_exp_f32 v79, v79\n"
        "v_exp_f32 v80, v80\n"
        "v_exp_f32 v81, v81\n"
        "v_exp_f32 v82, v82\n"
        "v_smfmac_f32_32x32x32_bf16 %[oa], %[v01], v[72:79], %[idx]\n"
        "s_waitcnt lgkmcnt(2)\n"
        "global_load_dwordx4 a[224:227], %[voff], s[94:95] offset:0 sc1\n"
        "v_exp_f32 v83, v83\n"
        "v_exp_f32 v84, v84\n"
        "v_exp_f32 v85, v85\n"
        "v_exp_f32 v86, v86\n"
        "v_mfma_f32_32x32x16_bf16 v[64:79], %[k1], %[qa], 0\n"
        "global_load_dwordx4 a[232:235], %[voff], s[94:95] offset:1024 sc1\n"
        "v_exp_f32 v87, v87\n"
        "v_exp_f32 v88, v88\n"
        "v_exp_f32 v89, v89\n"
        "v_exp_f32 v90, v90\n"
        "v_smfmac_f32_32x32x32_bf16 %[ob], %[v00], v[80:87], %[idx]\n"
        "s_waitcnt lgkmcnt(0)\n"
        "global_load_lds_dwordx4 %[voff], s[74:75] offset:1024 sc1\n"
        "v_exp_f32 v91, v91\n"
        "v_exp_f32 v92, v92\n"
        "v_exp_f32 v93, v93\n"
        "v_exp_f32 v94, v94\n"
        "v_exp_f32 v95, v95\n"
        "v_exp_f32 v64, v64\n"
        "v_exp_f32 v65, v65\n"
        "v_exp_f32 v66, v66\n"
        "v_smfmac_f32_32x32x32_bf16 %[ob], %[v01], v[88:95], %[idx]\n"
        "ds_read_b128 %[k0], v99 offset:6144\n"
        "s_add_u32 m0, s73, 65536\n"
        "v_exp_f32 v67, v67\n"
        "v_exp_f32 v68, v68\n"
        "v_exp_f32 v69, v69\n"
        "v_exp_f32 v70, v70\n"
        "v_mfma_f32_32x32x16_bf16 v[80:95], %[k1], %[qb], 0\n"
        "ds_read_b128 %[v00], v100 offset:6144\n"
        "global_load_dwordx4 a[204:207], %[voff], s[98:99] offset:-3072 sc1\n"
        "v_exp_f32 v71, v71\n"
        "v_exp_f32 v72, v72\n"
        "v_exp_f32 v73, v73\n"
        "v_exp_f32 v74, v74\n"
        "v_smfmac_f32_32x32x32_bf16 %[oa], %[v10], v[64:71], %[idx]\n"
        "ds_read_b128 %[v01], v100 offset:6656\n"
        "global_load_lds_dwordx4 %[voff], s[76:77] sc1\n"
        "v_exp_f32 v75, v75\n"
        "v_exp_f32 v76, v76\n"
        "v_exp_f32 v77, v77\n"
        "v_exp_f32 v78, v78\n"
        "v_exp_f32 v79, v79\n"
        "v_exp_f32 v80, v80\n"
        "v_exp_f32 v81, v81\n"
        "v_exp_f32 v82, v82\n"
        "v_smfmac_f32_32x32x32_bf16 %[oa], %[v11], v[72:79], %[idx]\n"
        "s_waitcnt lgkmcnt(2)\n"
        "global_load_dwordx4 a[248:251], %[voff], s[94:95] offset:3072 sc1\n"
        "v_exp_f32 v83, v83\n"
        "v_exp_f32 v84, v84\n"
        "v_exp_f32 v85, v85\n"
        "v_exp_f32 v86, v86\n"
        "v_mfma_f32_32x32x16_bf16 v[64:79], %[k0], %[qa], 0\n"
        "global_load_dwordx4 a[196:199], %[voff], s[98:99] offset:-4096 sc1\n"
        "v_exp_f32 v87, v87\n"
        "v_exp_f32 v88, v88\n"
        "v_exp_f32 v89, v89\n"
        "v_exp_f32 v90, v90\n"
        "v_smfmac_f32_32x32x32_bf16 %[ob], %[v10], v[80:87], %[idx]\n"
        "s_waitcnt lgkmcnt(0)\n"
        "global_load_lds_dwordx4 %[voff], s[76:77] offset:1024 sc1\n"
        "v_exp_f32 v91, v91\n"
        "v_exp_f32 v92, v92\n"
        "v_exp_f32 v93, v93\n"
        "v_exp_f32 v94, v94\n"
        "v_exp_f32 v95, v95\n"
        "v_exp_f32 v64, v64\n"
        "v_exp_f32 v65, v65\n"
        "v_exp_f32 v66, v66\n"
        "v_smfmac_f32_32x32x32_bf16 %[ob], %[v11], v[88:95], %[idx]\n"
        "ds_read_b128 %[k1], v99 offset:7168\n"
        "s_add_u32 s73, s73, 8192\n"
        "v_exp_f32 v67, v67\n"
        "v_exp_f32 v68, v68\n"
        "v_exp_f32 v69, v69\n"
        "v_exp_f32 v70, v70\n"
        "v_mfma_f32_32x32x16_bf16 v[80:95], %[k0], %[qb], 0\n"
        "ds_read_b128 %[v10], v100 offset:7168\n"
        "global_load_dwordx4 a[228:231], %[voff], s[98:99] offset:0 sc1\n"
        "v_exp_f32 v71, v71\n"
        "v_exp_f32 v72, v72\n"
        "v_exp_f32 v73, v73\n"
        "v_exp_f32 v74, v74\n"
        "v_smfmac_f32_32x32x32_bf16 %[oa], %[v00], v[64:71], %[idx]\n"
        "ds_read_b128 %[v11], v100 offset:7680\n"
        "s_add_u32 s74, s74, 8192\n"
        "v_exp_f32 v75, v75\n"
        "v_exp_f32 v76, v76\n"
        "v_exp_f32 v77, v77\n"
        "v_exp_f32 v78, v78\n"
        "v_exp_f32 v79, v79\n"
        "v_exp_f32 v80, v80\n"
        "v_exp_f32 v81, v81\n"
        "v_exp_f32 v82, v82\n"
        "v_smfmac_f32_32x32x32_bf16 %[oa], %[v01], v[72:79], %[idx]\n"
        "s_waitcnt lgkmcnt(2)\n"
        "s_addc_u32 s75, s75, 0\n"
        "v_exp_f32 v83, v83\n"
        "v_exp_f32 v84, v84\n"
        "v_exp_f32 v85, v85\n"
        "v_exp_f32 v86, v86\n"
        "v_mfma_f32_32x32x16_bf16 v[64:79], %[k1], %[qa], 0\n"
        "global_load_dwordx4 a[212:215], %[voff], s[98:99] offset:-2048 sc1\n"
        "v_exp_f32 v87, v87\n"
        "v_exp_f32 v88, v88\n"
        "v_exp_f32 v89, v89\n"
        "v_exp_f32 v90, v90\n"
        "v_smfmac_f32_32x32x32_bf16 %[ob], %[v00], v[80:87], %[idx]\n"
        "s_waitcnt lgkmcnt(0)\n"
        "global_load_dwordx4 a[220:223], %[voff], s[98:99] offset:-1024 sc1\n"
        "v_exp_f32 v91, v91\n"
        "v_exp_f32 v92, v92\n"
        "v_exp_f32 v93, v93\n"
        "v_exp_f32 v94, v94\n"
        "v_exp_f32 v95, v95\n"
        "v_exp_f32 v64, v64\n"
        "v_exp_f32 v65, v65\n"
        "v_exp_f32 v66, v66\n"
        "v_smfmac_f32_32x32x32_bf16 %[ob], %[v01], v[88:95], %[idx]\n"
        "ds_read_b128 %[k0], v99 offset:8192\n"
        "global_load_dwordx4 a[236:239], %[voff], s[98:99] offset:1024 sc1\n"
        "v_exp_f32 v67, v67\n"
        "v_exp_f32 v68, v68\n"
        "v_exp_f32 v69, v69\n"
        "v_exp_f32 v70, v70\n"
        "v_mfma_f32_32x32x16_bf16 v[80:95], %[k1], %[qb], 0\n"
        "ds_read_b128 %[v00], v100 offset:8192\n"
        "v_exp_f32 v71, v71\n"
        "v_exp_f32 v72, v72\n"
        "v_exp_f32 v73, v73\n"
        "v_exp_f32 v74, v74\n"
        "v_smfmac_f32_32x32x32_bf16 %[oa], %[v10], v[64:71], %[idx]\n"
        "ds_read_b128 %[v01], v100 offset:8704\n"
        "global_load_dwordx4 a[244:247], %[voff], s[98:99] offset:2048 sc1\n"
        "v_exp_f32 v75, v75\n"
        "v_exp_f32 v76, v76\n"
        "v_exp_f32 v77, v77\n"
        "v_exp_f32 v78, v78\n"
        "v_exp_f32 v79, v79\n"
        "v_exp_f32 v80, v80\n"
        "v_exp_f32 v81, v81\n"
        "v_exp_f32 v82, v82\n"
        "v_smfmac_f32_32x32x32_bf16 %[oa], %[v11], v[72:79], %[idx]\n"
        "s_waitcnt lgkmcnt(2)\n"
        "global_load_dwordx4 a[252:255], %[voff], s[98:99] offset:3072 sc1\n"
        "v_exp_f32 v83, v83\n"
        "v_exp_f32 v84, v84\n"
        "v_exp_f32 v85, v85\n"
        "v_exp_f32 v86, v86\n"
        "v_mfma_f32_32x32x16_bf16 v[64:79], %[k0], %[qa], 0\n"
        "v_exp_f32 v87, v87\n"
        "v_exp_f32 v88, v88\n"
        "v_exp_f32 v89, v89\n"
        "v_exp_f32 v90, v90\n"
        "v_smfmac_f32_32x32x32_bf16 %[ob], %[v10], v[80:87], %[idx]\n"
        "s_waitcnt lgkmcnt(0)\n"
        "v_add_u32 v99, 8192, v99\n"
        "v_add_u32 v100, %[vinc], v100\n"
        "v_exp_f32 v91, v91\n"
        "v_exp_f32 v92, v92\n"
        "v_exp_f32 v93, v93\n"
        "v_exp_f32 v94, v94\n"
        "v_exp_f32 v95, v95\n"
        "8:\n"
        "v_exp_f32 v64, v64\n"
        "v_exp_f32 v65, v65\n"
        "v_exp_f32 v66, v66\n"
        "v_smfmac_f32_32x32x32_bf16 %[ob], %[v11], v[88:95], %[idx]\n"
        "ds_read_b128 %[k1], v99 offset:1024\n"
        "s_add_u32 m0, %[m13], 0\n"
        "s_add_u32 s82, %[w13l], 0\n"
        "s_addc_u32 s83, %[w13h], 0\n"
        "global_load_lds_dwordx4 %[voff], s[82:83] offset:0 sc1\n"
        "v_exp_f32 v67, v67\n"
        "v_exp_f32 v68, v68\n"
        "v_exp_f32 v69, v69\n"
        "v_exp_f32 v70, v70\n"
        "v_mfma_f32_32x32x16_bf16 v[80:95], %[k0], %[qb], 0\n"
        "ds_read_b128 %[v10], v100 offset:1024\n"
        "global_load_lds_dwordx4 %[voff], s[82:83] offset:1024 sc1\n"
        "v_exp_f32 v71, v71\n"
        "v_exp_f32 v72, v72\n"
        "v_exp_f32 v73, v73\n"
        "v_exp_f32 v74, v74\n"
        "v_smfmac_f32_32x32x32_bf16 %[oa], %[v00], v[64:71], %[idx]\n"
        "ds_read_b128 %[v11], v100 offset:1536\n"
        "global_load_lds_dwordx4 %[voff], s[82:83] offset:2048 sc1\n"
        "v_exp_f32 v75, v75\n"
        "v_exp_f32 v76, v76\n"
        "v_exp_f32 v77, v77\n"
        "v_exp_f32 v78, v78\n"
        "v_exp_f32 v79, v79\n"
        "v_exp_f32 v80, v80\n"
        "v_exp_f32 v81, v81\n"
        "v_exp_f32 v82, v82\n"
        "v_smfmac_f32_32x32x32_bf16 %[oa], %[v01], v[72:79], %[idx]\n"
        "s_waitcnt lgkmcnt(2)\n"
        "s_add_u32 s76, s74, 65536\n"
        "v_exp_f32 v83, v83\n"
        "v_exp_f32 v84, v84\n"
        "v_exp_f32 v85, v85\n"
        "v_exp_f32 v86, v86\n"
        "v_mfma_f32_32x32x16_bf16 v[64:79], %[k1], %[qa], 0\n"
        "s_addc_u32 s77, s75, 0\n"
        "v_exp_f32 v87, v87\n"
        "v_exp_f32 v88, v88\n"
        "v_exp_f32 v89, v89\n"
        "v_exp_f32 v90, v90\n"
        "v_smfmac_f32_32x32x32_bf16 %[ob], %[v00], v[80:87], %[idx]\n"
        "s_waitcnt lgkmcnt(0)\n"
        "global_load_lds_dwordx4 %[voff], s[82:83] offset:3072 sc1\n"
        "v_exp_f32 v91, v91\n"
        "v_exp_f32 v92, v92\n"
        "v_exp_f32 v93, v93\n"
        "v_exp_f32 v94, v94\n"
        "v_exp_f32 v95, v95\n"
        "v_exp_f32 v64, v64\n"
        "v_exp_f32 v65, v65\n"
        "v_exp_f32 v66, v66\n"
        "v_smfmac_f32_32x32x32_bf16 %[ob], %[v01], v[88:95], %[idx]\n"
        "ds_read_b128 %[k0], v99 offset:2048\n"
        "global_load_dwordx4 a[0:3], %[voff], s[84:85] offset:-4096 sc1\n"
        "v_exp_f32 v67, v67\n"
        "v_exp_f32 v68, v68\n"
        "v_exp_f32 v69, v69\n"
        "v_exp_f32 v70, v70\n"
        "v_mfma_f32_32x32x16_bf16 v[80:95], %[k1], %[qb], 0\n"
        "ds_read_b128 %[v00], v100 offset:2048\n"
        "v_exp_f32 v71, v71\n"
        "v_exp_f32 v72, v72\n"
        "v_exp_f32 v73, v73\n"
        "v_exp_f32 v74, v74\n"
        "v_smfmac_f32_32x32x32_bf16 %[oa], %[v10], v[64:71], %[idx]\n"
        "ds_read_b128 %[v01], v100 offset:2560\n"
        "global_load_dwordx4 a[4:7], %[voff], s[84:85] offset:-3072 sc1\n"
        "v_exp_f32 v75, v75\n"
        "v_exp_f32 v76, v76\n"
        "v_exp_f32 v77, v77\n"
        "v_exp_f32 v78, v78\n"
        "v_exp_f32 v79, v79\n"
        "v_exp_f32 v80, v80\n"
        "v_exp_f32 v81, v81\n"
        "v_exp_f32 v82, v82\n"
        "v_smfmac_f32_32x32x32_bf16 %[oa], %[v11], v[72:79], %[idx]\n"
        "s_waitcnt lgkmcnt(2)\n"
        "global_load_dwordx4 a[8:11], %[voff], s[84:85] offset:-2048 sc1\n"
        "v_exp_f32 v83, v83\n"
        "v_exp_f32 v84, v84\n"
        "v_exp_f32 v85, v85\n"
        "v_exp_f32 v86, v86\n"
        "v_mfma_f32_32x32x16_bf16 v[64:79], %[k0], %[qa], 0\n"
        "global_load_dwordx4 a[12:15], %[voff], s[84:85] offset:-1024 sc1\n"
        "v_exp_f32 v87, v87\n"
        "v_exp_f32 v88, v88\n"
        "v_exp_f32 v89, v89\n"
        "v_exp_f32 v90, v90\n"
        "v_smfmac_f32_32x32x32_bf16 %[ob], %[v10], v[80:87], %[idx]\n"
        "s_waitcnt lgkmcnt(0)\n"
        "v_exp_f32 v91, v91\n"
        "v_exp_f32 v92, v92\n"
        "v_exp_f32 v93, v93\n"
        "v_exp_f32 v94, v94\n"
        "v_exp_f32 v95, v95\n"
        "v_exp_f32 v64, v64\n"
        "v_exp_f32 v65, v65\n"
        "v_exp_f32 v66, v66\n"
        "v_smfmac_f32_32x32x32_bf16 %[ob], %[v11], v[88:95], %[idx]\n"
        "ds_read_b128 %[k1], v99 offset:3072\n"
        "s_add_u32 m0, %[m13], 4096\n"
        "s_add_u32 s82, %[w13l], 4096\n"
        "s_addc_u32 s83, %[w13h], 0\n"
        "global_load_lds_dwordx4 %[voff], s[82:83] offset:0 sc1\n"
        "v_exp_f32 v67, v67\n"
        "v_exp_f32 v68, v68\n"
        "v_exp_f32 v69, v69\n"
        "v_exp_f32 v70, v70\n"
        "v_mfma_f32_32x32x16_bf16 v[80:95], %[k0], %[qb], 0\n"
        "ds_read_b128 %[v10], v100 offset:3072\n"
        "global_load_lds_dwordx4 %[voff], s[82:83] offset:1024 sc1\n"
        "v_exp_f32 v71, v71\n"
        "v_exp_f32 v72, v72\n"
        "v_exp_f32 v73, v73\n"
        "v_exp_f32 v74, v74\n"
        "v_smfmac_f32_32x32x32_bf16 %[oa], %[v00], v[64:71], %[idx]\n"
        "ds_read_b128 %[v11], v100 offset:3584\n"
        "global_load_lds_dwordx4 %[voff], s[82:83] offset:2048 sc1\n"
        "v_exp_f32 v75, v75\n"
        "v_exp_f32 v76, v76\n"
        "v_exp_f32 v77, v77\n"
        "v_exp_f32 v78, v78\n"
        "v_exp_f32 v79, v79\n"
        "v_exp_f32 v80, v80\n"
        "v_exp_f32 v81, v81\n"
        "v_exp_f32 v82, v82\n"
        "v_smfmac_f32_32x32x32_bf16 %[oa], %[v01], v[72:79], %[idx]\n"
        "s_waitcnt lgkmcnt(2)\n"
        "global_load_lds_dwordx4 %[voff], s[82:83] offset:3072 sc1\n"
        "v_exp_f32 v83, v83\n"
        "v_exp_f32 v84, v84\n"
        "v_exp_f32 v85, v85\n"
        "v_exp_f32 v86, v86\n"
        "v_mfma_f32_32x32x16_bf16 v[64:79], %[k1], %[qa], 0\n"
        "v_exp_f32 v87, v87\n"
        "v_exp_f32 v88, v88\n"
        "v_exp_f32 v89, v89\n"
        "v_exp_f32 v90, v90\n"
        "v_smfmac_f32_32x32x32_bf16 %[ob], %[v00], v[80:87], %[idx]\n"
        "s_waitcnt lgkmcnt(0)\n"
        "s_nop 0\n"
        "v_exp_f32 v91, v91\n"
        "v_exp_f32 v92, v92\n"
        "v_exp_f32 v93, v93\n"
        "v_exp_f32 v94, v94\n"
        "v_exp_f32 v95, v95\n"
        "v_exp_f32 v64, v64\n"
        "v_exp_f32 v65, v65\n"
        "v_exp_f32 v66, v66\n"
        "v_smfmac_f32_32x32x32_bf16 %[ob], %[v01], v[88:95], %[idx]\n"
        "ds_read_b128 %[k0], v99 offset:4096\n"
        "global_load_dwordx4 a[16:19], %[voff], s[84:85] offset:0 sc1\n"
        "v_exp_f32 v67, v67\n"
        "v_exp_f32 v68, v68\n"
        "v_exp_f32 v69, v69\n"
        "v_exp_f32 v70, v70\n"
        "v_mfma_f32_32x32x16_bf16 v[80:95], %[k1], %[qb], 0\n"
        "ds_read_b128 %[v00], v100 offset:4096\n"
        "global_load_dwordx4 a[28:31], %[voff], s[84:85] offset:3072 sc1\n"
        "v_exp_f32 v71, v71\n"
        "v_exp_f32 v72, v72\n"
        "v_exp_f32 v73, v73\n"
        "v_exp_f32 v74, v74\n"
        "v_smfmac_f32_32x32x32_bf16 %[oa], %[v10], v[64:71], %[idx]\n"
        "ds_read_b128 %[v01], v100 offset:4608\n"
        "global_load_dwordx4 a[20:23], %[voff], s[84:85] offset:1024 sc1\n"
        "v_exp_f32 v75, v75\n"
        "v_exp_f32 v76, v76\n"
        "v_exp_f32 v77, v77\n"
        "v_exp_f32 v78, v78\n"
        "v_exp_f32 v79, v79\n"
        "v_exp_f32 v80, v80\n"
        "v_exp_f32 v81, v81\n"
        "v_exp_f32 v82, v82\n"
        "v_smfmac_f32_32x32x32_bf16 %[oa], %[v11], v[72:79], %[idx]\n"
        "s_waitcnt lgkmcnt(2)\n"
        "global_load_dwordx4 a[24:27], %[voff], s[84:85] offset:2048 sc1\n"
        "v_exp_f32 v83, v83\n"
        "v_exp_f32 v84, v84\n"
        "v_exp_f32 v85, v85\n"
        "v_exp_f32 v86, v86\n"
        "v_mfma_f32_32x32x16_bf16 v[64:79], %[k0], %[qa], 0\n"
        "s_waitcnt vmcnt(16)\n"
        "v_exp_f32 v87, v87\n"
        "v_exp_f32 v88, v88\n"
        "v_exp_f32 v89, v89\n"
        "v_exp_f32 v90, v90\n"
        "v_smfmac_f32_32x32x32_bf16 %[ob], %[v10], v[80:87], %[idx]\n"
        "s_waitcnt lgkmcnt(0)\n"
        "s_barrier\n"
        "v_exp_f32 v91, v91\n"
        "v_exp_f32 v92, v92\n"
        "v_exp_f32 v93, v93\n"
        "v_exp_f32 v94, v94\n"
        "v_exp_f32 v95, v95\n"
        "v_exp_f32 v64, v64\n"
        "v_exp_f32 v65, v65\n"
        "v_exp_f32 v66, v66\n"
        "v_smfmac_f32_32x32x32_bf16 %[ob], %[v11], v[88:95], %[idx]\n"
        "ds_read_b128 %[k1], v99 offset:5120\n"
        "s_mov_b32 m0, s73\n"
        "v_exp_f32 v67, v67\n"
        "v_exp_f32 v68, v68\n"
        "v_exp_f32 v69, v69\n"
        "v_exp_f32 v70, v70\n"
        "v_mfma_f32_32x32x16_bf16 v[80:95], %[k0], %[qb], 0\n"
        "ds_read_b128 %[v10], v100 offset:5120\n"
        "global_load_dwordx4 a[40:43], %[voff], s[86:87] offset:-2048 sc1\n"
        "v_exp_f32 v71, v71\n"
        "v_exp_f32 v72, v72\n"
        "v_exp_f32 v73, v73\n"
        "v_exp_f32 v74, v74\n"
        "v_smfmac_f32_32x32x32_bf16 %[oa], %[v00], v[64:71], %[idx]\n"
        "ds_read_b128 %[v11], v100 offset:5632\n"
        "global_load_lds_dwordx4 %[voff], s[74:75] sc1\n"
        "v_exp_f32 v75, v75\n"
        "v_exp_f32 v76, v76\n"
        "v_exp_f32 v77, v77\n"
        "v_exp_f32 v78, v78\n"
        "v_exp_f32 v79, v79\n"
        "v_exp_f32 v80, v80\n"
        "v_exp_f32 v81, v81\n"
        "v_exp_f32 v82, v82\n"
        "v_smfmac_f32_32x32x32_bf16 %[oa], %[v01], v[72:79], %[idx]\n"
        "s_waitcnt lgkmcnt(2)\n"
        "global_load_dwordx4 a[32:35], %[voff], s[86:87] offset:-4096 sc1\n"
        "v_exp_f32 v83, v83\n"
        "v_exp_f32 v84, v84\n"
        "v_exp_f32 v85, v85\n"
        "v_exp_f32 v86, v86\n"
        "v_mfma_f32_32x32x16_bf16 v[64:79], %[k1], %[qa], 0\n"
        "global_load_dwordx4 a[36:39], %[voff], s[86:87] offset:-3072 sc1\n"
        "v_exp_f32 v87, v87\n"
        "v_exp_f32 v88, v88\n"
        "v_exp_f32 v89, v89\n"
        "v_exp_f32 v90, v90\n"
        "v_smfmac_f32_32x32x32_bf16 %[ob], %[v00], v[80:87], %[idx]\n"
        "s_waitcnt lgkmcnt(0)\n"
        "global_load_lds_dwordx4 %[voff], s[74:75] offset:1024 sc1\n"
        "v_exp_f32 v91, v91\n"
        "v_exp_f32 v92, v92\n"
        "v_exp_f32 v93, v93\n"
        "v_exp_f32 v94, v94\n"
        "v_exp_f32 v95, v95\n"
        "v_exp_f32 v64, v64\n"
        "v_exp_f32 v65, v65\n"
        "v_exp_f32 v66, v66\n"
        "v_smfmac_f32_32x32x32_bf16 %[ob], %[v01], v[88:95], %[idx]\n"
        "ds_read_b128 %[k0], v99 offset:6144\n"
        "s_add_u32 m0, s73, 65536\n"
        "v_exp_f32 v67, v67\n"
        "v_exp_f32 v68, v68\n"
        "v_exp_f32 v69, v69\n"
        "v_exp_f32 v70, v70\n"
        "v_mfma_f32_32x32x16_bf16 v[80:95], %[k1], %[qb], 0\n"
        "ds_read_b128 %[v00], v100 offset:6144\n"
        "global_load_dwordx4 a[52:55], %[voff], s[86:87] offset:1024 sc1\n"
        "v_exp_f32 v71, v71\n"
        "v_exp_f32 v72, v72\n"
        "v_exp_f32 v73, v73\n"
        "v_exp_f32 v74, v74\n"
        "v_smfmac_f32_32x32x32_bf16 %[oa], %[v10], v[64:71], %[idx]\n"
        "ds_read_b128 %[v01], v100 offset:6656\n"
        "global_load_lds_dwordx4 %[voff], s[76:77] sc1\n"
        "v_exp_f32 v75, v75\n"
        "v_exp_f32 v76, v76\n"
        "v_exp_f32 v77, v77\n"
        "v_exp_f32 v78, v78\n"
        "v_exp_f32 v79, v79\n"
        "v_exp_f32 v80, v80\n"
        "v_exp_f32 v81, v81\n"
        "v_exp_f32 v82, v82\n"
        "v_smfmac_f32_32x32x32_bf16 %[oa], %[v11], v[72:79], %[idx]\n"
        "s_waitcnt lgkmcnt(2)\n"
        "global_load_dwordx4 a[44:47], %[voff], s[86:87] offset:-1024 sc1\n"
        "v_exp_f32 v83, v83\n"
        "v_exp_f32 v84, v84\n"
        "v_exp_f32 v85, v85\n"
        "v_exp_f32 v86, v86\n"
        "v_mfma_f32_32x32x16_bf16 v[64:79], %[k0], %[qa], 0\n"
        "global_load_dwordx4 a[48:51], %[voff], s[86:87] offset:0 sc1\n"
        "v_exp_f32 v87, v87\n"
        "v_exp_f32 v88, v88\n"
        "v_exp_f32 v89, v89\n"
        "v_exp_f32 v90, v90\n"
        "v_smfmac_f32_32x32x32_bf16 %[ob], %[v10], v[80:87], %[idx]\n"
        "s_waitcnt lgkmcnt(0)\n"
        "global_load_lds_dwordx4 %[voff], s[76:77] offset:1024 sc1\n"
        "v_exp_f32 v91, v91\n"
        "v_exp_f32 v92, v92\n"
        "v_exp_f32 v93, v93\n"
        "v_exp_f32 v94, v94\n"
        "v_exp_f32 v95, v95\n"
        "v_exp_f32 v64, v64\n"
        "v_exp_f32 v65, v65\n"
        "v_exp_f32 v66, v66\n"
        "v_smfmac_f32_32x32x32_bf16 %[ob], %[v11], v[88:95], %[idx]\n"
        "ds_read_b128 %[k1], v99 offset:7168\n"
        "s_add_u32 s73, s73, 8192\n"
        "v_exp_f32 v67, v67\n"
        "v_exp_f32 v68, v68\n"
        "v_exp_f32 v69, v69\n"
        "v_exp_f32 v70, v70\n"
        "v_mfma_f32_32x32x16_bf16 v[80:95], %[k0], %[qb], 0\n"
        "ds_read_b128 %[v10], v100 offset:7168\n"
        "global_load_dwordx4 a[64:67], %[voff], s[88:89] offset:-4096 sc1\n"
        "v_exp_f32 v71, v71\n"
        "v_exp_f32 v72, v72\n"
        "v_exp_f32 v73, v73\n"
        "v_exp_f32 v74, v74\n"
        "v_smfmac_f32_32x32x32_bf16 %[oa], %[v00], v[64:71], %[idx]\n"
        "ds_read_b128 %[v11], v100 offset:7680\n"
        "s_add_u32 s74, s74, 8192\n"
        "v_exp_f32 v75, v75\n"
        "v_exp_f32 v76, v76\n"
        "v_exp_f32 v77, v77\n"
        "v_exp_f32 v78, v78\n"
        "v_exp_f32 v79, v79\n"
        "v_exp_f32 v80, v80\n"
        "v_exp_f32 v81, v81\n"
        "v_exp_f32 v82, v82\n"
        "v_smfmac_f32_32x32x32_bf16 %[oa], %[v01], v[72:79], %[idx]\n"
        "s_waitcnt lgkmcnt(2)\n"
        "s_addc_u32 s75, s75, 0\n"
        "v_exp_f32 v83, v83\n"
        "v_exp_f32 v84, v84\n"
        "v_exp_f32 v85, v85\n"
        "v_exp_f32 v86, v86\n"
        "v_mfma_f32_32x32x16_bf16 v[64:79], %[k1], %[qa], 0\n"
        "global_load_dwordx4 a[56:59], %[voff], s[86:87] offset:2048 sc1\n"
        "v_exp_f32 v87, v87\n"
        "v_exp_f32 v88, v88\n"
        "v_exp_f32 v89, v89\n"
        "v_exp_f32 v90, v90\n"
        "v_smfmac_f32_32x32x32_bf16 %[ob], %[v00], v[80:87], %[idx]\n"
        "s_waitcnt lgkmcnt(0)\n"
        "global_load_dwordx4 a[60:63], %[voff], s[86:87] offset:3072 sc1\n"
        "v_exp_f32 v91, v91\n"
        "v_exp_f32 v92, v92\n"
        "v_exp_f32 v93, v93\n"
        "v_exp_f32 v94, v94\n"
        "v_exp_f32 v95, v95\n"
        "v_exp_f32 v64, v64\n"
        "v_exp_f32 v65, v65\n"
        "v_exp_f32 v66, v66\n"
        "v_smfmac_f32_32x32x32_bf16 %[ob], %[v01], v[88:95], %[idx]\n"
        "ds_read_b128 %[k0], v99 offset:8192\n"
        "global_load_dwordx4 a[68:71], %[voff], s[88:89] offset:-3072 sc1\n"
        "v_exp_f32 v67, v67\n"
        "v_exp_f32 v68, v68\n"
        "v_exp_f32 v69, v69\n"
        "v_exp_f32 v70, v70\n"
        "v_mfma_f32_32x32x16_bf16 v[80:95], %[k1], %[qb], 0\n"
        "ds_read_b128 %[v00], v100 offset:8192\n"
        "v_exp_f32 v71, v71\n"
        "v_exp_f32 v72, v72\n"
        "v_exp_f32 v73, v73\n"
        "v_exp_f32 v74, v74\n"
        "v_smfmac_f32_32x32x32_bf16 %[oa], %[v10], v[64:71], %[idx]\n"
        "ds_read_b128 %[v01], v100 offset:8704\n"
        "global_load_dwordx4 a[72:75], %[voff], s[88:89] offset:-2048 sc1\n"
        "v_exp_f32 v75, v75\n"
        "v_exp_f32 v76, v76\n"
        "v_exp_f32 v77, v77\n"
        "v_exp_f32 v78, v78\n"
        "v_exp_f32 v79, v79\n"
        "v_exp_f32 v80, v80\n"
        "v_exp_f32 v81, v81\n"
        "v_exp_f32 v82, v82\n"
        "v_smfmac_f32_32x32x32_bf16 %[oa], %[v11], v[72:79], %[idx]\n"
        "s_waitcnt lgkmcnt(2)\n"
        "global_load_dwordx4 a[76:79], %[voff], s[88:89] offset:-1024 sc1\n"
        "v_exp_f32 v83, v83\n"
        "v_exp_f32 v84, v84\n"
        "v_exp_f32 v85, v85\n"
        "v_exp_f32 v86, v86\n"
        "v_mfma_f32_32x32x16_bf16 v[64:79], %[k0], %[qa], 0\n"
        "global_load_dwordx4 a[80:83], %[voff], s[88:89] offset:0 sc1\n"
        "v_exp_f32 v87, v87\n"
        "v_exp_f32 v88, v88\n"
        "v_exp_f32 v89, v89\n"
        "v_exp_f32 v90, v90\n"
        "v_smfmac_f32_32x32x32_bf16 %[ob], %[v10], v[80:87], %[idx]\n"
        "s_waitcnt lgkmcnt(0)\n"
        "v_add_u32 v99, 8192, v99\n"
        "v_add_u32 v100, %[vinc], v100\n"
        "v_exp_f32 v91, v91\n"
        "v_exp_f32 v92, v92\n"
        "v_exp_f32 v93, v93\n"
        "v_exp_f32 v94, v94\n"
        "v_exp_f32 v95, v95\n"
        "v_exp_f32 v64, v64\n"
        "v_exp_f32 v65, v65\n"
        "v_exp_f32 v66, v66\n"
        "v_smfmac_f32_32x32x32_bf16 %[ob], %[v11], v[88:95], %[idx]\n"
        "ds_read_b128 %[k1], v99 offset:1024\n"
        "global_load_dwordx4 a[84:87], %[voff], s[88:89] offset:1024 sc1\n"
        "v_exp_f32 v67, v67\n"
        "v_exp_f32 v68, v68\n"
        "v_exp_f32 v69, v69\n"
        "v_exp_f32 v70, v70\n"
        "v_mfma_f32_32x32x16_bf16 v[80:95], %[k0], %[qb], 0\n"
        "ds_read_b128 %[v10], v100 offset:1024\n"
        "global_load_dwordx4 a[96:99], %[voff], s[90:91] offset:-4096 sc1\n"
        "v_exp_f32 v71, v71\n"
        "v_exp_f32 v72, v72\n"
        "v_exp_f32 v73, v73\n"
        "v_exp_f32 v74, v74\n"
        "v_smfmac_f32_32x32x32_bf16 %[oa], %[v00], v[64:71], %[idx]\n"
        "ds_read_b128 %[v11], v100 offset:1536\n"
        "global_load_dwordx4 a[88:91], %[voff], s[88:89] offset:2048 sc1\n"
        "v_exp_f32 v75, v75\n"
        "v_exp_f32 v76, v76\n"
        "v_exp_f32 v77, v77\n"
        "v_exp_f32 v78, v78\n"
        "v_exp_f32 v79, v79\n"
        "v_exp_f32 v80, v80\n"
        "v_exp_f32 v81, v81\n"
        "v_exp_f32 v82, v82\n"
        "v_smfmac_f32_32x32x32_bf16 %[oa], %[v01], v[72:79], %[idx]\n"
        "s_waitcnt lgkmcnt(2)\n"
        "s_add_u32 s76, s74, 65536\n"
        "v_exp_f32 v83, v83\n"
        "v_exp_f32 v84, v84\n"
        "v_exp_f32 v85, v85\n"
        "v_exp_f32 v86, v86\n"
        "v_mfma_f32_32x32x16_bf16 v[64:79], %[k1], %[qa], 0\n"
        "s_addc_u32 s77, s75, 0\n"
        "v_exp_f32 v87, v87\n"
        "v_exp_f32 v88, v88\n"
        "v_exp_f32 v89, v89\n"
        "v_exp_f32 v90, v90\n"
        "v_smfmac_f32_32x32x32_bf16 %[ob], %[v00], v[80:87], %[idx]\n"
        "s_waitcnt lgkmcnt(0)\n"
        "global_load_dwordx4 a[92:95], %[voff], s[88:89] offset:3072 sc1\n"
        "v_exp_f32 v91, v91\n"
        "v_exp_f32 v92, v92\n"
        "v_exp_f32 v93, v93\n"
        "v_exp_f32 v94, v94\n"
        "v_exp_f32 v95, v95\n"
        "v_exp_f32 v64, v64\n"
        "v_exp_f32 v65, v65\n"
        "v_exp_f32 v66, v66\n"
        "v_smfmac_f32_32x32x32_bf16 %[ob], %[v01], v[88:95], %[idx]\n"
        "ds_read_b128 %[k0], v99 offset:2048\n"
        "global_load_dwordx4 a[100:103], %[voff], s[90:91] offset:-3072 sc1\n"
        "v_exp_f32 v67, v67\n"
        "v_exp_f32 v68, v68\n"
        "v_exp_f32 v69, v69\n"
        "v_exp_f32 v70, v70\n"
        "v_mfma_f32_32x32x16_bf16 v[80:95], %[k1], %[qb], 0\n"
        "ds_read_b128 %[v00], v100 offset:2048\n"
        "v_exp_f32 v71, v71\n"
        "v_exp_f32 v72, v72\n"
        "v_exp_f32 v73, v73\n"
        "v_exp_f32 v74, v74\n"
        "v_smfmac_f32_32x32x32_bf16 %[oa], %[v10], v[64:71], %[idx]\n"
        "ds_read_b128 %[v01], v100 offset:2560\n"
        "global_load_dwordx4 a[104:107], %[voff], s[90:91] offset:-2048 sc1\n"
        "v_exp_f32 v75, v75\n"
        "v_exp_f32 v76, v76\n"
        "v_exp_f32 v77, v77\n"
        "v_exp_f32 v78, v78\n"
        "v_exp_f32 v79, v79\n"
        "v_exp_f32 v80, v80\n"
        "v_exp_f32 v81, v81\n"
        "v_exp_f32 v82, v82\n"
        "v_smfmac_f32_32x32x32_bf16 %[oa], %[v11], v[72:79], %[idx]\n"
        "s_waitcnt lgkmcnt(2)\n"
        "global_load_dwordx4 a[108:111], %[voff], s[90:91] offset:-1024 sc1\n"
        "v_exp_f32 v83, v83\n"
        "v_exp_f32 v84, v84\n"
        "v_exp_f32 v85, v85\n"
        "v_exp_f32 v86, v86\n"
        "v_mfma_f32_32x32x16_bf16 v[64:79], %[k0], %[qa], 0\n"
        "global_load_dwordx4 a[112:115], %[voff], s[90:91] offset:0 sc1\n"
        "v_exp_f32 v87, v87\n"
        "v_exp_f32 v88, v88\n"
        "v_exp_f32 v89, v89\n"
        "v_exp_f32 v90, v90\n"
        "v_smfmac_f32_32x32x32_bf16 %[ob], %[v10], v[80:87], %[idx]\n"
        "s_waitcnt lgkmcnt(0)\n"
        "v_exp_f32 v91, v91\n"
        "v_exp_f32 v92, v92\n"
        "v_exp_f32 v93, v93\n"
        "v_exp_f32 v94, v94\n"
        "v_exp_f32 v95, v95\n"
        "v_exp_f32 v64, v64\n"
        "v_exp_f32 v65, v65\n"
        "v_exp_f32 v66, v66\n"
        "v_smfmac_f32_32x32x32_bf16 %[ob], %[v11], v[88:95], %[idx]\n"
        "ds_read_b128 %[k1], v99 offset:3072\n"
        "global_load_dwordx4 a[116:119], %[voff], s[90:91] offset:1024 sc1\n"
        "v_exp_f32 v67, v67\n"
        "v_exp_f32 v68, v68\n"
        "v_exp_f32 v69, v69\n"
        "v_exp_f32 v70, v70\n"
        "v_mfma_f32_32x32x16_bf16 v[80:95], %[k0], %[qb], 0\n"
        "ds_read_b128 %[v10], v100 offset:3072\n"
        "v_exp_f32 v71, v71\n"
        "v_exp_f32 v72, v72\n"
        "v_exp_f32 v73, v73\n"
        "v_exp_f32 v74, v74\n"
        "v_smfmac_f32_32x32x32_bf16 %[oa], %[v00], v[64:71], %[idx]\n"
        "ds_read_b128 %[v11], v100 offset:3584\n"
        "global_load_dwordx4 a[120:123], %[voff], s[90:91] offset:2048 sc1\n"
        "v_exp_f32 v75, v75\n"
        "v_exp_f32 v76, v76\n"
        "v_exp_f32 v77, v77\n"
        "v_exp_f32 v78, v78\n"
        "v_exp_f32 v79, v79\n"
        "v_exp_f32 v80, v80\n"
        "v_exp_f32 v81, v81\n"
        "v_exp_f32 v82, v82\n"
        "v_smfmac_f32_32x32x32_bf16 %[oa], %[v01], v[72:79], %[idx]\n"
        "s_waitcnt lgkmcnt(2)\n"
        "global_load_dwordx4 a[124:127], %[voff], s[90:91] offset:3072 sc1\n"
        "v_exp_f32 v83, v83\n"
        "v_exp_f32 v84, v84\n"
        "v_exp_f32 v85, v85\n"
        "v_exp_f32 v86, v86\n"
        "v_mfma_f32_32x32x16_bf16 v[64:79], %[k1], %[qa], 0\n"
        "v_exp_f32 v87, v87\n"
        "v_exp_f32 v88, v88\n"
        "v_exp_f32 v89, v89\n"
        "v_exp_f32 v90, v90\n"
        "v_smfmac_f32_32x32x32_bf16 %[ob], %[v00], v[80:87], %[idx]\n"
        "s_waitcnt lgkmcnt(0)\n"
        "s_nop 0\n"
        "v_exp_f32 v91, v91\n"
        "v_exp_f32 v92, v92\n"
        "v_exp_f32 v93, v93\n"
        "v_exp_f32 v94, v94\n"
        "v_exp_f32 v95, v95\n"
        "v_exp_f32 v64, v64\n"
        "v_exp_f32 v65, v65\n"
        "v_exp_f32 v66, v66\n"
        "v_smfmac_f32_32x32x32_bf16 %[ob], %[v01], v[88:95], %[idx]\n"
        "ds_read_b128 %[k0], v99 offset:4096\n"
        "v_exp_f32 v67, v67\n"
        "v_exp_f32 v68, v68\n"
        "v_exp_f32 v69, v69\n"
        "v_exp_f32 v70, v70\n"
        "v_mfma_f32_32x32x16_bf16 v[80:95], %[k1], %[qb], 0\n"
        "ds_read_b128 %[v00], v100 offset:4096\n"
        "v_exp_f32 v71, v71\n"
        "v_exp_f32 v72, v72\n"
        "v_exp_f32 v73, v73\n"
        "v_exp_f32 v74, v74\n"
        "v_smfmac_f32_32x32x32_bf16 %[oa], %[v10], v[64:71], %[idx]\n"
        "ds_read_b128 %[v01], v100 offset:4608\n"
        "v_exp_f32 v75, v75\n"
        "v_exp_f32 v76, v76\n"
        "v_exp_f32 v77, v77\n"
        "v_exp_f32 v78, v78\n"
        "v_exp_f32 v79, v79\n"
        "v_exp_f32 v80, v80\n"
        "v_exp_f32 v81, v81\n"
        "v_exp_f32 v82, v82\n"
        "v_smfmac_f32_32x32x32_bf16 %[oa], %[v11], v[72:79], %[idx]\n"
        "s_waitcnt lgkmcnt(2)\n"
        "v_exp_f32 v83, v83\n"
        "v_exp_f32 v84, v84\n"
        "v_exp_f32 v85, v85\n"
        "v_exp_f32 v86, v86\n"
        "v_mfma_f32_32x32x16_bf16 v[64:79], %[k0], %[qa], 0\n"
        "s_waitcnt vmcnt(18)\n"
        "v_exp_f32 v87, v87\n"
        "v_exp_f32 v88, v88\n"
        "v_exp_f32 v89, v89\n"
        "v_exp_f32 v90, v90\n"
        "v_smfmac_f32_32x32x32_bf16 %[ob], %[v10], v[80:87], %[idx]\n"
        "s_waitcnt lgkmcnt(0)\n"
        "s_barrier\n"
        "v_exp_f32 v91, v91\n"
        "v_exp_f32 v92, v92\n"
        "v_exp_f32 v93, v93\n"
        "v_exp_f32 v94, v94\n"
        "v_exp_f32 v95, v95\n"
        "v_exp_f32 v64, v64\n"
        "v_exp_f32 v65, v65\n"
        "v_exp_f32 v66, v66\n"
        "v_smfmac_f32_32x32x32_bf16 %[ob], %[v11], v[88:95], %[idx]\n"
        "ds_read_b128 %[k1], v99 offset:5120\n"
        "s_add_u32 m0, %[m13], 65536\n"
        "s_add_u32 s82, %[w13l], 65536\n"
        "s_addc_u32 s83, %[w13h], 0\n"
        "global_load_lds_dwordx4 %[voff], s[82:83] offset:0 sc1\n"
        "v_exp_f32 v67, v67\n"
        "v_exp_f32 v68, v68\n"
        "v_exp_f32 v69, v69\n"
        "v_exp_f32 v70, v70\n"
        "v_mfma_f32_32x32x16_bf16 v[80:95], %[k0], %[qb], 0\n"
        "ds_read_b128 %[v10], v100 offset:5120\n"
        "global_load_lds_dwordx4 %[voff], s[82:83] offset:1024 sc1\n"
        "v_exp_f32 v71, v71\n"
        "v_exp_f32 v72, v72\n"
        "v_exp_f32 v73, v73\n"
        "v_exp_f32 v74, v74\n"
        "v_smfmac_f32_32x32x32_bf16 %[oa], %[v00], v[64:71], %[idx]\n"
        "ds_read_b128 %[v11], v100 offset:5632\n"
        "global_load_lds_dwordx4 %[voff], s[82:83] offset:2048 sc1\n"
        "v_exp_f32 v75, v75\n"
        "v_exp_f32 v76, v76\n"
        "v_exp_f32 v77, v77\n"
        "v_exp_f32 v78, v78\n"
        "v_exp_f32 v79, v79\n"
        "v_exp_f32 v80, v80\n"
        "v_exp_f32 v81, v81\n"
        "v_exp_f32 v82, v82\n"
        "v_smfmac_f32_32x32x32_bf16 %[oa], %[v01], v[72:79], %[idx]\n"
        "s_waitcnt lgkmcnt(2)\n"
        "global_load_lds_dwordx4 %[voff], s[82:83] offset:3072 sc1\n"
        "v_exp_f32 v83, v83\n"
        "v_exp_f32 v84, v84\n"
        "v_exp_f32 v85, v85\n"
        "v_exp_f32 v86, v86\n"
        "v_mfma_f32_32x32x16_bf16 v[64:79], %[k1], %[qa], 0\n"
        "v_exp_f32 v87, v87\n"
        "v_exp_f32 v88, v88\n"
        "v_exp_f32 v89, v89\n"
        "v_exp_f32 v90, v90\n"
        "v_smfmac_f32_32x32x32_bf16 %[ob], %[v00], v[80:87], %[idx]\n"
        "s_waitcnt lgkmcnt(0)\n"
        "s_nop 0\n"
        "v_exp_f32 v91, v91\n"
        "v_exp_f32 v92, v92\n"
        "v_exp_f32 v93, v93\n"
        "v_exp_f32 v94, v94\n"
        "v_exp_f32 v95, v95\n"
        "v_exp_f32 v64, v64\n"
        "v_exp_f32 v65, v65\n"
        "v_exp_f32 v66, v66\n"
        "v_smfmac_f32_32x32x32_bf16 %[ob], %[v01], v[88:95], %[idx]\n"
        "ds_read_b128 %[k0], v99 offset:6144\n"
        "s_add_u32 m0, %[m13], 69632\n"
        "s_add_u32 s82, %[w13l], 69632\n"
        "s_addc_u32 s83, %[w13h], 0\n"
        "global_load_lds_dwordx4 %[voff], s[82:83] offset:0 sc1\n"
        "v_exp_f32 v67, v67\n"
        "v_exp_f32 v68, v68\n"
        "v_exp_f32 v69, v69\n"
        "v_exp_f32 v70, v70\n"
        "v_mfma_f32_32x32x16_bf16 v[80:95], %[k1], %[qb], 0\n"
        "ds_read_b128 %[v00], v100 offset:6144\n"
        "global_load_lds_dwordx4 %[voff], s[82:83] offset:1024 sc1\n"
        "v_exp_f32 v71, v71\n"
        "v_exp_f32 v72, v72\n"
        "v_exp_f32 v73, v73\n"
        "v_exp_f32 v74, v74\n"
        "v_smfmac_f32_32x32x32_bf16 %[oa], %[v10], v[64:71], %[idx]\n"
        "ds_read_b128 %[v01], v100 offset:6656\n"
        "global_load_lds_dwordx4 %[voff], s[82:83] offset:2048 sc1\n"
        "v_exp_f32 v75, v75\n"
        "v_exp_f32 v76, v76\n"
        "v_exp_f32 v77, v77\n"
        "v_exp_f32 v78, v78\n"
        "v_exp_f32 v79, v79\n"
        "v_exp_f32 v80, v80\n"
        "v_exp_f32 v81, v81\n"
        "v_exp_f32 v82, v82\n"
        "v_smfmac_f32_32x32x32_bf16 %[oa], %[v11], v[72:79], %[idx]\n"
        "s_waitcnt lgkmcnt(2)\n"
        "global_load_lds_dwordx4 %[voff], s[82:83] offset:3072 sc1\n"
        "v_exp_f32 v83, v83\n"
        "v_exp_f32 v84, v84\n"
        "v_exp_f32 v85, v85\n"
        "v_exp_f32 v86, v86\n"
        "v_mfma_f32_32x32x16_bf16 v[64:79], %[k0], %[qa], 0\n"
        "v_exp_f32 v87, v87\n"
        "v_exp_f32 v88, v88\n"
        "v_exp_f32 v89, v89\n"
        "v_exp_f32 v90, v90\n"
        "v_smfmac_f32_32x32x32_bf16 %[ob], %[v10], v[80:87], %[idx]\n"
        "s_waitcnt lgkmcnt(0)\n"
        "s_nop 0\n"
        "v_exp_f32 v91, v91\n"
        "v_exp_f32 v92, v92\n"
        "v_exp_f32 v93, v93\n"
        "v_exp_f32 v94, v94\n"
        "v_exp_f32 v95, v95\n"
        "v_exp_f32 v64, v64\n"
        "v_exp_f32 v65, v65\n"
        "v_exp_f32 v66, v66\n"
        "v_smfmac_f32_32x32x32_bf16 %[ob], %[v11], v[88:95], %[idx]\n"
        "ds_read_b128 %[k1], v99 offset:7168\n"
        "s_add_u32 s73, s73, 8192\n"
        "v_exp_f32 v67, v67\n"
        "v_exp_f32 v68, v68\n"
        "v_exp_f32 v69, v69\n"
        "v_exp_f32 v70, v70\n"
        "v_mfma_f32_32x32x16_bf16 v[80:95], %[k0], %[qb], 0\n"
        "ds_read_b128 %[v10], v100 offset:7168\n"
        "v_exp_f32 v71, v71\n"
        "v_exp_f32 v72, v72\n"
        "v_exp_f32 v73, v73\n"
        "v_exp_f32 v74, v74\n"
        "v_smfmac_f32_32x32x32_bf16 %[oa], %[v00], v[64:71], %[idx]\n"
        "ds_read_b128 %[v11], v100 offset:7680\n"
        "s_add_u32 s74, s74, 8192\n"
        "v_exp_f32 v75, v75\n"
        "v_exp_f32 v76, v76\n"
        "v_exp_f32 v77, v77\n"
        "v_exp_f32 v78, v78\n"
        "v_exp_f32 v79, v79\n"
        "v_exp_f32 v80, v80\n"
        "v_exp_f32 v81, v81\n"
        "v_exp_f32 v82, v82\n"
        "v_smfmac_f32_32x32x32_bf16 %[oa], %[v01], v[72:79], %[idx]\n"
        "s_waitcnt lgkmcnt(2)\n"
        "s_addc_u32 s75, s75, 0\n"
        "v_exp_f32 v83, v83\n"
        "v_exp_f32 v84, v84\n"
        "v_exp_f32 v85, v85\n"
        "v_exp_f32 v86, v86\n"
        "v_mfma_f32_32x32x16_bf16 v[64:79], %[k1], %[qa], 0\n"
        "v_exp_f32 v87, v87\n"
        "v_exp_f32 v88, v88\n"
        "v_exp_f32 v89, v89\n"
        "v_exp_f32 v90, v90\n"
        "v_smfmac_f32_32x32x32_bf16 %[ob], %[v00], v[80:87], %[idx]\n"
        "s_waitcnt lgkmcnt(0)\n"
        "s_nop 0\n"
        "v_exp_f32 v91, v91\n"
        "v_exp_f32 v92, v92\n"
        "v_exp_f32 v93, v93\n"
        "v_exp_f32 v94, v94\n"
        "v_exp_f32 v95, v95\n"
        "v_exp_f32 v64, v64\n"
        "v_exp_f32 v65, v65\n"
        "v_exp_f32 v66, v66\n"
        "v_smfmac_f32_32x32x32_bf16 %[ob], %[v01], v[88:95], %[idx]\n"
        "ds_read_b128 %[k0], v99 offset:8192\n"
        "v_exp_f32 v67, v67\n"
        "v_exp_f32 v68, v68\n"
        "v_exp_f32 v69, v69\n"
        "v_exp_f32 v70, v70\n"
        "v_mfma_f32_32x32x16_bf16 v[80:95], %[k1], %[qb], 0\n"
        "ds_read_b128 %[v00], v100 offset:8192\n"
        "v_exp_f32 v71, v71\n"
        "v_exp_f32 v72, v72\n"
        "v_exp_f32 v73, v73\n"
        "v_exp_f32 v74, v74\n"
        "v_smfmac_f32_32x32x32_bf16 %[oa], %[v10], v[64:71], %[idx]\n"
        "ds_read_b128 %[v01], v100 offset:8704\n"
        "v_exp_f32 v75, v75\n"
        "v_exp_f32 v76, v76\n"
        "v_exp_f32 v77, v77\n"
        "v_exp_f32 v78, v78\n"
        "v_exp_f32 v79, v79\n"
        "v_exp_f32 v80, v80\n"
        "v_exp_f32 v81, v81\n"
        "v_exp_f32 v82, v82\n"
        "v_smfmac_f32_32x32x32_bf16 %[oa], %[v11], v[72:79], %[idx]\n"
        "s_waitcnt lgkmcnt(2)\n"
        "v_exp_f32 v83, v83\n"
        "v_exp_f32 v84, v84\n"
        "v_exp_f32 v85, v85\n"
        "v_exp_f32 v86, v86\n"
        "v_mfma_f32_32x32x16_bf16 v[64:79], %[k0], %[qa], 0\n"
        "v_exp_f32 v87, v87\n"
        "v_exp_f32 v88, v88\n"
        "v_exp_f32 v89, v89\n"
        "v_exp_f32 v90, v90\n"
        "v_smfmac_f32_32x32x32_bf16 %[ob], %[v10], v[80:87], %[idx]\n"
        "s_waitcnt lgkmcnt(0)\n"
        "v_add_u32 v99, 8192, v99\n"
        "v_add_u32 v100, %[vinc], v100\n"
        "v_exp_f32 v91, v91\n"
        "v_exp_f32 v92, v92\n"
        "v_exp_f32 v93, v93\n"
        "v_exp_f32 v94, v94\n"
        "v_exp_f32 v95, v95\n"
        "v_exp_f32 v64, v64\n"
        "v_exp_f32 v65, v65\n"
        "v_exp_f32 v66, v66\n"
        "v_smfmac_f32_32x32x32_bf16 %[ob], %[v11], v[88:95], %[idx]\n"
        "ds_read_b128 %[k1], v99 offset:1024\n"
        "v_exp_f32 v67, v67\n"
        "v_exp_f32 v68, v68\n"
        "v_exp_f32 v69, v69\n"
        "v_exp_f32 v70, v70\n"
        "v_mfma_f32_32x32x16_bf16 v[80:95], %[k0], %[qb], 0\n"
        "ds_read_b128 %[v10], v100 offset:1024\n"
        "v_exp_f32 v71, v71\n"
        "v_exp_f32 v72, v72\n"
        "v_exp_f32 v73, v73\n"
        "v_exp_f32 v74, v74\n"
        "v_smfmac_f32_32x32x32_bf16 %[oa], %[v00], v[64:71], %[idx]\n"
        "ds_read_b128 %[v11], v100 offset:1536\n"
        "v_exp_f32 v75, v75\n"
        "v_exp_f32 v76, v76\n"
        "v_exp_f32 v77, v77\n"
        "v_exp_f32 v78, v78\n"
        "v_exp_f32 v79, v79\n"
        "v_exp_f32 v80, v80\n"
        "v_exp_f32 v81, v81\n"
        "v_exp_f32 v82, v82\n"
        "v_smfmac_f32_32x32x32_bf16 %[oa], %[v01], v[72:79], %[idx]\n"
        "s_waitcnt lgkmcnt(2)\n"
        "s_add_u32 s76, s74, 65536\n"
        "v_exp_f32 v83, v83\n"
        "v_exp_f32 v84, v84\n"
        "v_exp_f32 v85, v85\n"
        "v_exp_f32 v86, v86\n"
        "v_mfma_f32_32x32x16_bf16 v[64:79], %[k1], %[qa], 0\n"
        "s_addc_u32 s77, s75, 0\n"
        "v_exp_f32 v87, v87\n"
        "v_exp_f32 v88, v88\n"
        "v_exp_f32 v89, v89\n"
        "v_exp_f32 v90, v90\n"
        "v_smfmac_f32_32x32x32_bf16 %[ob], %[v00], v[80:87], %[idx]\n"
        "s_waitcnt lgkmcnt(0)\n"
        "v_exp_f32 v91, v91\n"
        "v_exp_f32 v92, v92\n"
        "v_exp_f32 v93, v93\n"
        "v_exp_f32 v94, v94\n"
        "v_exp_f32 v95, v95\n"
        "v_exp_f32 v64, v64\n"
        "v_exp_f32 v65, v65\n"
        "v_exp_f32 v66, v66\n"
        "v_smfmac_f32_32x32x32_bf16 %[ob], %[v01], v[88:95], %[idx]\n"
        "ds_read_b128 %[k0], v99 offset:2048\n"
        "v_exp_f32 v67, v67\n"
        "v_exp_f32 v68, v68\n"
        "v_exp_f32 v69, v69\n"
        "v_exp_f32 v70, v70\n"
        "v_mfma_f32_32x32x16_bf16 v[80:95], %[k1], %[qb], 0\n"
        "ds_read_b128 %[v00], v100 offset:2048\n"
        "v_exp_f32 v71, v71\n"
        "v_exp_f32 v72, v72\n"
        "v_exp_f32 v73, v73\n"
        "v_exp_f32 v74, v74\n"
        "v_smfmac_f32_32x32x32_bf16 %[oa], %[v10], v[64:71], %[idx]\n"
        "ds_read_b128 %[v01], v100 offset:2560\n"
        "v_exp_f32 v75, v75\n"
        "v_exp_f32 v76, v76\n"
        "v_exp_f32 v77, v77\n"
        "v_exp_f32 v78, v78\n"
        "v_exp_f32 v79, v79\n"
        "v_exp_f32 v80, v80\n"
        "v_exp_f32 v81, v81\n"
        "v_exp_f32 v82, v82\n"
        "v_smfmac_f32_32x32x32_bf16 %[oa], %[v11], v[72:79], %[idx]\n"
        "s_waitcnt lgkmcnt(2)\n"
        "v_exp_f32 v83, v83\n"
        "v_exp_f32 v84, v84\n"
        "v_exp_f32 v85, v85\n"
        "v_exp_f32 v86, v86\n"
        "v_mfma_f32_32x32x16_bf16 v[64:79], %[k0], %[qa], 0\n"
        "v_exp_f32 v87, v87\n"
        "v_exp_f32 v88, v88\n"
        "v_exp_f32 v89, v89\n"
        "v_exp_f32 v90, v90\n"
        "v_smfmac_f32_32x32x32_bf16 %[ob], %[v10], v[80:87], %[idx]\n"
        "s_waitcnt lgkmcnt(0)\n"
        "s_nop 0\n"
        "v_exp_f32 v91, v91\n"
        "v_exp_f32 v92, v92\n"
        "v_exp_f32 v93, v93\n"
        "v_exp_f32 v94, v94\n"
        "v_exp_f32 v95, v95\n"
        "v_exp_f32 v64, v64\n"
        "v_exp_f32 v65, v65\n"
        "v_exp_f32 v66, v66\n"
        "v_smfmac_f32_32x32x32_bf16 %[ob], %[v11], v[88:95], %[idx]\n"
        "ds_read_b128 %[k1], v99 offset:3072\n"
        "v_exp_f32 v67, v67\n"
        "v_exp_f32 v68, v68\n"
        "v_exp_f32 v69, v69\n"
        "v_exp_f32 v70, v70\n"
        "v_mfma_f32_32x32x16_bf16 v[80:95], %[k0], %[qb], 0\n"
        "ds_read_b128 %[v10], v100 offset:3072\n"
        "v_exp_f32 v71, v71\n"
        "v_exp_f32 v72, v72\n"
        "v_exp_f32 v73, v73\n"
        "v_exp_f32 v74, v74\n"
        "v_smfmac_f32_32x32x32_bf16 %[oa], %[v00], v[64:71], %[idx]\n"
        "ds_read_b128 %[v11], v100 offset:3584\n"
        "v_exp_f32 v75, v75\n"
        "v_exp_f32 v76, v76\n"
        "v_exp_f32 v77, v77\n"
        "v_exp_f32 v78, v78\n"
        "v_exp_f32 v79, v79\n"
        "v_exp_f32 v80, v80\n"
        "v_exp_f32 v81, v81\n"
        "v_exp_f32 v82, v82\n"
        "v_smfmac_f32_32x32x32_bf16 %[oa], %[v01], v[72:79], %[idx]\n"
        "s_waitcnt lgkmcnt(2)\n"
        "v_exp_f32 v83, v83\n"
        "v_exp_f32 v84, v84\n"
        "v_exp_f32 v85, v85\n"
        "v_exp_f32 v86, v86\n"
        "v_mfma_f32_32x32x16_bf16 v[64:79], %[k1], %[qa], 0\n"
        "v_exp_f32 v87, v87\n"
        "v_exp_f32 v88, v88\n"
        "v_exp_f32 v89, v89\n"
        "v_exp_f32 v90, v90\n"
        "v_smfmac_f32_32x32x32_bf16 %[ob], %[v00], v[80:87], %[idx]\n"
        "s_waitcnt lgkmcnt(0)\n"
        "s_nop 0\n"
        "v_exp_f32 v91, v91\n"
        "v_exp_f32 v92, v92\n"
        "v_exp_f32 v93, v93\n"
        "v_exp_f32 v94, v94\n"
        "v_exp_f32 v95, v95\n"
        "v_exp_f32 v64, v64\n"
        "v_exp_f32 v65, v65\n"
        "v_exp_f32 v66, v66\n"
        "v_smfmac_f32_32x32x32_bf16 %[ob], %[v01], v[88:95], %[idx]\n"
        "ds_read_b128 %[k0], v99 offset:4096\n"
        "v_exp_f32 v67, v67\n"
        "v_exp_f32 v68, v68\n"
        "v_exp_f32 v69, v69\n"
        "v_exp_f32 v70, v70\n"
        "v_mfma_f32_32x32x16_bf16 v[80:95], %[k1], %[qb], 0\n"
        "ds_read_b128 %[v00], v100 offset:4096\n"
        "v_exp_f32 v71, v71\n"
        "v_exp_f32 v72, v72\n"
        "v_exp_f32 v73, v73\n"
        "v_exp_f32 v74, v74\n"
        "v_smfmac_f32_32x32x32_bf16 %[oa], %[v10], v[64:71], %[idx]\n"
        "ds_read_b128 %[v01], v100 offset:4608\n"
        "v_exp_f32 v75, v75\n"
        "v_exp_f32 v76, v76\n"
        "v_exp_f32 v77, v77\n"
        "v_exp_f32 v78, v78\n"
        "v_exp_f32 v79, v79\n"
        "v_exp_f32 v80, v80\n"
        "v_exp_f32 v81, v81\n"
        "v_exp_f32 v82, v82\n"
        "v_smfmac_f32_32x32x32_bf16 %[oa], %[v11], v[72:79], %[idx]\n"
        "s_waitcnt lgkmcnt(2)\n"
        "v_exp_f32 v83, v83\n"
        "v_exp_f32 v84, v84\n"
        "v_exp_f32 v85, v85\n"
        "v_exp_f32 v86, v86\n"
        "v_mfma_f32_32x32x16_bf16 v[64:79], %[k0], %[qa], 0\n"
        "v_exp_f32 v87, v87\n"
        "v_exp_f32 v88, v88\n"
        "v_exp_f32 v89, v89\n"
        "v_exp_f32 v90, v90\n"
        "v_smfmac_f32_32x32x32_bf16 %[ob], %[v10], v[80:87], %[idx]\n"
        "s_waitcnt lgkmcnt(0)\n"
        "s_nop 0\n"
        "v_exp_f32 v91, v91\n"
        "v_exp_f32 v92, v92\n"
        "v_exp_f32 v93, v93\n"
        "v_exp_f32 v94, v94\n"
        "v_exp_f32 v95, v95\n"
        "v_exp_f32 v64, v64\n"
        "v_exp_f32 v65, v65\n"
        "v_exp_f32 v66, v66\n"
        "v_smfmac_f32_32x32x32_bf16 %[ob], %[v11], v[88:95], %[idx]\n"
        "ds_read_b128 %[k1], v99 offset:5120\n"
        "v_exp_f32 v67, v67\n"
        "v_exp_f32 v68, v68\n"
        "v_exp_f32 v69, v69\n"
        "v_exp_f32 v70, v70\n"
        "v_mfma_f32_32x32x16_bf16 v[80:95], %[k0], %[qb], 0\n"
        "ds_read_b128 %[v10], v100 offset:5120\n"
        "v_exp_f32 v71, v71\n"
        "v_exp_f32 v72, v72\n"
        "v_exp_f32 v73, v73\n"
        "v_exp_f32 v74, v74\n"
        "v_smfmac_f32_32x32x32_bf16 %[oa], %[v00], v[64:71], %[idx]\n"
        "ds_read_b128 %[v11], v100 offset:5632\n"
        "v_exp_f32 v75, v75\n"
        "v_exp_f32 v76, v76\n"
        "v_exp_f32 v77, v77\n"
        "v_exp_f32 v78, v78\n"
        "v_exp_f32 v79, v79\n"
        "v_exp_f32 v80, v80\n"
        "v_exp_f32 v81, v81\n"
        "v_exp_f32 v82, v82\n"
        "v_smfmac_f32_32x32x32_bf16 %[oa], %[v01], v[72:79], %[idx]\n"
        "s_waitcnt lgkmcnt(2)\n"
        "v_exp_f32 v83, v83\n"
        "v_exp_f32 v84, v84\n"
        "v_exp_f32 v85, v85\n"
        "v_exp_f32 v86, v86\n"
        "v_mfma_f32_32x32x16_bf16 v[64:79], %[k1], %[qa], 0\n"
        "v_exp_f32 v87, v87\n"
        "v_exp_f32 v88, v88\n"
        "v_exp_f32 v89, v89\n"
        "v_exp_f32 v90, v90\n"
        "v_smfmac_f32_32x32x32_bf16 %[ob], %[v00], v[80:87], %[idx]\n"
        "s_waitcnt lgkmcnt(0)\n"
        "s_nop 0\n"
        "v_exp_f32 v91, v91\n"
        "v_exp_f32 v92, v92\n"
        "v_exp_f32 v93, v93\n"
        "v_exp_f32 v94, v94\n"
        "v_exp_f32 v95, v95\n"
        "v_exp_f32 v64, v64\n"
        "v_exp_f32 v65, v65\n"
        "v_exp_f32 v66, v66\n"
        "v_smfmac_f32_32x32x32_bf16 %[ob], %[v01], v[88:95], %[idx]\n"
        "ds_read_b128 %[k0], v99 offset:6144\n"
        "v_exp_f32 v67, v67\n"
        "v_exp_f32 v68, v68\n"
        "v_exp_f32 v69, v69\n"
        "v_exp_f32 v70, v70\n"
        "v_mfma_f32_32x32x16_bf16 v[80:95], %[k1], %[qb], 0\n"
        "ds_read_b128 %[v00], v100 offset:6144\n"
        "v_exp_f32 v71, v71\n"
        "v_exp_f32 v72, v72\n"
        "v_exp_f32 v73, v73\n"
        "v_exp_f32 v74, v74\n"
        "v_smfmac_f32_32x32x32_bf16 %[oa], %[v10], v[64:71], %[idx]\n"
        "ds_read_b128 %[v01], v100 offset:6656\n"
        "v_exp_f32 v75, v75\n"
        "v_exp_f32 v76, v76\n"
        "v_exp_f32 v77, v77\n"
        "v_exp_f32 v78, v78\n"
        "v_exp_f32 v79, v79\n"
        "v_exp_f32 v80, v80\n"
        "v_exp_f32 v81, v81\n"
        "v_exp_f32 v82, v82\n"
        "v_smfmac_f32_32x32x32_bf16 %[oa], %[v11], v[72:79], %[idx]\n"
        "s_waitcnt lgkmcnt(2)\n"
        "v_exp_f32 v83, v83\n"
        "v_exp_f32 v84, v84\n"
        "v_exp_f32 v85, v85\n"
        "v_exp_f32 v86, v86\n"
        "v_mfma_f32_32x32x16_bf16 v[64:79], %[k0], %[qa], 0\n"
        "v_exp_f32 v87, v87\n"
        "v_exp_f32 v88, v88\n"
        "v_exp_f32 v89, v89\n"
        "v_exp_f32 v90, v90\n"
        "v_smfmac_f32_32x32x32_bf16 %[ob], %[v10], v[80:87], %[idx]\n"
        "s_waitcnt lgkmcnt(0)\n"
        "s_nop 0\n"
        "v_exp_f32 v91, v91\n"
        "v_exp_f32 v92, v92\n"
        "v_exp_f32 v93, v93\n"
        "v_exp_f32 v94, v94\n"
        "v_exp_f32 v95, v95\n"
        "v_exp_f32 v64, v64\n"
        "v_exp_f32 v65, v65\n"
        "v_exp_f32 v66, v66\n"
        "v_smfmac_f32_32x32x32_bf16 %[ob], %[v11], v[88:95], %[idx]\n"
        "ds_read_b128 %[k1], v99 offset:7168\n"
        "s_add_u32 s73, s73, 8192\n"
        "v_exp_f32 v67, v67\n"
        "v_exp_f32 v68, v68\n"
        "v_exp_f32 v69, v69\n"
        "v_exp_f32 v70, v70\n"
        "v_mfma_f32_32x32x16_bf16 v[80:95], %[k0], %[qb], 0\n"
        "ds_read_b128 %[v10], v100 offset:7168\n"
        "v_exp_f32 v71, v71\n"
        "v_exp_f32 v72, v72\n"
        "v_exp_f32 v73, v73\n"
        "v_exp_f32 v74, v74\n"
        "v_smfmac_f32_32x32x32_bf16 %[oa], %[v00], v[64:71], %[idx]\n"
        "ds_read_b128 %[v11], v100 offset:7680\n"
        "s_add_u32 s74, s74, 8192\n"
        "v_exp_f32 v75, v75\n"
        "v_exp_f32 v76, v76\n"
        "v_exp_f32 v77, v77\n"
        "v_exp_f32 v78, v78\n"
        "v_exp_f32 v79, v79\n"
        "v_exp_f32 v80, v80\n"
        "v_exp_f32 v81, v81\n"
        "v_exp_f32 v82, v82\n"
        "v_smfmac_f32_32x32x32_bf16 %[oa], %[v01], v[72:79], %[idx]\n"
        "s_waitcnt lgkmcnt(2)\n"
        "s_addc_u32 s75, s75, 0\n"
        "v_exp_f32 v83, v83\n"
        "v_exp_f32 v84, v84\n"
        "v_exp_f32 v85, v85\n"
        "v_exp_f32 v86, v86\n"
        "v_mfma_f32_32x32x16_bf16 v[64:79], %[k1], %[qa], 0\n"
        "v_exp_f32 v87, v87\n"
        "v_exp_f32 v88, v88\n"
        "v_exp_f32 v89, v89\n"
        "v_exp_f32 v90, v90\n"
        "v_smfmac_f32_32x32x32_bf16 %[ob], %[v00], v[80:87], %[idx]\n"
        "s_waitcnt lgkmcnt(0)\n"
        "s_nop 0\n"
        "v_exp_f32 v91, v91\n"
        "v_exp_f32 v92, v92\n"
        "v_exp_f32 v93, v93\n"
        "v_exp_f32 v94, v94\n"
        "v_exp_f32 v95, v95\n"
        "v_exp_f32 v64, v64\n"
        "v_exp_f32 v65, v65\n"
        "v_exp_f32 v66, v66\n"
        "v_smfmac_f32_32x32x32_bf16 %[ob], %[v01], v[88:95], %[idx]\n"
        "ds_read_b128 %[k0], v99 offset:8192\n"
        "v_exp_f32 v67, v67\n"
        "v_exp_f32 v68, v68\n"
        "v_exp_f32 v69, v69\n"
        "v_exp_f32 v70, v70\n"
        "v_mfma_f32_32x32x16_bf16 v[80:95], %[k1], %[qb], 0\n"
        "ds_read_b128 %[v00], v100 offset:8192\n"
        "v_exp_f32 v71, v71\n"
        "v_exp_f32 v72, v72\n"
        "v_exp_f32 v73, v73\n"
        "v_exp_f32 v74, v74\n"
        "v_smfmac_f32_32x32x32_bf16 %[oa], %[v10], v[64:71], %[idx]\n"
        "ds_read_b128 %[v01], v100 offset:8704\n"
        "v_exp_f32 v75, v75\n"
        "v_exp_f32 v76, v76\n"
        "v_exp_f32 v77, v77\n"
        "v_exp_f32 v78, v78\n"
        "v_exp_f32 v79, v79\n"
        "v_exp_f32 v80, v80\n"
        "v_exp_f32 v81, v81\n"
        "v_exp_f32 v82, v82\n"
        "v_smfmac_f32_32x32x32_bf16 %[oa], %[v11], v[72:79], %[idx]\n"
        "s_waitcnt lgkmcnt(2)\n"
        "v_exp_f32 v83, v83\n"
        "v_exp_f32 v84, v84\n"
        "v_exp_f32 v85, v85\n"
        "v_exp_f32 v86, v86\n"
        "v_mfma_f32_32x32x16_bf16 v[64:79], %[k0], %[qa], 0\n"
        "v_exp_f32 v87, v87\n"
        "v_exp_f32 v88, v88\n"
        "v_exp_f32 v89, v89\n"
        "v_exp_f32 v90, v90\n"
        "v_smfmac_f32_32x32x32_bf16 %[ob], %[v10], v[80:87], %[idx]\n"
        "s_waitcnt lgkmcnt(0)\n"
        "s_nop 0\n"
        "v_exp_f32 v91, v91\n"
        "v_exp_f32 v92, v92\n"
        "v_exp_f32 v93, v93\n"
        "v_exp_f32 v94, v94\n"
        "v_exp_f32 v95, v95\n"
        "s_nop 1\n"
        "v_smfmac_f32_32x32x32_bf16 %[ob], %[v11], v[88:95], %[idx]\n"
        "s_nop 7\n"
        "s_nop 7\n"
                    ATT_OUT : ATT_IN);
                SBAR();
            } else {
                f32x16 ma, mb;
                float m0 = -INFINITY, m1 = -INFINITY;
                for (int t = 0; t < 64; t++) {
                    u32x4 kf;
                    f32x16 s0, s1;
                    asm volatile("global_load_dwordx4 %0, %3, %4 sc1\n\ts_waitcnt vmcnt(0)\n\tv_mfma_f32_32x32x16_bf16 %1, %0, %5, 0\n\t"
                                 "v_mfma_f32_32x32x16_bf16 %2, %0, %6, 0\n\ts_nop 7\n\ts_nop 7"
                                 : "=&v"(kf), "=&v"(s0), "=&v"(s1) : "v"(voff + t * 1024), "s"(kv), "v"(qa), "v"(qb) : "memory");
#pragma unroll
                    for (int i = 0; i < 16; i++) { m0 = fmaxf(m0, s0[i]); m1 = fmaxf(m1, s1[i]); }
                }
                m0 = xmax(m0); m1 = xmax(m1);
#pragma unroll
                for (int i = 0; i < 16; i++) { ma[i] = -m0; mb[i] = -m1; }
                SBAR();
                asm volatile(
        "v_mov_b32 v99, %[ka0]\n"
        "v_mov_b32 v100, %[va0]\n"
        "ds_read_b128 %[k0], v99\n"
        "ds_read_b128 %[v00], v100\n"
        "ds_read_b128 %[v01], v100 offset:512\n"
        "ds_read_b128 %[v11], v100 offset:512\n"
        "v_mov_b64 v[80:81], 0\n"
        "v_mov_b64 v[82:83], 0\n"
        "v_mov_b64 v[84:85], 0\n"
        "v_mov_b64 v[86:87], 0\n"
        "v_mov_b64 v[88:89], 0\n"
        "v_mov_b64 v[90:91], 0\n"
        "v_mov_b64 v[92:93], 0\n"
        "v_mov_b64 v[94:95], 0\n"
        "v_mov_b64 v[128:129], 0\n"
        "v_mov_b64 v[130:131], 0\n"
        "v_mov_b64 v[132:133], 0\n"
        "v_mov_b64 v[134:135], 0\n"
        "v_mov_b64 v[136:137], 0\n"
        "v_mov_b64 v[138:139], 0\n"
        "v_mov_b64 v[140:141], 0\n"
        "v_mov_b64 v[142:143], 0\n"
        "v_mov_b64 v[144:145], 0\n"
        "v_mov_b64 v[146:147], 0\n"
        "v_mov_b64 v[148:149], 0\n"
        "v_mov_b64 v[150:151], 0\n"
        "v_mov_b64 v[152:153], 0\n"
        "v_mov_b64 v[154:155], 0\n"
        "v_mov_b64 v[156:157], 0\n"
        "v_mov_b64 v[158:159], 0\n"
        "s_mov_b32 s72, 8\n"
        "s_mov_b64 s[74:75], %[skv]\n"
        "s_add_u32 s73, %[mkv], 17408\n"
        "s_add_u32 s74, s74, 17408\n"
        "s_addc_u32 s75, s75, 0\n"
        "s_add_u32 s84, %[wol], 0\n"
        "s_addc_u32 s85, %[woh], 0\n"
        "s_add_u32 s86, %[wol], 8192\n"
        "s_addc_u32 s87, %[woh], 0\n"
        "s_add_u32 s88, %[wol], 16384\n"
        "s_addc_u32 s89, %[woh], 0\n"
        "s_add_u32 s90, %[wol], 24576\n"
        "s_addc_u32 s91, %[woh], 0\n"
        "s_add_u32 s92, %[w2al], 0\n"
        "s_addc_u32 s93, %[w2ah], 0\n"
        "s_add_u32 s94, %[w2al], 8192\n"
        "s_addc_u32 s95, %[w2ah], 0\n"
        "s_add_u32 s96, %[w2bl], 0\n"
        "s_addc_u32 s97, %[w2bh], 0\n"
        "s_add_u32 s98, %[w2bl], 8192\n"
        "s_addc_u32 s99, %[w2bh], 0\n"
        "s_waitcnt lgkmcnt(0)\n"
        "v_mfma_f32_32x32x16_bf16 v[64:79], %[k0], %[qa], %[ma]\n"
        "s_nop 7\n"
        "s_nop 3\n"
        "v_mov_b32 v97, 0\n"
        "v_mov_b32 v98, 1\n"
        "s_or_b32 s80, %[wid], %[lay1]\n"
        "s_cselect_b32 s80, 99, 7\n"
        "s_sub_u32 s79, 4, %[lay1]\n"
        "s_cmp_eq_u64 %[jd], 0\n"
        "s_cbranch_scc1 1f\n"
        "v_exp_f32 v64, v64\n"
        "v_exp_f32 v65, v65\n"
        "v_exp_f32 v66, v66\n"
        "v_smfmac_f32_32x32x32_bf16 %[ob], %[v11], v[88:95], %[idx]\n"
        "ds_read_b128 %[k1], v99 offset:1024\n"
        "v_exp_f32 v67, v67\n"
        "v_exp_f32 v68, v68\n"
        "v_exp_f32 v69, v69\n"
        "v_exp_f32 v70, v70\n"
        "v_mfma_f32_32x32x16_bf16 v[80:95], %[k0], %[qb], %[mb]\n"
        "ds_read_b128 %[v10], v100 offset:1024\n"
        "v_exp_f32 v71, v71\n"
        "v_exp_f32 v72, v72\n"
        "v_exp_f32 v73, v73\n"
        "v_exp_f32 v74, v74\n"
        "v_smfmac_f32_32x32x32_bf16 %[oa], %[v00], v[64:71], %[idx]\n"
        "ds_read_b128 %[v11], v100 offset:1536\n"
        "global_store_dwordx4 v224, v[192:195], %[jd] sc1\n"
        "v_exp_f32 v75, v75\n"
        "v_exp_f32 v76, v76\n"
        "v_exp_f32 v77, v77\n"
        "v_exp_f32 v78, v78\n"
        "v_exp_f32 v79, v79\n"
        "v_exp_f32 v80, v80\n"
        "v_exp_f32 v81, v81\n"
        "v_exp_f32 v82, v82\n"
        "v_smfmac_f32_32x32x32_bf16 %[oa], %[v01], v[72:79], %[idx]\n"
        "s_waitcnt lgkmcnt(2)\n"
        "s_add_u32 s76, s74, 65536\n"
        "v_exp_f32 v83, v83\n"
        "v_exp_f32 v84, v84\n"
        "v_exp_f32 v85, v85\n"
        "v_exp_f32 v86, v86\n"
        "v_mfma_f32_32x32x16_bf16 v[64:79], %[k1], %[qa], %[ma]\n"
        "s_addc_u32 s77, s75, 0\n"
        "v_exp_f32 v87, v87\n"
        "v_exp_f32 v88, v88\n"
        "v_exp_f32 v89, v89\n"
        "v_exp_f32 v90, v90\n"
        "v_smfmac_f32_32x32x32_bf16 %[ob], %[v00], v[80:87], %[idx]\n"
        "s_waitcnt lgkmcnt(0)\n"
        "v_exp_f32 v91, v91\n"
        "v_exp_f32 v92, v92\n"
        "v_exp_f32 v93, v93\n"
        "v_exp_f32 v94, v94\n"
        "v_exp_f32 v95, v95\n"
        "v_exp_f32 v64, v64\n"
        "v_exp_f32 v65, v65\n"
        "v_exp_f32 v66, v66\n"
        "v_smfmac_f32_32x32x32_bf16 %[ob], %[v01], v[88:95], %[idx]\n"
        "ds_read_b128 %[k0], v99 offset:2048\n"
        "v_exp_f32 v67, v67\n"
        "v_exp_f32 v68, v68\n"
        "v_exp_f32 v69, v69\n"
        "v_exp_f32 v70, v70\n"
        "v_mfma_f32_32x32x16_bf16 v[80:95], %[k1], %[qb], %[mb]\n"
        "ds_read_b128 %[v00], v100 offset:2048\n"
        "v_exp_f32 v71, v71\n"
        "v_exp_f32 v72, v72\n"
        "v_exp_f32 v73, v73\n"
        "v_exp_f32 v74, v74\n"
        "v_smfmac_f32_32x32x32_bf16 %[oa], %[v10], v[64:71], %[idx]\n"
        "ds_read_b128 %[v01], v100 offset:2560\n"
        "v_exp_f32 v75, v75\n"
        "v_exp_f32 v76, v76\n"
        "v_exp_f32 v77, v77\n"
        "v_exp_f32 v78, v78\n"
        "v_exp_f32 v79, v79\n"
        "v_exp_f32 v80, v80\n"
        "v_exp_f32 v81, v81\n"
        "v_exp_f32 v82, v82\n"
        "v_smfmac_f32_32x32x32_bf16 %[oa], %[v11], v[72:79], %[idx]\n"
        "s_waitcnt lgkmcnt(2)\n"
        "global_store_dwordx4 v225, v[196:199], %[jd] sc1\n"
        "v_exp_f32 v83, v83\n"
        "v_exp_f32 v84, v84\n"
        "v_exp_f32 v85, v85\n"
        "v_exp_f32 v86, v86\n"
        "v_mfma_f32_32x32x16_bf16 v[64:79], %[k0], %[qa], %[ma]\n"
        "v_exp_f32 v87, v87\n"
        "v_exp_f32 v88, v88\n"
        "v_exp_f32 v89, v89\n"
        "v_exp_f32 v90, v90\n"
        "v_smfmac_f32_32x32x32_bf16 %[ob], %[v10], v[80:87], %[idx]\n"
        "s_waitcnt lgkmcnt(0)\n"
        "s_nop 0\n"
        "v_exp_f32 v91, v91\n"
        "v_exp_f32 v92, v92\n"
        "v_exp_f32 v93, v93\n"
        "v_exp_f32 v94, v94\n"
        "v_exp_f32 v95, v95\n"
        "v_exp_f32 v64, v64\n"
        "v_exp_f32 v65, v65\n"
        "v_exp_f32 v66, v66\n"
        "v_smfmac_f32_32x32x32_bf16 %[ob], %[v11], v[88:95], %[idx]\n"
        "ds_read_b128 %[k1], v99 offset:3072\n"
        "v_exp_f32 v67, v67\n"
        "v_exp_f32 v68, v68\n"
        "v_exp_f32 v69, v69\n"
        "v_exp_f32 v70, v70\n"
        "v_mfma_f32_32x32x16_bf16 v[80:95], %[k0], %[qb], %[mb]\n"
        "ds_read_b128 %[v10], v100 offset:3072\n"
        "v_exp_f32 v71, v71\n"
        "v_exp_f32 v72, v72\n"
        "v_exp_f32 v73, v73\n"
        "v_exp_f32 v74, v74\n"
        "v_smfmac_f32_32x32x32_bf16 %[oa], %[v00], v[64:71], %[idx]\n"
        "ds_read_b128 %[v11], v100 offset:3584\n"
        "v_exp_f32 v75, v75\n"
        "v_exp_f32 v76, v76\n"
        "v_exp_f32 v77, v77\n"
        "v_exp_f32 v78, v78\n"
        "v_exp_f32 v79, v79\n"
        "v_exp_f32 v80, v80\n"
        "v_exp_f32 v81, v81\n"
        "v_exp_f32 v82, v82\n"
        "v_smfmac_f32_32x32x32_bf16 %[oa], %[v01], v[72:79], %[idx]\n"
        "s_waitcnt lgkmcnt(2)\n"
        "global_store_dwordx4 v226, v[200:203], %[jd] sc1\n"
        "v_exp_f32 v83, v83\n"
        "v_exp_f32 v84, v84\n"
        "v_exp_f32 v85, v85\n"
        "v_exp_f32 v86, v86\n"
        "v_mfma_f32_32x32x16_bf16 v[64:79], %[k1], %[qa], %[ma]\n"
        "v_exp_f32 v87, v87\n"
        "v_exp_f32 v88, v88\n"
        "v_exp_f32 v89, v89\n"
        "v_exp_f32 v90, v90\n"
        "v_smfmac_f32_32x32x32_bf16 %[ob], %[v00], v[80:87], %[idx]\n"
        "s_waitcnt lgkmcnt(0)\n"
        "s_nop 0\n"
        "v_exp_f32 v91, v91\n"
        "v_exp_f32 v92, v92\n"
        "v_exp_f32 v93, v93\n"
        "v_exp_f32 v94, v94\n"
        "v_exp_f32 v95, v95\n"
        "v_exp_f32 v64, v64\n"
        "v_exp_f32 v65, v65\n"
        "v_exp_f32 v66, v66\n"
        "v_smfmac_f32_32x32x32_bf16 %[ob], %[v01], v[88:95], %[idx]\n"
        "ds_read_b128 %[k0], v99 offset:4096\n"
        "v_exp_f32 v67, v67\n"
        "v_exp_f32 v68, v68\n"
        "v_exp_f32 v69, v69\n"
        "v_exp_f32 v70, v70\n"
        "v_mfma_f32_32x32x16_bf16 v[80:95], %[k1], %[qb], %[mb]\n"
        "ds_read_b128 %[v00], v100 offset:4096\n"
        "v_exp_f32 v71, v71\n"
        "v_exp_f32 v72, v72\n"
        "v_exp_f32 v73, v73\n"
        "v_exp_f32 v74, v74\n"
        "v_smfmac_f32_32x32x32_bf16 %[oa], %[v10], v[64:71], %[idx]\n"
        "ds_read_b128 %[v01], v100 offset:4608\n"
        "v_exp_f32 v75, v75\n"
        "v_exp_f32 v76, v76\n"
        "v_exp_f32 v77, v77\n"
        "v_exp_f32 v78, v78\n"
        "v_exp_f32 v79, v79\n"
        "v_exp_f32 v80, v80\n"
        "v_exp_f32 v81, v81\n"
        "v_exp_f32 v82, v82\n"
        "v_smfmac_f32_32x32x32_bf16 %[oa], %[v11], v[72:79], %[idx]\n"
        "s_waitcnt lgkmcnt(2)\n"
        "global_store_dwordx4 v227, v[204:207], %[jd] sc1\n"
        "v_exp_f32 v83, v83\n"
        "v_exp_f32 v84, v84\n"
        "v_exp_f32 v85, v85\n"
        "v_exp_f32 v86, v86\n"
        "v_mfma_f32_32x32x16_bf16 v[64:79], %[k0], %[qa], %[ma]\n"
        "s_waitcnt vmcnt(4)\n"
        "v_exp_f32 v87, v87\n"
        "v_exp_f32 v88, v88\n"
        "v_exp_f32 v89, v89\n"
        "v_exp_f32 v90, v90\n"
        "v_smfmac_f32_32x32x32_bf16 %[ob], %[v10], v[80:87], %[idx]\n"
        "s_waitcnt lgkmcnt(0)\n"
        "s_barrier\n"
        "v_exp_f32 v91, v91\n"
        "v_exp_f32 v92, v92\n"
        "v_exp_f32 v93, v93\n"
        "v_exp_f32 v94, v94\n"
        "v_exp_f32 v95, v95\n"
        "v_exp_f32 v64, v64\n"
        "v_exp_f32 v65, v65\n"
        "v_exp_f32 v66, v66\n"
        "v_smfmac_f32_32x32x32_bf16 %[ob], %[v11], v[88:95], %[idx]\n"
        "ds_read_b128 %[k1], v99 offset:5120\n"
        "s_mov_b32 m0, s73\n"
        "v_exp_f32 v67, v67\n"
        "v_exp_f32 v68, v68\n"
        "v_exp_f32 v69, v69\n"
        "v_exp_f32 v70, v70\n"
        "v_mfma_f32_32x32x16_bf16 v[80:95], %[k0], %[qb], %[mb]\n"
        "ds_read_b128 %[v10], v100 offset:5120\n"
        "v_exp_f32 v71, v71\n"
        "v_exp_f32 v72, v72\n"
        "v_exp_f32 v73, v73\n"
        "v_exp_f32 v74, v74\n"
        "v_smfmac_f32_32x32x32_bf16 %[oa], %[v00], v[64:71], %[idx]\n"
        "ds_read_b128 %[v11], v100 offset:5632\n"
        "global_load_lds_dwordx4 %[voff], s[74:75] sc1\n"
        "v_exp_f32 v75, v75\n"
        "v_exp_f32 v76, v76\n"
        "v_exp_f32 v77, v77\n"
        "v_exp_f32 v78, v78\n"
        "v_exp_f32 v79, v79\n"
        "v_exp_f32 v80, v80\n"
        "v_exp_f32 v81, v81\n"
        "v_exp_f32 v82, v82\n"
        "v_smfmac_f32_32x32x32_bf16 %[oa], %[v01], v[72:79], %[idx]\n"
        "s_waitcnt lgkmcnt(2)\n"
        "global_store_dwordx4 v228, v[208:211], %[jd] sc1\n"
        "v_exp_f32 v83, v83\n"
        "v_exp_f32 v84, v84\n"
        "v_exp_f32 v85, v85\n"
        "v_exp_f32 v86, v86\n"
        "v_mfma_f32_32x32x16_bf16 v[64:79], %[k1], %[qa], %[ma]\n"
        "v_exp_f32 v87, v87\n"
        "v_exp_f32 v88, v88\n"
        "v_exp_f32 v89, v89\n"
        "v_exp_f32 v90, v90\n"
        "v_smfmac_f32_32x32x32_bf16 %[ob], %[v00], v[80:87], %[idx]\n"
        "s_waitcnt lgkmcnt(0)\n"
        "global_load_lds_dwordx4 %[voff], s[74:75] offset:1024 sc1\n"
        "v_exp_f32 v91, v91\n"
        "v_exp_f32 v92, v92\n"
        "v_exp_f32 v93, v93\n"
        "v_exp_f32 v94, v94\n"
        "v_exp_f32 v95, v95\n"
        "v_exp_f32 v64, v64\n"
        "v_exp_f32 v65, v65\n"
        "v_exp_f32 v66, v66\n"
        "v_smfmac_f32_32x32x32_bf16 %[ob], %[v01], v[88:95], %[idx]\n"
        "ds_read_b128 %[k0], v99 offset:6144\n"
        "s_add_u32 m0, s73, 65536\n"
        "v_exp_f32 v67, v67\n"
        "v_exp_f32 v68, v68\n"
        "v_exp_f32 v69, v69\n"
        "v_exp_f32 v70, v70\n"
        "v_mfma_f32_32x32x16_bf16 v[80:95], %[k1], %[qb], %[mb]\n"
        "ds_read_b128 %[v00], v100 offset:6144\n"
        "v_exp_f32 v71, v71\n"
        "v_exp_f32 v72, v72\n"
        "v_exp_f32 v73, v73\n"
        "v_exp_f32 v74, v74\n"
        "v_smfmac_f32_32x32x32_bf16 %[oa], %[v10], v[64:71], %[idx]\n"
        "ds_read_b128 %[v01], v100 offset:6656\n"
        "global_load_lds_dwordx4 %[voff], s[76:77] sc1\n"
        "v_exp_f32 v75, v75\n"
        "v_exp_f32 v76, v76\n"
        "v_exp_f32 v77, v77\n"
        "v_exp_f32 v78, v78\n"
        "v_exp_f32 v79, v79\n"
        "v_exp_f32 v80, v80\n"
        "v_exp_f32 v81, v81\n"
        "v_exp_f32 v82, v82\n"
        "v_smfmac_f32_32x32x32_bf16 %[oa], %[v11], v[72:79], %[idx]\n"
        "s_waitcnt lgkmcnt(2)\n"
        "global_store_dwordx4 v229, v[212:215], %[jd] sc1\n"
        "v_exp_f32 v83, v83\n"
        "v_exp_f32 v84, v84\n"
        "v_exp_f32 v85, v85\n"
        "v_exp_f32 v86, v86\n"
        "v_mfma_f32_32x32x16_bf16 v[64:79], %[k0], %[qa], %[ma]\n"
        "v_exp_f32 v87, v87\n"
        "v_exp_f32 v88, v88\n"
        "v_exp_f32 v89, v89\n"
        "v_exp_f32 v90, v90\n"
        "v_smfmac_f32_32x32x32_bf16 %[ob], %[v10], v[80:87], %[idx]\n"
        "s_waitcnt lgkmcnt(0)\n"
        "global_load_lds_dwordx4 %[voff], s[76:77] offset:1024 sc1\n"
        "v_exp_f32 v91, v91\n"
        "v_exp_f32 v92, v92\n"
        "v_exp_f32 v93, v93\n"
        "v_exp_f32 v94, v94\n"
        "v_exp_f32 v95, v95\n"
        "v_exp_f32 v64, v64\n"
        "v_exp_f32 v65, v65\n"
        "v_exp_f32 v66, v66\n"
        "v_smfmac_f32_32x32x32_bf16 %[ob], %[v11], v[88:95], %[idx]\n"
        "ds_read_b128 %[k1], v99 offset:7168\n"
        "s_add_u32 s73, s73, 8192\n"
        "v_exp_f32 v67, v67\n"
        "v_exp_f32 v68, v68\n"
        "v_exp_f32 v69, v69\n"
        "v_exp_f32 v70, v70\n"
        "v_mfma_f32_32x32x16_bf16 v[80:95], %[k0], %[qb], %[mb]\n"
        "ds_read_b128 %[v10], v100 offset:7168\n"
        "v_exp_f32 v71, v71\n"
        "v_exp_f32 v72, v72\n"
        "v_exp_f32 v73, v73\n"
        "v_exp_f32 v74, v74\n"
        "v_smfmac_f32_32x32x32_bf16 %[oa], %[v00], v[64:71], %[idx]\n"
        "ds_read_b128 %[v11], v100 offset:7680\n"
        "s_add_u32 s74, s74, 8192\n"
        "v_exp_f32 v75, v75\n"
        "v_exp_f32 v76, v76\n"
        "v_exp_f32 v77, v77\n"
        "v_exp_f32 v78, v78\n"
        "v_exp_f32 v79, v79\n"
        "v_exp_f32 v80, v80\n"
        "v_exp_f32 v81, v81\n"
        "v_exp_f32 v82, v82\n"
        "v_smfmac_f32_32x32x32_bf16 %[oa], %[v01], v[72:79], %[idx]\n"
        "s_waitcnt lgkmcnt(2)\n"
        "s_addc_u32 s75, s75, 0\n"
        "v_exp_f32 v83, v83\n"
        "v_exp_f32 v84, v84\n"
        "v_exp_f32 v85, v85\n"
        "v_exp_f32 v86, v86\n"
        "v_mfma_f32_32x32x16_bf16 v[64:79], %[k1], %[qa], %[ma]\n"
        "global_store_dwordx4 v230, v[216:219], %[jd] sc1\n"
        "v_exp_f32 v87, v87\n"
        "v_exp_f32 v88, v88\n"
        "v_exp_f32 v89, v89\n"
        "v_exp_f32 v90, v90\n"
        "v_smfmac_f32_32x32x32_bf16 %[ob], %[v00], v[80:87], %[idx]\n"
        "s_waitcnt lgkmcnt(0)\n"
        "v_exp_f32 v91, v91\n"
        "v_exp_f32 v92, v92\n"
        "v_exp_f32 v93, v93\n"
        "v_exp_f32 v94, v94\n"
        "v_exp_f32 v95, v95\n"
        "v_exp_f32 v64, v64\n"
        "v_exp_f32 v65, v65\n"
        "v_exp_f32 v66, v66\n"
        "v_smfmac_f32_32x32x32_bf16 %[ob], %[v01], v[88:95], %[idx]\n"
        "ds_read_b128 %[k0], v99 offset:8192\n"
        "global_store_dwordx4 v231, v[220:223], %[jd] sc1\n"
        "v_exp_f32 v67, v67\n"
        "v_exp_f32 v68, v68\n"
        "v_exp_f32 v69, v69\n"
        "v_exp_f32 v70, v70\n"
        "v_mfma_f32_32x32x16_bf16 v[80:95], %[k1], %[qb], %[mb]\n"
        "ds_read_b128 %[v00], v100 offset:8192\n"
        "v_exp_f32 v71, v71\n"
        "v_exp_f32 v72, v72\n"
        "v_exp_f32 v73, v73\n"
        "v_exp_f32 v74, v74\n"
        "v_smfmac_f32_32x32x32_bf16 %[oa], %[v10], v[64:71], %[idx]\n"
        "ds_read_b128 %[v01], v100 offset:8704\n"
        "v_exp_f32 v75, v75\n"
        "v_exp_f32 v76, v76\n"
        "v_exp_f32 v77, v77\n"
        "v_exp_f32 v78, v78\n"
        "v_exp_f32 v79, v79\n"
        "v_exp_f32 v80, v80\n"
        "v_exp_f32 v81, v81\n"
        "v_exp_f32 v82, v82\n"
        "v_smfmac_f32_32x32x32_bf16 %[oa], %[v11], v[72:79], %[idx]\n"
        "s_waitcnt lgkmcnt(2)\n"
        "s_sub_u32 s72, s72, 1\n"
        "v_exp_f32 v83, v83\n"
        "v_exp_f32 v84, v84\n"
        "v_exp_f32 v85, v85\n"
        "v_exp_f32 v86, v86\n"
        "v_mfma_f32_32x32x16_bf16 v[64:79], %[k0], %[qa], %[ma]\n"
        "s_cmp_lg_u32 s72, s79\n"
        "v_exp_f32 v87, v87\n"
        "v_exp_f32 v88, v88\n"
        "v_exp_f32 v89, v89\n"
        "v_exp_f32 v90, v90\n"
        "v_smfmac_f32_32x32x32_bf16 %[ob], %[v10], v[80:87], %[idx]\n"
        "s_waitcnt lgkmcnt(0)\n"
        "v_add_u32 v99, 8192, v99\n"
        "v_add_u32 v100, %[vinc], v100\n"
        "v_exp_f32 v91, v91\n"
        "v_exp_f32 v92, v92\n"
        "v_exp_f32 v93, v93\n"
        "v_exp_f32 v94, v94\n"
        "v_exp_f32 v95, v95\n"
        "1:\n"
        "v_exp_f32 v64, v64\n"
        "v_exp_f32 v65, v65\n"
        "v_exp_f32 v66, v66\n"
        "v_smfmac_f32_32x32x32_bf16 %[ob], %[v11], v[88:95], %[idx]\n"
        "ds_read_b128 %[k1], v99 offset:1024\n"
        "v_exp_f32 v67, v67\n"
        "v_exp_f32 v68, v68\n"
        "v_exp_f32 v69, v69\n"
        "v_exp_f32 v70, v70\n"
        "v_mfma_f32_32x32x16_bf16 v[80:95], %[k0], %[qb], %[mb]\n"
        "ds_read_b128 %[v10], v100 offset:1024\n"
        "v_exp_f32 v71, v71\n"
        "v_exp_f32 v72, v72\n"
        "v_exp_f32 v73, v73\n"
        "v_exp_f32 v74, v74\n"
        "v_smfmac_f32_32x32x32_bf16 %[oa], %[v00], v[64:71], %[idx]\n"
        "ds_read_b128 %[v11], v100 offset:1536\n"
        "v_exp_f32 v75, v75\n"
        "v_exp_f32 v76, v76\n"
        "v_exp_f32 v77, v77\n"
        "v_exp_f32 v78, v78\n"
        "v_exp_f32 v79, v79\n"
        "v_exp_f32 v80, v80\n"
        "v_exp_f32 v81, v81\n"
        "v_exp_f32 v82, v82\n"
        "v_smfmac_f32_32x32x32_bf16 %[oa], %[v01], v[72:79], %[idx]\n"
        "s_waitcnt lgkmcnt(2)\n"
        "s_add_u32 s76, s74, 65536\n"
        "v_exp_f32 v83, v83\n"
        "v_exp_f32 v84, v84\n"
        "v_exp_f32 v85, v85\n"
        "v_exp_f32 v86, v86\n"
        "v_mfma_f32_32x32x16_bf16 v[64:79], %[k1], %[qa], %[ma]\n"
        "s_addc_u32 s77, s75, 0\n"
        "v_exp_f32 v87, v87\n"
        "v_exp_f32 v88, v88\n"
        "v_exp_f32 v89, v89\n"
        "v_exp_f32 v90, v90\n"
        "v_smfmac_f32_32x32x32_bf16 %[ob], %[v00], v[80:87], %[idx]\n"
        "s_waitcnt lgkmcnt(0)\n"
        "v_exp_f32 v91, v91\n"
        "v_exp_f32 v92, v92\n"
        "v_exp_f32 v93, v93\n"
        "v_exp_f32 v94, v94\n"
        "v_exp_f32 v95, v95\n"
        "v_exp_f32 v64, v64\n"
        "v_exp_f32 v65, v65\n"
        "v_exp_f32 v66, v66\n"
        "v_smfmac_f32_32x32x32_bf16 %[ob], %[v01], v[88:95], %[idx]\n"
        "ds_read_b128 %[k0], v99 offset:2048\n"
        "s_cmp_eq_u32 s72, 5\n"
        "v_exp_f32 v67, v67\n"
        "v_exp_f32 v68, v68\n"
        "v_exp_f32 v69, v69\n"
        "v_exp_f32 v70, v70\n"
        "v_mfma_f32_32x32x16_bf16 v[80:95], %[k1], %[qb], %[mb]\n"
        "ds_read_b128 %[v00], v100 offset:2048\n"
        "v_exp_f32 v71, v71\n"
        "v_exp_f32 v72, v72\n"
        "v_exp_f32 v73, v73\n"
        "v_exp_f32 v74, v74\n"
        "v_smfmac_f32_32x32x32_bf16 %[oa], %[v10], v[64:71], %[idx]\n"
        "ds_read_b128 %[v01], v100 offset:2560\n"
        "s_cselect_b64 exec, -1, 0\n"
        "global_load_dword v96, %[vwr], %[swr] sc1\n"
        "s_mov_b64 exec, -1\n"
        "v_exp_f32 v75, v75\n"
        "v_exp_f32 v76, v76\n"
        "v_exp_f32 v77, v77\n"
        "v_exp_f32 v78, v78\n"
        "v_exp_f32 v79, v79\n"
        "v_exp_f32 v80, v80\n"
        "v_exp_f32 v81, v81\n"
        "v_exp_f32 v82, v82\n"
        "v_smfmac_f32_32x32x32_bf16 %[oa], %[v11], v[72:79], %[idx]\n"
        "s_waitcnt lgkmcnt(2)\n"
        "v_exp_f32 v83, v83\n"
        "v_exp_f32 v84, v84\n"
        "v_exp_f32 v85, v85\n"
        "v_exp_f32 v86, v86\n"
        "v_mfma_f32_32x32x16_bf16 v[64:79], %[k0], %[qa], %[ma]\n"
        "v_exp_f32 v87, v87\n"
        "v_exp_f32 v88, v88\n"
        "v_exp_f32 v89, v89\n"
        "v_exp_f32 v90, v90\n"
        "v_smfmac_f32_32x32x32_bf16 %[ob], %[v10], v[80:87], %[idx]\n"
        "s_waitcnt lgkmcnt(0)\n"
        "s_nop 0\n"
        "v_exp_f32 v91, v91\n"
        "v_exp_f32 v92, v92\n"
        "v_exp_f32 v93, v93\n"
        "v_exp_f32 v94, v94\n"
        "v_exp_f32 v95, v95\n"
        "v_exp_f32 v64, v64\n"
        "v_exp_f32 v65, v65\n"
        "v_exp_f32 v66, v66\n"
        "v_smfmac_f32_32x32x32_bf16 %[ob], %[v11], v[88:95], %[idx]\n"
        "ds_read_b128 %[k1], v99 offset:3072\n"
        "v_exp_f32 v67, v67\n"
        "v_exp_f32 v68, v68\n"
        "v_exp_f32 v69, v69\n"
        "v_exp_f32 v70, v70\n"
        "v_mfma_f32_32x32x16_bf16 v[80:95], %[k0], %[qb], %[mb]\n"
        "ds_read_b128 %[v10], v100 offset:3072\n"
        "v_exp_f32 v71, v71\n"
        "v_exp_f32 v72, v72\n"
        "v_exp_f32 v73, v73\n"
        "v_exp_f32 v74, v74\n"
        "v_smfmac_f32_32x32x32_bf16 %[oa], %[v00], v[64:71], %[idx]\n"
        "ds_read_b128 %[v11], v100 offset:3584\n"
        "v_exp_f32 v75, v75\n"
        "v_exp_f32 v76, v76\n"
        "v_exp_f32 v77, v77\n"
        "v_exp_f32 v78, v78\n"
        "v_exp_f32 v79, v79\n"
        "v_exp_f32 v80, v80\n"
        "v_exp_f32 v81, v81\n"
        "v_exp_f32 v82, v82\n"
        "v_smfmac_f32_32x32x32_bf16 %[oa], %[v01], v[72:79], %[idx]\n"
        "s_waitcnt lgkmcnt(2)\n"
        "v_exp_f32 v83, v83\n"
        "v_exp_f32 v84, v84\n"
        "v_exp_f32 v85, v85\n"
        "v_exp_f32 v86, v86\n"
        "v_mfma_f32_32x32x16_bf16 v[64:79], %[k1], %[qa], %[ma]\n"
        "v_exp_f32 v87, v87\n"
        "v_exp_f32 v88, v88\n"
        "v_exp_f32 v89, v89\n"
        "v_exp_f32 v90, v90\n"
        "v_smfmac_f32_32x32x32_bf16 %[ob], %[v00], v[80:87], %[idx]\n"
        "s_waitcnt lgkmcnt(0)\n"
        "s_nop 0\n"
        "v_exp_f32 v91, v91\n"
        "v_exp_f32 v92, v92\n"
        "v_exp_f32 v93, v93\n"
        "v_exp_f32 v94, v94\n"
        "v_exp_f32 v95, v95\n"
        "v_exp_f32 v64, v64\n"
        "v_exp_f32 v65, v65\n"
        "v_exp_f32 v66, v66\n"
        "v_smfmac_f32_32x32x32_bf16 %[ob], %[v01], v[88:95], %[idx]\n"
        "ds_read_b128 %[k0], v99 offset:4096\n"
        "v_exp_f32 v67, v67\n"
        "v_exp_f32 v68, v68\n"
        "v_exp_f32 v69, v69\n"
        "v_exp_f32 v70, v70\n"
        "v_mfma_f32_32x32x16_bf16 v[80:95], %[k1], %[qb], %[mb]\n"
        "ds_read_b128 %[v00], v100 offset:4096\n"
        "v_exp_f32 v71, v71\n"
        "v_exp_f32 v72, v72\n"
        "v_exp_f32 v73, v73\n"
        "v_exp_f32 v74, v74\n"
        "v_smfmac_f32_32x32x32_bf16 %[oa], %[v10], v[64:71], %[idx]\n"
        "ds_read_b128 %[v01], v100 offset:4608\n"
        "v_exp_f32 v75, v75\n"
        "v_exp_f32 v76, v76\n"
        "v_exp_f32 v77, v77\n"
        "v_exp_f32 v78, v78\n"
        "v_exp_f32 v79, v79\n"
        "v_exp_f32 v80, v80\n"
        "v_exp_f32 v81, v81\n"
        "v_exp_f32 v82, v82\n"
        "v_smfmac_f32_32x32x32_bf16 %[oa], %[v11], v[72:79], %[idx]\n"
        "s_waitcnt lgkmcnt(2)\n"
        "v_exp_f32 v83, v83\n"
        "v_exp_f32 v84, v84\n"
        "v_exp_f32 v85, v85\n"
        "v_exp_f32 v86, v86\n"
        "v_mfma_f32_32x32x16_bf16 v[64:79], %[k0], %[qa], %[ma]\n"
        "s_waitcnt vmcnt(0)\n"
        "v_exp_f32 v87, v87\n"
        "v_exp_f32 v88, v88\n"
        "v_exp_f32 v89, v89\n"
        "v_exp_f32 v90, v90\n"
        "v_smfmac_f32_32x32x32_bf16 %[ob], %[v10], v[80:87], %[idx]\n"
        "s_waitcnt lgkmcnt(0)\n"
        "s_barrier\n"
        "v_exp_f32 v91, v91\n"
        "v_exp_f32 v92, v92\n"
        "v_exp_f32 v93, v93\n"
        "v_exp_f32 v94, v94\n"
        "v_exp_f32 v95, v95\n"
        "v_exp_f32 v64, v64\n"
        "v_exp_f32 v65, v65\n"
        "v_exp_f32 v66, v66\n"
        "v_smfmac_f32_32x32x32_bf16 %[ob], %[v11], v[88:95], %[idx]\n"
        "ds_read_b128 %[k1], v99 offset:5120\n"
        "s_mov_b32 m0, s73\n"
        "v_exp_f32 v67, v67\n"
        "v_exp_f32 v68, v68\n"
        "v_exp_f32 v69, v69\n"
        "v_exp_f32 v70, v70\n"
        "v_mfma_f32_32x32x16_bf16 v[80:95], %[k0], %[qb], %[mb]\n"
        "ds_read_b128 %[v10], v100 offset:5120\n"
        "v_exp_f32 v71, v71\n"
        "v_exp_f32 v72, v72\n"
        "v_exp_f32 v73, v73\n"
        "v_exp_f32 v74, v74\n"
        "v_smfmac_f32_32x32x32_bf16 %[oa], %[v00], v[64:71], %[idx]\n"
        "ds_read_b128 %[v11], v100 offset:5632\n"
        "global_load_lds_dwordx4 %[voff], s[74:75] sc1\n"
        "v_exp_f32 v75, v75\n"
        "v_exp_f32 v76, v76\n"
        "v_exp_f32 v77, v77\n"
        "v_exp_f32 v78, v78\n"
        "v_exp_f32 v79, v79\n"
        "v_exp_f32 v80, v80\n"
        "v_exp_f32 v81, v81\n"
        "v_exp_f32 v82, v82\n"
        "v_smfmac_f32_32x32x32_bf16 %[oa], %[v01], v[72:79], %[idx]\n"
        "s_waitcnt lgkmcnt(2)\n"
        "v_exp_f32 v83, v83\n"
        "v_exp_f32 v84, v84\n"
        "v_exp_f32 v85, v85\n"
        "v_exp_f32 v86, v86\n"
        "v_mfma_f32_32x32x16_bf16 v[64:79], %[k1], %[qa], %[ma]\n"
        "v_exp_f32 v87, v87\n"
        "v_exp_f32 v88, v88\n"
        "v_exp_f32 v89, v89\n"
        "v_exp_f32 v90, v90\n"
        "v_smfmac_f32_32x32x32_bf16 %[ob], %[v00], v[80:87], %[idx]\n"
        "s_waitcnt lgkmcnt(0)\n"
        "global_load_lds_dwordx4 %[voff], s[74:75] offset:1024 sc1\n"
        "v_exp_f32 v91, v91\n"
        "v_exp_f32 v92, v92\n"
        "v_exp_f32 v93, v93\n"
        "v_exp_f32 v94, v94\n"
        "v_exp_f32 v95, v95\n"
        "v_exp_f32 v64, v64\n"
        "v_exp_f32 v65, v65\n"
        "v_exp_f32 v66, v66\n"
        "v_smfmac_f32_32x32x32_bf16 %[ob], %[v01], v[88:95], %[idx]\n"
        "ds_read_b128 %[k0], v99 offset:6144\n"
        "s_add_u32 m0, s73, 65536\n"
        "v_exp_f32 v67, v67\n"
        "v_exp_f32 v68, v68\n"
        "v_exp_f32 v69, v69\n"
        "v_exp_f32 v70, v70\n"
        "v_mfma_f32_32x32x16_bf16 v[80:95], %[k1], %[qb], %[mb]\n"
        "ds_read_b128 %[v00], v100 offset:6144\n"
        "v_exp_f32 v71, v71\n"
        "v_exp_f32 v72, v72\n"
        "v_exp_f32 v73, v73\n"
        "v_exp_f32 v74, v74\n"
        "v_smfmac_f32_32x32x32_bf16 %[oa], %[v10], v[64:71], %[idx]\n"
        "ds_read_b128 %[v01], v100 offset:6656\n"
        "global_load_lds_dwordx4 %[voff], s[76:77] sc1\n"
        "v_exp_f32 v75, v75\n"
        "v_exp_f32 v76, v76\n"
        "v_exp_f32 v77, v77\n"
        "v_exp_f32 v78, v78\n"
        "v_exp_f32 v79, v79\n"
        "v_exp_f32 v80, v80\n"
        "v_exp_f32 v81, v81\n"
        "v_exp_f32 v82, v82\n"
        "v_smfmac_f32_32x32x32_bf16 %[oa], %[v11], v[72:79], %[idx]\n"
        "s_waitcnt lgkmcnt(2)\n"
        "v_exp_f32 v83, v83\n"
        "v_exp_f32 v84, v84\n"
        "v_exp_f32 v85, v85\n"
        "v_exp_f32 v86, v86\n"
        "v_mfma_f32_32x32x16_bf16 v[64:79], %[k0], %[qa], %[ma]\n"
        "v_exp_f32 v87, v87\n"
        "v_exp_f32 v88, v88\n"
        "v_exp_f32 v89, v89\n"
        "v_exp_f32 v90, v90\n"
        "v_smfmac_f32_32x32x32_bf16 %[ob], %[v10], v[80:87], %[idx]\n"
        "s_waitcnt lgkmcnt(0)\n"
        "global_load_lds_dwordx4 %[voff], s[76:77] offset:1024 sc1\n"
        "v_exp_f32 v91, v91\n"
        "v_exp_f32 v92, v92\n"
        "v_exp_f32 v93, v93\n"
        "v_exp_f32 v94, v94\n"
        "v_exp_f32 v95, v95\n"
        "v_exp_f32 v64, v64\n"
        "v_exp_f32 v65, v65\n"
        "v_exp_f32 v66, v66\n"
        "v_smfmac_f32_32x32x32_bf16 %[ob], %[v11], v[88:95], %[idx]\n"
        "ds_read_b128 %[k1], v99 offset:7168\n"
        "s_add_u32 s73, s73, 8192\n"
        "v_exp_f32 v67, v67\n"
        "v_exp_f32 v68, v68\n"
        "v_exp_f32 v69, v69\n"
        "v_exp_f32 v70, v70\n"
        "v_mfma_f32_32x32x16_bf16 v[80:95], %[k0], %[qb], %[mb]\n"
        "ds_read_b128 %[v10], v100 offset:7168\n"
        "v_exp_f32 v71, v71\n"
        "v_exp_f32 v72, v72\n"
        "v_exp_f32 v73, v73\n"
        "v_exp_f32 v74, v74\n"
        "v_smfmac_f32_32x32x32_bf16 %[oa], %[v00], v[64:71], %[idx]\n"
        "ds_read_b128 %[v11], v100 offset:7680\n"
        "s_add_u32 s74, s74, 8192\n"
        "v_exp_f32 v75, v75\n"
        "v_exp_f32 v76, v76\n"
        "v_exp_f32 v77, v77\n"
        "v_exp_f32 v78, v78\n"
        "v_exp_f32 v79, v79\n"
        "v_exp_f32 v80, v80\n"
        "v_exp_f32 v81, v81\n"
        "v_exp_f32 v82, v82\n"
        "v_smfmac_f32_32x32x32_bf16 %[oa], %[v01], v[72:79], %[idx]\n"
        "s_waitcnt lgkmcnt(2)\n"
        "s_addc_u32 s75, s75, 0\n"
        "v_exp_f32 v83, v83\n"
        "v_exp_f32 v84, v84\n"
        "v_exp_f32 v85, v85\n"
        "v_exp_f32 v86, v86\n"
        "v_mfma_f32_32x32x16_bf16 v[64:79], %[k1], %[qa], %[ma]\n"
        "s_cmp_eq_u32 s72, s80\n"
        "v_exp_f32 v87, v87\n"
        "v_exp_f32 v88, v88\n"
        "v_exp_f32 v89, v89\n"
        "v_exp_f32 v90, v90\n"
        "v_smfmac_f32_32x32x32_bf16 %[ob], %[v00], v[80:87], %[idx]\n"
        "s_waitcnt lgkmcnt(0)\n"
        "s_cselect_b64 exec, 1, 0\n"
        "global_atomic_add v97, v98, %[swa] sc1\n"
        "s_mov_b64 exec, -1\n"
        "v_exp_f32 v91, v91\n"
        "v_exp_f32 v92, v92\n"
        "v_exp_f32 v93, v93\n"
        "v_exp_f32 v94, v94\n"
        "v_exp_f32 v95, v95\n"
        "v_exp_f32 v64, v64\n"
        "v_exp_f32 v65, v65\n"
        "v_exp_f32 v66, v66\n"
        "v_smfmac_f32_32x32x32_bf16 %[ob], %[v01], v[88:95], %[idx]\n"
        "ds_read_b128 %[k0], v99 offset:8192\n"
        "v_exp_f32 v67, v67\n"
        "v_exp_f32 v68, v68\n"
        "v_exp_f32 v69, v69\n"
        "v_exp_f32 v70, v70\n"
        "v_mfma_f32_32x32x16_bf16 v[80:95], %[k1], %[qb], %[mb]\n"
        "ds_read_b128 %[v00], v100 offset:8192\n"
        "v_exp_f32 v71, v71\n"
        "v_exp_f32 v72, v72\n"
        "v_exp_f32 v73, v73\n"
        "v_exp_f32 v74, v74\n"
        "v_smfmac_f32_32x32x32_bf16 %[oa], %[v10], v[64:71], %[idx]\n"
        "ds_read_b128 %[v01], v100 offset:8704\n"
        "v_exp_f32 v75, v75\n"
        "v_exp_f32 v76, v76\n"
        "v_exp_f32 v77, v77\n"
        "v_exp_f32 v78, v78\n"
        "v_exp_f32 v79, v79\n"
        "v_exp_f32 v80, v80\n"
        "v_exp_f32 v81, v81\n"
        "v_exp_f32 v82, v82\n"
        "v_smfmac_f32_32x32x32_bf16 %[oa], %[v11], v[72:79], %[idx]\n"
        "s_waitcnt lgkmcnt(2)\n"
        "s_sub_u32 s72, s72, 1\n"
        "v_exp_f32 v83, v83\n"
        "v_exp_f32 v84, v84\n"
        "v_exp_f32 v85, v85\n"
        "v_exp_f32 v86, v86\n"
        "v_mfma_f32_32x32x16_bf16 v[64:79], %[k0], %[qa], %[ma]\n"
        "s_cmp_lg_u32 s72, s79\n"
        "v_exp_f32 v87, v87\n"
        "v_exp_f32 v88, v88\n"
        "v_exp_f32 v89, v89\n"
        "v_exp_f32 v90, v90\n"
        "v_smfmac_f32_32x32x32_bf16 %[ob], %[v10], v[80:87], %[idx]\n"
        "s_waitcnt lgkmcnt(0)\n"
        "v_add_u32 v99, 8192, v99\n"
        "v_add_u32 v100, %[vinc], v100\n"
        "v_exp_f32 v91, v91\n"
        "v_exp_f32 v92, v92\n"
        "v_exp_f32 v93, v93\n"
        "v_exp_f32 v94, v94\n"
        "v_exp_f32 v95, v95\n"
        "s_cbranch_scc1 1b\n"
        "s_mov_b32 s81, 0x100000\n"
        "2:\n"
        "v_cmp_gt_u32 vcc, 32, v96\n"
        "s_nop 4\n"
        "s_cbranch_vccz 3f\n"
        "global_load_dword v96, %[vwr], %[swr] sc1\n"
        "s_waitcnt vmcnt(0)\n"
        "s_sub_u32 s81, s81, 1\n"
        "s_cbranch_scc0 2b\n"
        "3:\n"
        "s_cmp_lg_u32 %[lay1], 0\n"
        "s_cbranch_scc1 8f\n"
        "v_exp_f32 v64, v64\n"
        "v_exp_f32 v65, v65\n"
        "v_exp_f32 v66, v66\n"
        "v_smfmac_f32_32x32x32_bf16 %[ob], %[v11], v[88:95], %[idx]\n"
        "ds_read_b128 %[k1], v99 offset:1024\n"
        "global_load_dwordx4 a[128:131], %[voff], s[92:93] offset:-4096 sc1\n"
        "v_exp_f32 v67, v67\n"
        "v_exp_f32 v68, v68\n"
        "v_exp_f32 v69, v69\n"
        "v_exp_f32 v70, v70\n"
        "v_mfma_f32_32x32x16_bf16 v[80:95], %[k0], %[qb], %[mb]\n"
        "ds_read_b128 %[v10], v100 offset:1024\n"
        "global_load_dwordx4 a[152:155], %[voff], s[92:93] offset:-1024 sc1\n"
        "v_exp_f32 v71, v71\n"
        "v_exp_f32 v72, v72\n"
        "v_exp_f32 v73, v73\n"
        "v_exp_f32 v74, v74\n"
        "v_smfmac_f32_32x32x32_bf16 %[oa], %[v00], v[64:71], %[idx]\n"
        "ds_read_b128 %[v11], v100 offset:1536\n"
        "global_load_dwordx4 a[136:139], %[voff], s[92:93] offset:-3072 sc1\n"
        "v_exp_f32 v75, v75\n"
        "v_exp_f32 v76, v76\n"
        "v_exp_f32 v77, v77\n"
        "v_exp_f32 v78, v78\n"
        "v_exp_f32 v79, v79\n"
        "v_exp_f32 v80, v80\n"
        "v_exp_f32 v81, v81\n"
        "v_exp_f32 v82, v82\n"
        "v_smfmac_f32_32x32x32_bf16 %[oa], %[v01], v[72:79], %[idx]\n"
        "s_waitcnt lgkmcnt(2)\n"
        "s_add_u32 s76, s74, 65536\n"
        "v_exp_f32 v83, v83\n"
        "v_exp_f32 v84, v84\n"
        "v_exp_f32 v85, v85\n"
        "v_exp_f32 v86, v86\n"
        "v_mfma_f32_32x32x16_bf16 v[64:79], %[k1], %[qa], %[ma]\n"
        "s_addc_u32 s77, s75, 0\n"
        "v_exp_f32 v87, v87\n"
        "v_exp_f32 v88, v88\n"
        "v_exp_f32 v89, v89\n"
        "v_exp_f32 v90, v90\n"
        "v_smfmac_f32_32x32x32_bf16 %[ob], %[v00], v[80:87], %[idx]\n"
        "s_waitcnt lgkmcnt(0)\n"
        "global_load_dwordx4 a[144:147], %[voff], s[92:93] offset:-2048 sc1\n"
        "v_exp_f32 v91, v91\n"
        "v_exp_f32 v92, v92\n"
        "v_exp_f32 v93, v93\n"
        "v_exp_f32 v94, v94\n"
        "v_exp_f32 v95, v95\n"
        "v_exp_f32 v64, v64\n"
        "v_exp_f32 v65, v65\n"
        "v_exp_f32 v66, v66\n"
        "v_smfmac_f32_32x32x32_bf16 %[ob], %[v01], v[88:95], %[idx]\n"
        "ds_read_b128 %[k0], v99 offset:2048\n"
        "global_load_dwordx4 a[160:163], %[voff], s[92:93] offset:0 sc1\n"
        "v_exp_f32 v67, v67\n"
        "v_exp_f32 v68, v68\n"
        "v_exp_f32 v69, v69\n"
        "v_exp_f32 v70, v70\n"
        "v_mfma_f32_32x32x16_bf16 v[80:95], %[k1], %[qb], %[mb]\n"
        "ds_read_b128 %[v00], v100 offset:2048\n"
        "global_load_dwordx4 a[140:143], %[voff], s[96:97] offset:-3072 sc1\n"
        "v_exp_f32 v71, v71\n"
        "v_exp_f32 v72, v72\n"
        "v_exp_f32 v73, v73\n"
        "v_exp_f32 v74, v74\n"
        "v_smfmac_f32_32x32x32_bf16 %[oa], %[v10], v[64:71], %[idx]\n"
        "ds_read_b128 %[v01], v100 offset:2560\n"
        "global_load_dwordx4 a[168:171], %[voff], s[92:93] offset:1024 sc1\n"
        "v_exp_f32 v75, v75\n"
        "v_exp_f32 v76, v76\n"
        "v_exp_f32 v77, v77\n"
        "v_exp_f32 v78, v78\n"
        "v_exp_f32 v79, v79\n"
        "v_exp_f32 v80, v80\n"
        "v_exp_f32 v81, v81\n"
        "v_exp_f32 v82, v82\n"
        "v_smfmac_f32_32x32x32_bf16 %[oa], %[v11], v[72:79], %[idx]\n"
        "s_waitcnt lgkmcnt(2)\n"
        "global_load_dwordx4 a[176:179], %[voff], s[92:93] offset:2048 sc1\n"
        "v_exp_f32 v83, v83\n"
        "v_exp_f32 v84, v84\n"
        "v_exp_f32 v85, v85\n"
        "v_exp_f32 v86, v86\n"
        "v_mfma_f32_32x32x16_bf16 v[64:79], %[k0], %[qa], %[ma]\n"
        "global_load_dwordx4 a[184:187], %[voff], s[92:93] offset:3072 sc1\n"
        "v_exp_f32 v87, v87\n"
        "v_exp_f32 v88, v88\n"
        "v_exp_f32 v89, v89\n"
        "v_exp_f32 v90, v90\n"
        "v_smfmac_f32_32x32x32_bf16 %[ob], %[v10], v[80:87], %[idx]\n"
        "s_waitcnt lgkmcnt(0)\n"
        "global_load_dwordx4 a[132:135], %[voff], s[96:97] offset:-4096 sc1\n"
        "v_exp_f32 v91, v91\n"
        "v_exp_f32 v92, v92\n"
        "v_exp_f32 v93, v93\n"
        "v_exp_f32 v94, v94\n"
        "v_exp_f32 v95, v95\n"
        "v_exp_f32 v64, v64\n"
        "v_exp_f32 v65, v65\n"
        "v_exp_f32 v66, v66\n"
        "v_smfmac_f32_32x32x32_bf16 %[ob], %[v11], v[88:95], %[idx]\n"
        "ds_read_b128 %[k1], v99 offset:3072\n"
        "global_load_dwordx4 a[148:151], %[voff], s[96:97] offset:-2048 sc1\n"
        "v_exp_f32 v67, v67\n"
        "v_exp_f32 v68, v68\n"
        "v_exp_f32 v69, v69\n"
        "v_exp_f32 v70, v70\n"
        "v_mfma_f32_32x32x16_bf16 v[80:95], %[k0], %[qb], %[mb]\n"
        "ds_read_b128 %[v10], v100 offset:3072\n"
        "global_load_dwordx4 a[188:191], %[voff], s[96:97] offset:3072 sc1\n"
        "v_exp_f32 v71, v71\n"
        "v_exp_f32 v72, v72\n"
        "v_exp_f32 v73, v73\n"
        "v_exp_f32 v74, v74\n"
        "v_smfmac_f32_32x32x32_bf16 %[oa], %[v00], v[64:71], %[idx]\n"
        "ds_read_b128 %[v11], v100 offset:3584\n"
        "global_load_dwordx4 a[156:159], %[voff], s[96:97] offset:-1024 sc1\n"
        "v_exp_f32 v75, v75\n"
        "v_exp_f32 v76, v76\n"
        "v_exp_f32 v77, v77\n"
        "v_exp_f32 v78, v78\n"
        "v_exp_f32 v79, v79\n"
        "v_exp_f32 v80, v80\n"
        "v_exp_f32 v81, v81\n"
        "v_exp_f32 v82, v82\n"
        "v_smfmac_f32_32x32x32_bf16 %[oa], %[v01], v[72:79], %[idx]\n"
        "s_waitcnt lgkmcnt(2)\n"
        "global_load_dwordx4 a[164:167], %[voff], s[96:97] offset:0 sc1\n"
        "v_exp_f32 v83, v83\n"
        "v_exp_f32 v84, v84\n"
        "v_exp_f32 v85, v85\n"
        "v_exp_f32 v86, v86\n"
        "v_mfma_f32_32x32x16_bf16 v[64:79], %[k1], %[qa], %[ma]\n"
        "global_load_dwordx4 a[172:175], %[voff], s[96:97] offset:1024 sc1\n"
        "v_exp_f32 v87, v87\n"
        "v_exp_f32 v88, v88\n"
        "v_exp_f32 v89, v89\n"
        "v_exp_f32 v90, v90\n"
        "v_smfmac_f32_32x32x32_bf16 %[ob], %[v00], v[80:87], %[idx]\n"
        "s_waitcnt lgkmcnt(0)\n"
        "global_load_dwordx4 a[180:183], %[voff], s[96:97] offset:2048 sc1\n"
        "v_exp_f32 v91, v91\n"
        "v_exp_f32 v92, v92\n"
        "v_exp_f32 v93, v93\n"
        "v_exp_f32 v94, v94\n"
        "v_exp_f32 v95, v95\n"
        "v_exp_f32 v64, v64\n"
        "v_exp_f32 v65, v65\n"
        "v_exp_f32 v66, v66\n"
        "v_smfmac_f32_32x32x32_bf16 %[ob], %[v01], v[88:95], %[idx]\n"
        "ds_read_b128 %[k0], v99 offset:4096\n"
        "global_load_dwordx4 a[192:195], %[voff], s[94:95] offset:-4096 sc1\n"
        "v_exp_f32 v67, v67\n"
        "v_exp_f32 v68, v68\n"
        "v_exp_f32 v69, v69\n"
        "v_exp_f32 v70, v70\n"
        "v_mfma_f32_32x32x16_bf16 v[80:95], %[k1], %[qb], %[mb]\n"
        "ds_read_b128 %[v00], v100 offset:4096\n"
        "global_load_dwordx4 a[216:219], %[voff], s[94:95] offset:-1024 sc1\n"
        "v_exp_f32 v71, v71\n"
        "v_exp_f32 v72, v72\n"
        "v_exp_f32 v73, v73\n"
        "v_exp_f32 v74, v74\n"
        "v_smfmac_f32_32x32x32_bf16 %[oa], %[v10], v[64:71], %[idx]\n"
        "ds_read_b128 %[v01], v100 offset:4608\n"
        "global_load_dwordx4 a[200:203], %[voff], s[94:95] offset:-3072 sc1\n"
        "v_exp_f32 v75, v75\n"
        "v_exp_f32 v76, v76\n"
        "v_exp_f32 v77, v77\n"
        "v_exp_f32 v78, v78\n"
        "v_exp_f32 v79, v79\n"
        "v_exp_f32 v80, v80\n"
        "v_exp_f32 v81, v81\n"
        "v_exp_f32 v82, v82\n"
        "v_smfmac_f32_32x32x32_bf16 %[oa], %[v11], v[72:79], %[idx]\n"
        "s_waitcnt lgkmcnt(2)\n"
        "global_load_dwordx4 a[208:211], %[voff], s[94:95] offset:-2048 sc1\n"
        "v_exp_f32 v83, v83\n"
        "v_exp_f32 v84, v84\n"
        "v_exp_f32 v85, v85\n"
        "v_exp_f32 v86, v86\n"
        "v_mfma_f32_32x32x16_bf16 v[64:79], %[k0], %[qa], %[ma]\n"
        "s_waitcnt vmcnt(20)\n"
        "v_exp_f32 v87, v87\n"
        "v_exp_f32 v88, v88\n"
        "v_exp_f32 v89, v89\n"
        "v_exp_f32 v90, v90\n"
        "v_smfmac_f32_32x32x32_bf16 %[ob], %[v10], v[80:87], %[idx]\n"
        "s_waitcnt lgkmcnt(0)\n"
        "s_barrier\n"
        "v_exp_f32 v91, v91\n"
        "v_exp_f32 v92, v92\n"
        "v_exp_f32 v93, v93\n"
        "v_exp_f32 v94, v94\n"
        "v_exp_f32 v95, v95\n"
        "v_exp_f32 v64, v64\n"
        "v_exp_f32 v65, v65\n"
        "v_exp_f32 v66, v66\n"
        "v_smfmac_f32_32x32x32_bf16 %[ob], %[v11], v[88:95], %[idx]\n"
        "ds_read_b128 %[k1], v99 offset:5120\n"
        "s_mov_b32 m0, s73\n"
        "v_exp_f32 v67, v67\n"
        "v_exp_f32 v68, v68\n"
        "v_exp_f32 v69, v69\n"
        "v_exp_f32 v70, v70\n"
        "v_mfma_f32_32x32x16_bf16 v[80:95], %[k0], %[qb], %[mb]\n"
        "ds_read_b128 %[v10], v100 offset:5120\n"
        "global_load_dwordx4 a[240:243], %[voff], s[94:95] offset:2048 sc1\n"
        "v_exp_f32 v71, v71\n"
        "v_exp_f32 v72, v72\n"
        "v_exp_f32 v73, v73\n"
        "v_exp_f32 v74, v74\n"
        "v_smfmac_f32_32x32x32_bf16 %[oa], %[v00], v[64:71], %[idx]\n"
        "ds_read_b128 %[v11], v100 offset:5632\n"
        "global_load_lds_dwordx4 %[voff], s[74:75] sc1\n"
        "v_exp_f32 v75, v75\n"
        "v_exp_f32 v76, v76\n"
        "v_exp_f32 v77, v77\n"
        "v_exp_f32 v78, v78\n"
        "v_exp_f32 v79, v79\n"
        "v_exp_f32 v80, v80\n"
        "v_exp_f32 v81, v81\n"
        "v_exp_f32 v82, v82\n"
        "v_smfmac_f32_32x32x32_bf16 %[oa], %[v01], v[72:79], %[idx]\n"
        "s_waitcnt lgkmcnt(2)\n"
        "global_load_dwordx4 a[224:227], %[voff], s[94:95] offset:0 sc1\n"
        "v_exp_f32 v83, v83\n"
        "v_exp_f32 v84, v84\n"
        "v_exp_f32 v85, v85\n"
        "v_exp_f32 v86, v86\n"
        "v_mfma_f32_32x32x16_bf16 v[64:79], %[k1], %[qa], %[ma]\n"
        "global_load_dwordx4 a[232:235], %[voff], s[94:95] offset:1024 sc1\n"
        "v_exp_f32 v87, v87\n"
        "v_exp_f32 v88, v88\n"
        "v_exp_f32 v89, v89\n"
        "v_exp_f32 v90, v90\n"
        "v_smfmac_f32_32x32x32_bf16 %[ob], %[v00], v[80:87], %[idx]\n"
        "s_waitcnt lgkmcnt(0)\n"
        "global_load_lds_dwordx4 %[voff], s[74:75] offset:1024 sc1\n"
        "v_exp_f32 v91, v91\n"
        "v_exp_f32 v92, v92\n"
        "v_exp_f32 v93, v93\n"
        "v_exp_f32 v94, v94\n"
        "v_exp_f32 v95, v95\n"
        "v_exp_f32 v64, v64\n"
        "v_exp_f32 v65, v65\n"
        "v_exp_f32 v66, v66\n"
        "v_smfmac_f32_32x32x32_bf16 %[ob], %[v01], v[88:95], %[idx]\n"
        "ds_read_b128 %[k0], v99 offset:6144\n"
        "s_add_u32 m0, s73, 65536\n"
        "v_exp_f32 v67, v67\n"
        "v_exp_f32 v68, v68\n"
        "v_exp_f32 v69, v69\n"
        "v_exp_f32 v70, v70\n"
        "v_mfma_f32_32x32x16_bf16 v[80:95], %[k1], %[qb], %[mb]\n"
        "ds_read_b128 %[v00], v100 offset:6144\n"
        "global_load_dwordx4 a[204:207], %[voff], s[98:99] offset:-3072 sc1\n"
        "v_exp_f32 v71, v71\n"
        "v_exp_f32 v72, v72\n"
        "v_exp_f32 v73, v73\n"
        "v_exp_f32 v74, v74\n"
        "v_smfmac_f32_32x32x32_bf16 %[oa], %[v10], v[64:71], %[idx]\n"
        "ds_read_b128 %[v01], v100 offset:6656\n"
        "global_load_lds_dwordx4 %[voff], s[76:77] sc1\n"
        "v_exp_f32 v75, v75\n"
        "v_exp_f32 v76, v76\n"
        "v_exp_f32 v77, v77\n"
        "v_exp_f32 v78, v78\n"
        "v_exp_f32 v79, v79\n"
        "v_exp_f32 v80, v80\n"
        "v_exp_f32 v81, v81\n"
        "v_exp_f32 v82, v82\n"
        "v_smfmac_f32_32x32x32_bf16 %[oa], %[v11], v[72:79], %[idx]\n"
        "s_waitcnt lgkmcnt(2)\n"
        "global_load_dwordx4 a[248:251], %[voff], s[94:95] offset:3072 sc1\n"
        "v_exp_f32 v83, v83\n"
        "v_exp_f32 v84, v84\n"
        "v_exp_f32 v85, v85\n"
        "v_exp_f32 v86, v86\n"
        "v_mfma_f32_32x32x16_bf16 v[64:79], %[k0], %[qa], %[ma]\n"
        "global_load_dwordx4 a[196:199], %[voff], s[98:99] offset:-4096 sc1\n"
        "v_exp_f32 v87, v87\n"
        "v_exp_f32 v88, v88\n"
        "v_exp_f32 v89, v89\n"
        "v_exp_f32 v90, v90\n"
        "v_smfmac_f32_32x32x32_bf16 %[ob], %[v10], v[80:87], %[idx]\n"
        "s_waitcnt lgkmcnt(0)\n"
        "global_load_lds_dwordx4 %[voff], s[76:77] offset:1024 sc1\n"
        "v_exp_f32 v91, v91\n"
        "v_exp_f32 v92, v92\n"
        "v_exp_f32 v93, v93\n"
        "v_exp_f32 v94, v94\n"
        "v_exp_f32 v95, v95\n"
        "v_exp_f32 v64, v64\n"
        "v_exp_f32 v65, v65\n"
        "v_exp_f32 v66, v66\n"
        "v_smfmac_f32_32x32x32_bf16 %[ob], %[v11], v[88:95], %[idx]\n"
        "ds_read_b128 %[k1], v99 offset:7168\n"
        "s_add_u32 s73, s73, 8192\n"
        "v_exp_f32 v67, v67\n"
        "v_exp_f32 v68, v68\n"
        "v_exp_f32 v69, v69\n"
        "v_exp_f32 v70, v70\n"
        "v_mfma_f32_32x32x16_bf16 v[80:95], %[k0], %[qb], %[mb]\n"
        "ds_read_b128 %[v10], v100 offset:7168\n"
        "global_load_dwordx4 a[228:231], %[voff], s[98:99] offset:0 sc1\n"
        "v_exp_f32 v71, v71\n"
        "v_exp_f32 v72, v72\n"
        "v_exp_f32 v73, v73\n"
        "v_exp_f32 v74, v74\n"
        "v_smfmac_f32_32x32x32_bf16 %[oa], %[v00], v[64:71], %[idx]\n"
        "ds_read_b128 %[v11], v100 offset:7680\n"
        "s_add_u32 s74, s74, 8192\n"
        "v_exp_f32 v75, v75\n"
        "v_exp_f32 v76, v76\n"
        "v_exp_f32 v77, v77\n"
        "v_exp_f32 v78, v78\n"
        "v_exp_f32 v79, v79\n"
        "v_exp_f32 v80, v80\n"
        "v_exp_f32 v81, v81\n"
        "v_exp_f32 v82, v82\n"
        "v_smfmac_f32_32x32x32_bf16 %[oa], %[v01], v[72:79], %[idx]\n"
        "s_waitcnt lgkmcnt(2)\n"
        "s_addc_u32 s75, s75, 0\n"
        "v_exp_f32 v83, v83\n"
        "v_exp_f32 v84, v84\n"
        "v_exp_f32 v85, v85\n"
        "v_exp_f32 v86, v86\n"
        "v_mfma_f32_32x32x16_bf16 v[64:79], %[k1], %[qa], %[ma]\n"
        "global_load_dwordx4 a[212:215], %[voff], s[98:99] offset:-2048 sc1\n"
        "v_exp_f32 v87, v87\n"
        "v_exp_f32 v88, v88\n"
        "v_exp_f32 v89, v89\n"
        "v_exp_f32 v90, v90\n"
        "v_smfmac_f32_32x32x32_bf16 %[ob], %[v00], v[80:87], %[idx]\n"
        "s_waitcnt lgkmcnt(0)\n"
        "global_load_dwordx4 a[220:223], %[voff], s[98:99] offset:-1024 sc1\n"
        "v_exp_f32 v91, v91\n"
        "v_exp_f32 v92, v92\n"
        "v_exp_f32 v93, v93\n"
        "v_exp_f32 v94, v94\n"
        "v_exp_f32 v95, v95\n"
        "v_exp_f32 v64, v64\n"
        "v_exp_f32 v65, v65\n"
        "v_exp_f32 v66, v66\n"
        "v_smfmac_f32_32x32x32_bf16 %[ob], %[v01], v[88:95], %[idx]\n"
        "ds_read_b128 %[k0], v99 offset:8192\n"
        "global_load_dwordx4 a[236:239], %[voff], s[98:99] offset:1024 sc1\n"
        "v_exp_f32 v67, v67\n"
        "v_exp_f32 v68, v68\n"
        "v_exp_f32 v69, v69\n"
        "v_exp_f32 v70, v70\n"
        "v_mfma_f32_32x32x16_bf16 v[80:95], %[k1], %[qb], %[mb]\n"
        "ds_read_b128 %[v00], v100 offset:8192\n"
        "v_exp_f32 v71, v71\n"
        "v_exp_f32 v72, v72\n"
        "v_exp_f32 v73, v73\n"
        "v_exp_f32 v74, v74\n"
        "v_smfmac_f32_32x32x32_bf16 %[oa], %[v10], v[64:71], %[idx]\n"
        "ds_read_b128 %[v01], v100 offset:8704\n"
        "global_load_dwordx4 a[244:247], %[voff], s[98:99] offset:2048 sc1\n"
        "v_exp_f32 v75, v75\n"
        "v_exp_f32 v76, v76\n"
        "v_exp_f32 v77, v77\n"
        "v_exp_f32 v78, v78\n"
        "v_exp_f32 v79, v79\n"
        "v_exp_f32 v80, v80\n"
        "v_exp_f32 v81, v81\n"
        "v_exp_f32 v82, v82\n"
        "v_smfmac_f32_32x32x32_bf16 %[oa], %[v11], v[72:79], %[idx]\n"
        "s_waitcnt lgkmcnt(2)\n"
        "global_load_dwordx4 a[252:255], %[voff], s[98:99] offset:3072 sc1\n"
        "v_exp_f32 v83, v83\n"
        "v_exp_f32 v84, v84\n"
        "v_exp_f32 v85, v85\n"
        "v_exp_f32 v86, v86\n"
        "v_mfma_f32_32x32x16_bf16 v[64:79], %[k0], %[qa], %[ma]\n"
        "v_exp_f32 v87, v87\n"
        "v_exp_f32 v88, v88\n"
        "v_exp_f32 v89, v89\n"
        "v_exp_f32 v90, v90\n"
        "v_smfmac_f32_32x32x32_bf16 %[ob], %[v10], v[80:87], %[idx]\n"
        "s_waitcnt lgkmcnt(0)\n"
        "v_add_u32 v99, 8192, v99\n"
        "v_add_u32 v100, %[vinc], v100\n"
        "v_exp_f32 v91, v91\n"
        "v_exp_f32 v92, v92\n"
        "v_exp_f32 v93, v93\n"
        "v_exp_f32 v94, v94\n"
        "v_exp_f32 v95, v95\n"
        "8:\n"
        "v_exp_f32 v64, v64\n"
        "v_exp_f32 v65, v65\n"
        "v_exp_f32 v66, v66\n"
        "v_smfmac_f32_32x32x32_bf16 %[ob], %[v11], v[88:95], %[idx]\n"
        "ds_read_b128 %[k1], v99 offset:1024\n"
        "s_add_u32 m0, %[m13], 0\n"
        "s_add_u32 s82, %[w13l], 0\n"
        "s_addc_u32 s83, %[w13h], 0\n"
        "global_load_lds_dwordx4 %[voff], s[82:83] offset:0 sc1\n"
        "v_exp_f32 v67, v67\n"
        "v_exp_f32 v68, v68\n"
        "v_exp_f32 v69, v69\n"
        "v_exp_f32 v70, v70\n"
        "v_mfma_f32_32x32x16_bf16 v[80:95], %[k0], %[qb], %[mb]\n"
        "ds_read_b128 %[v10], v100 offset:1024\n"
        "global_load_lds_dwordx4 %[voff], s[82:83] offset:1024 sc1\n"
        "v_exp_f32 v71, v71\n"
        "v_exp_f32 v72, v72\n"
        "v_exp_f32 v73, v73\n"
        "v_exp_f32 v74, v74\n"
        "v_smfmac_f32_32x32x32_bf16 %[oa], %[v00], v[64:71], %[idx]\n"
        "ds_read_b128 %[v11], v100 offset:1536\n"
        "global_load_lds_dwordx4 %[voff], s[82:83] offset:2048 sc1\n"
        "v_exp_f32 v75, v75\n"
        "v_exp_f32 v76, v76\n"
        "v_exp_f32 v77, v77\n"
        "v_exp_f32 v78, v78\n"
        "v_exp_f32 v79, v79\n"
        "v_exp_f32 v80, v80\n"
        "v_exp_f32 v81, v81\n"
        "v_exp_f32 v82, v82\n"
        "v_smfmac_f32_32x32x32_bf16 %[oa], %[v01], v[72:79], %[idx]\n"
        "s_waitcnt lgkmcnt(2)\n"
        "s_add_u32 s76, s74, 65536\n"
        "v_exp_f32 v83, v83\n"
        "v_exp_f32 v84, v84\n"
        "v_exp_f32 v85, v85\n"
        "v_exp_f32 v86, v86\n"
        "v_mfma_f32_32x32x16_bf16 v[64:79], %[k1], %[qa], %[ma]\n"
        "s_addc_u32 s77, s75, 0\n"
        "v_exp_f32 v87, v87\n"
        "v_exp_f32 v88, v88\n"
        "v_exp_f32 v89, v89\n"
        "v_exp_f32 v90, v90\n"
        "v_smfmac_f32_32x32x32_bf16 %[ob], %[v00], v[80:87], %[idx]\n"
        "s_waitcnt lgkmcnt(0)\n"
        "global_load_lds_dwordx4 %[voff], s[82:83] offset:3072 sc1\n"
        "v_exp_f32 v91, v91\n"
        "v_exp_f32 v92, v92\n"
        "v_exp_f32 v93, v93\n"
        "v_exp_f32 v94, v94\n"
        "v_exp_f32 v95, v95\n"
        "v_exp_f32 v64, v64\n"
        "v_exp_f32 v65, v65\n"
        "v_exp_f32 v66, v66\n"
        "v_smfmac_f32_32x32x32_bf16 %[ob], %[v01], v[88:95], %[idx]\n"
        "ds_read_b128 %[k0], v99 offset:2048\n"
        "global_load_dwordx4 a[0:3], %[voff], s[84:85] offset:-4096 sc1\n"
        "v_exp_f32 v67, v67\n"
        "v_exp_f32 v68, v68\n"
        "v_exp_f32 v69, v69\n"
        "v_exp_f32 v70, v70\n"
        "v_mfma_f32_32x32x16_bf16 v[80:95], %[k1], %[qb], %[mb]\n"
        "ds_read_b128 %[v00], v100 offset:2048\n"
        "v_exp_f32 v71, v71\n"
        "v_exp_f32 v72, v72\n"
        "v_exp_f32 v73, v73\n"
        "v_exp_f32 v74, v74\n"
        "v_smfmac_f32_32x32x32_bf16 %[oa], %[v10], v[64:71], %[idx]\n"
        "ds_read_b128 %[v01], v100 offset:2560\n"
        "global_load_dwordx4 a[4:7], %[voff], s[84:85] offset:-3072 sc1\n"
        "v_exp_f32 v75, v75\n"
        "v_exp_f32 v76, v76\n"
        "v_exp_f32 v77, v77\n"
        "v_exp_f32 v78, v78\n"
        "v_exp_f32 v79, v79\n"
        "v_exp_f32 v80, v80\n"
        "v_exp_f32 v81, v81\n"
        "v_exp_f32 v82, v82\n"
        "v_smfmac_f32_32x32x32_bf16 %[oa], %[v11], v[72:79], %[idx]\n"
        "s_waitcnt lgkmcnt(2)\n"
        "global_load_dwordx4 a[8:11], %[voff], s[84:85] offset:-2048 sc1\n"
        "v_exp_f32 v83, v83\n"
        "v_exp_f32 v84, v84\n"
        "v_exp_f32 v85, v85\n"
        "v_exp_f32 v86, v86\n"
        "v_mfma_f32_32x32x16_bf16 v[64:79], %[k0], %[qa], %[ma]\n"
        "global_load_dwordx4 a[12:15], %[voff], s[84:85] offset:-1024 sc1\n"
        "v_exp_f32 v87, v87\n"
        "v_exp_f32 v88, v88\n"
        "v_exp_f32 v89, v89\n"
        "v_exp_f32 v90, v90\n"
        "v_smfmac_f32_32x32x32_bf16 %[ob], %[v10], v[80:87], %[idx]\n"
        "s_waitcnt lgkmcnt(0)\n"
        "v_exp_f32 v91, v91\n"
        "v_exp_f32 v92, v92\n"
        "v_exp_f32 v93, v93\n"
        "v_exp_f32 v94, v94\n"
        "v_exp_f32 v95, v95\n"
        "v_exp_f32 v64, v64\n"
        "v_exp_f32 v65, v65\n"
        "v_exp_f32 v66, v66\n"
        "v_smfmac_f32_32x32x32_bf16 %[ob], %[v11], v[88:95], %[idx]\n"
        "ds_read_b128 %[k1], v99 offset:3072\n"
        "s_add_u32 m0, %[m13], 4096\n"
        "s_add_u32 s82, %[w13l], 4096\n"
        "s_addc_u32 s83, %[w13h], 0\n"
        "global_load_lds_dwordx4 %[voff], s[82:83] offset:0 sc1\n"
        "v_exp_f32 v67, v67\n"
        "v_exp_f32 v68, v68\n"
        "v_exp_f32 v69, v69\n"
        "v_exp_f32 v70, v70\n"
        "v_mfma_f32_32x32x16_bf16 v[80:95], %[k0], %[qb], %[mb]\n"
        "ds_read_b128 %[v10], v100 offset:3072\n"
        "global_load_lds_dwordx4 %[voff], s[82:83] offset:1024 sc1\n"
        "v_exp_f32 v71, v71\n"
        "v_exp_f32 v72, v72\n"
        "v_exp_f32 v73, v73\n"
        "v_exp_f32 v74, v74\n"
        "v_smfmac_f32_32x32x32_bf16 %[oa], %[v00], v[64:71], %[idx]\n"
        "ds_read_b128 %[v11], v100 offset:3584\n"
        "global_load_lds_dwordx4 %[voff], s[82:83] offset:2048 sc1\n"
        "v_exp_f32 v75, v75\n"
        "v_exp_f32 v76, v76\n"
        "v_exp_f32 v77, v77\n"
        "v_exp_f32 v78, v78\n"
        "v_exp_f32 v79, v79\n"
        "v_exp_f32 v80, v80\n"
        "v_exp_f32 v81, v81\n"
        "v_exp_f32 v82, v82\n"
        "v_smfmac_f32_32x32x32_bf16 %[oa], %[v01], v[72:79], %[idx]\n"
        "s_waitcnt lgkmcnt(2)\n"
        "global_load_lds_dwordx4 %[voff], s[82:83] offset:3072 sc1\n"
        "v_exp_f32 v83, v83\n"
        "v_exp_f32 v84, v84\n"
        "v_exp_f32 v85, v85\n"
        "v_exp_f32 v86, v86\n"
        "v_mfma_f32_32x32x16_bf16 v[64:79], %[k1], %[qa], %[ma]\n"
        "v_exp_f32 v87, v87\n"
        "v_exp_f32 v88, v88\n"
        "v_exp_f32 v89, v89\n"
        "v_exp_f32 v90, v90\n"
        "v_smfmac_f32_32x32x32_bf16 %[ob], %[v00], v[80:87], %[idx]\n"
        "s_waitcnt lgkmcnt(0)\n"
        "s_nop 0\n"
        "v_exp_f32 v91, v91\n"
        "v_exp_f32 v92, v92\n"
        "v_exp_f32 v93, v93\n"
        "v_exp_f32 v94, v94\n"
        "v_exp_f32 v95, v95\n"
        "v_exp_f32 v64, v64\n"
        "v_exp_f32 v65, v65\n"
        "v_exp_f32 v66, v66\n"
        "v_smfmac_f32_32x32x32_bf16 %[ob], %[v01], v[88:95], %[idx]\n"
        "ds_read_b128 %[k0], v99 offset:4096\n"
        "global_load_dwordx4 a[16:19], %[voff], s[84:85] offset:0 sc1\n"
        "v_exp_f32 v67, v67\n"
        "v_exp_f32 v68, v68\n"
        "v_exp_f32 v69, v69\n"
        "v_exp_f32 v70, v70\n"
        "v_mfma_f32_32x32x16_bf16 v[80:95], %[k1], %[qb], %[mb]\n"
        "ds_read_b128 %[v00], v100 offset:4096\n"
        "global_load_dwordx4 a[28:31], %[voff], s[84:85] offset:3072 sc1\n"
        "v_exp_f32 v71, v71\n"
        "v_exp_f32 v72, v72\n"
        "v_exp_f32 v73, v73\n"
        "v_exp_f32 v74, v74\n"
        "v_smfmac_f32_32x32x32_bf16 %[oa], %[v10], v[64:71], %[idx]\n"
        "ds_read_b128 %[v01], v100 offset:4608\n"
        "global_load_dwordx4 a[20:23], %[voff], s[84:85] offset:1024 sc1\n"
        "v_exp_f32 v75, v75\n"
        "v_exp_f32 v76, v76\n"
        "v_exp_f32 v77, v77\n"
        "v_exp_f32 v78, v78\n"
        "v_exp_f32 v79, v79\n"
        "v_exp_f32 v80, v80\n"
        "v_exp_f32 v81, v81\n"
        "v_exp_f32 v82, v82\n"
        "v_smfmac_f32_32x32x32_bf16 %[oa], %[v11], v[72:79], %[idx]\n"
        "s_waitcnt lgkmcnt(2)\n"
        "global_load_dwordx4 a[24:27], %[voff], s[84:85] offset:2048 sc1\n"
        "v_exp_f32 v83, v83\n"
        "v_exp_f32 v84, v84\n"
        "v_exp_f32 v85, v85\n"
        "v_exp_f32 v86, v86\n"
        "v_mfma_f32_32x32x16_bf16 v[64:79], %[k0], %[qa], %[ma]\n"
        "s_waitcnt vmcnt(16)\n"
        "v_exp_f32 v87, v87\n"
        "v_exp_f32 v88, v88\n"
        "v_exp_f32 v89, v89\n"
        "v_exp_f32 v90, v90\n"
        "v_smfmac_f32_32x32x32_bf16 %[ob], %[v10], v[80:87], %[idx]\n"
        "s_waitcnt lgkmcnt(0)\n"
        "s_barrier\n"
        "v_exp_f32 v91, v91\n"
        "v_exp_f32 v92, v92\n"
        "v_exp_f32 v93, v93\n"
        "v_exp_f32 v94, v94\n"
        "v_exp_f32 v95, v95\n"
        "v_exp_f32 v64, v64\n"
        "v_exp_f32 v65, v65\n"
        "v_exp_f32 v66, v66\n"
        "v_smfmac_f32_32x32x32_bf16 %[ob], %[v11], v[88:95], %[idx]\n"
        "ds_read_b128 %[k1], v99 offset:5120\n"
        "s_mov_b32 m0, s73\n"
        "v_exp_f32 v67, v67\n"
        "v_exp_f32 v68, v68\n"
        "v_exp_f32 v69, v69\n"
        "v_exp_f32 v70, v70\n"
        "v_mfma_f32_32x32x16_bf16 v[80:95], %[k0], %[qb], %[mb]\n"
        "ds_read_b128 %[v10], v100 offset:5120\n"
        "global_load_dwordx4 a[40:43], %[voff], s[86:87] offset:-2048 sc1\n"
        "v_exp_f32 v71, v71\n"
        "v_exp_f32 v72, v72\n"
        "v_exp_f32 v73, v73\n"
        "v_exp_f32 v74, v74\n"
        "v_smfmac_f32_32x32x32_bf16 %[oa], %[v00], v[64:71], %[idx]\n"
        "ds_read_b128 %[v11], v100 offset:5632\n"
        "global_load_lds_dwordx4 %[voff], s[74:75] sc1\n"
        "v_exp_f32 v75, v75\n"
        "v_exp_f32 v76, v76\n"
        "v_exp_f32 v77, v77\n"
        "v_exp_f32 v78, v78\n"
        "v_exp_f32 v79, v79\n"
        "v_exp_f32 v80, v80\n"
        "v_exp_f32 v81, v81\n"
        "v_exp_f32 v82, v82\n"
        "v_smfmac_f32_32x32x32_bf16 %[oa], %[v01], v[72:79], %[idx]\n"
        "s_waitcnt lgkmcnt(2)\n"
        "global_load_dwordx4 a[32:35], %[voff], s[86:87] offset:-4096 sc1\n"
        "v_exp_f32 v83, v83\n"
        "v_exp_f32 v84, v84\n"
        "v_exp_f32 v85, v85\n"
        "v_exp_f32 v86, v86\n"
        "v_mfma_f32_32x32x16_bf16 v[64:79], %[k1], %[qa], %[ma]\n"
        "global_load_dwordx4 a[36:39], %[voff], s[86:87] offset:-3072 sc1\n"
        "v_exp_f32 v87, v87\n"
        "v_exp_f32 v88, v88\n"
        "v_exp_f32 v89, v89\n"
        "v_exp_f32 v90, v90\n"
        "v_smfmac_f32_32x32x32_bf16 %[ob], %[v00], v[80:87], %[idx]\n"
        "s_waitcnt lgkmcnt(0)\n"
        "global_load_lds_dwordx4 %[voff], s[74:75] offset:1024 sc1\n"
        "v_exp_f32 v91, v91\n"
        "v_exp_f32 v92, v92\n"
        "v_exp_f32 v93, v93\n"
        "v_exp_f32 v94, v94\n"
        "v_exp_f32 v95, v95\n"
        "v_exp_f32 v64, v64\n"
        "v_exp_f32 v65, v65\n"
        "v_exp_f32 v66, v66\n"
        "v_smfmac_f32_32x32x32_bf16 %[ob], %[v01], v[88:95], %[idx]\n"
        "ds_read_b128 %[k0], v99 offset:6144\n"
        "s_add_u32 m0, s73, 65536\n"
        "v_exp_f32 v67, v67\n"
        "v_exp_f32 v68, v68\n"
        "v_exp_f32 v69, v69\n"
        "v_exp_f32 v70, v70\n"
        "v_mfma_f32_32x32x16_bf16 v[80:95], %[k1], %[qb], %[mb]\n"
        "ds_read_b128 %[v00], v100 offset:6144\n"
        "global_load_dwordx4 a[52:55], %[voff], s[86:87] offset:1024 sc1\n"
        "v_exp_f32 v71, v71\n"
        "v_exp_f32 v72, v72\n"
        "v_exp_f32 v73, v73\n"
        "v_exp_f32 v74, v74\n"
        "v_smfmac_f32_32x32x32_bf16 %[oa], %[v10], v[64:71], %[idx]\n"
        "ds_read_b128 %[v01], v100 offset:6656\n"
        "global_load_lds_dwordx4 %[voff], s[76:77] sc1\n"
        "v_exp_f32 v75, v75\n"
        "v_exp_f32 v76, v76\n"
        "v_exp_f32 v77, v77\n"
        "v_exp_f32 v78, v78\n"
        "v_exp_f32 v79, v79\n"
        "v_exp_f32 v80, v80\n"
        "v_exp_f32 v81, v81\n"
        "v_exp_f32 v82, v82\n"
        "v_smfmac_f32_32x32x32_bf16 %[oa], %[v11], v[72:79], %[idx]\n"
        "s_waitcnt lgkmcnt(2)\n"
        "global_load_dwordx4 a[44:47], %[voff], s[86:87] offset:-1024 sc1\n"
        "v_exp_f32 v83, v83\n"
        "v_exp_f32 v84, v84\n"
        "v_exp_f32 v85, v85\n"
        "v_exp_f32 v86, v86\n"
        "v_mfma_f32_32x32x16_bf16 v[64:79], %[k0], %[qa], %[ma]\n"
        "global_load_dwordx4 a[48:51], %[voff], s[86:87] offset:0 sc1\n"
        "v_exp_f32 v87, v87\n"
        "v_exp_f32 v88, v88\n"
        "v_exp_f32 v89, v89\n"
        "v_exp_f32 v90, v90\n"
        "v_smfmac_f32_32x32x32_bf16 %[ob], %[v10], v[80:87], %[idx]\n"
        "s_waitcnt lgkmcnt(0)\n"
        "global_load_lds_dwordx4 %[voff], s[76:77] offset:1024 sc1\n"
        "v_exp_f32 v91, v91\n"
        "v_exp_f32 v92, v92\n"
        "v_exp_f32 v93, v93\n"
        "v_exp_f32 v94, v94\n"
        "v_exp_f32 v95, v95\n"
        "v_exp_f32 v64, v64\n"
        "v_exp_f32 v65, v65\n"
        "v_exp_f32 v66, v66\n"
        "v_smfmac_f32_32x32x32_bf16 %[ob], %[v11], v[88:95], %[idx]\n"
        "ds_read_b128 %[k1], v99 offset:7168\n"
        "s_add_u32 s73, s73, 8192\n"
        "v_exp_f32 v67, v67\n"
        "v_exp_f32 v68, v68\n"
        "v_exp_f32 v69, v69\n"
        "v_exp_f32 v70, v70\n"
        "v_mfma_f32_32x32x16_bf16 v[80:95], %[k0], %[qb], %[mb]\n"
        "ds_read_b128 %[v10], v100 offset:7168\n"
        "global_load_dwordx4 a[64:67], %[voff], s[88:89] offset:-4096 sc1\n"
        "v_exp_f32 v71, v71\n"
        "v_exp_f32 v72, v72\n"
        "v_exp_f32 v73, v73\n"
        "v_exp_f32 v74, v74\n"
        "v_smfmac_f32_32x32x32_bf16 %[oa], %[v00], v[64:71], %[idx]\n"
        "ds_read_b128 %[v11], v100 offset:7680\n"
        "s_add_u32 s74, s74, 8192\n"
        "v_exp_f32 v75, v75\n"
        "v_exp_f32 v76, v76\n"
        "v_exp_f32 v77, v77\n"
        "v_exp_f32 v78, v78\n"
        "v_exp_f32 v79, v79\n"
        "v_exp_f32 v80, v80\n"
        "v_exp_f32 v81, v81\n"
        "v_exp_f32 v82, v82\n"
        "v_smfmac_f32_32x32x32_bf16 %[oa], %[v01], v[72:79], %[idx]\n"
        "s_waitcnt lgkmcnt(2)\n"
        "s_addc_u32 s75, s75, 0\n"
        "v_exp_f32 v83, v83\n"
        "v_exp_f32 v84, v84\n"
        "v_exp_f32 v85, v85\n"
        "v_exp_f32 v86, v86\n"
        "v_mfma_f32_32x32x16_bf16 v[64:79], %[k1], %[qa], %[ma]\n"
        "global_load_dwordx4 a[56:59], %[voff], s[86:87] offset:2048 sc1\n"
        "v_exp_f32 v87, v87\n"
        "v_exp_f32 v88, v88\n"
        "v_exp_f32 v89, v89\n"
        "v_exp_f32 v90, v90\n"
        "v_smfmac_f32_32x32x32_bf16 %[ob], %[v00], v[80:87], %[idx]\n"
        "s_waitcnt lgkmcnt(0)\n"
        "global_load_dwordx4 a[60:63], %[voff], s[86:87] offset:3072 sc1\n"
        "v_exp_f32 v91, v91\n"
        "v_exp_f32 v92, v92\n"
        "v_exp_f32 v93, v93\n"
        "v_exp_f32 v94, v94\n"
        "v_exp_f32 v95, v95\n"
        "v_exp_f32 v64, v64\n"
        "v_exp_f32 v65, v65\n"
        "v_exp_f32 v66, v66\n"
        "v_smfmac_f32_32x32x32_bf16 %[ob], %[v01], v[88:95], %[idx]\n"
        "ds_read_b128 %[k0], v99 offset:8192\n"
        "global_load_dwordx4 a[68:71], %[voff], s[88:89] offset:-3072 sc1\n"
        "v_exp_f32 v67, v67\n"
        "v_exp_f32 v68, v68\n"
        "v_exp_f32 v69, v69\n"
        "v_exp_f32 v70, v70\n"
        "v_mfma_f32_32x32x16_bf16 v[80:95], %[k1], %[qb], %[mb]\n"
        "ds_read_b128 %[v00], v100 offset:8192\n"
        "v_exp_f32 v71, v71\n"
        "v_exp_f32 v72, v72\n"
        "v_exp_f32 v73, v73\n"
        "v_exp_f32 v74, v74\n"
        "v_smfmac_f32_32x32x32_bf16 %[oa], %[v10], v[64:71], %[idx]\n"
        "ds_read_b128 %[v01], v100 offset:8704\n"
        "global_load_dwordx4 a[72:75], %[voff], s[88:89] offset:-2048 sc1\n"
        "v_exp_f32 v75, v75\n"
        "v_exp_f32 v76, v76\n"
        "v_exp_f32 v77, v77\n"
        "v_exp_f32 v78, v78\n"
        "v_exp_f32 v79, v79\n"
        "v_exp_f32 v80, v80\n"
        "v_exp_f32 v81, v81\n"
        "v_exp_f32 v82, v82\n"
        "v_smfmac_f32_32x32x32_bf16 %[oa], %[v11], v[72:79], %[idx]\n"
        "s_waitcnt lgkmcnt(2)\n"
        "global_load_dwordx4 a[76:79], %[voff], s[88:89] offset:-1024 sc1\n"
        "v_exp_f32 v83, v83\n"
        "v_exp_f32 v84, v84\n"
        "v_exp_f32 v85, v85\n"
        "v_exp_f32 v86, v86\n"
        "v_mfma_f32_32x32x16_bf16 v[64:79], %[k0], %[qa], %[ma]\n"
        "global_load_dwordx4 a[80:83], %[voff], s[88:89] offset:0 sc1\n"
        "v_exp_f32 v87, v87\n"
        "v_exp_f32 v88, v88\n"
        "v_exp_f32 v89, v89\n"
        "v_exp_f32 v90, v90\n"
        "v_smfmac_f32_32x32x32_bf16 %[ob], %[v10], v[80:87], %[idx]\n"
        "s_waitcnt lgkmcnt(0)\n"
        "v_add_u32 v99, 8192, v99\n"
        "v_add_u32 v100, %[vinc], v100\n"
        "v_exp_f32 v91, v91\n"
        "v_exp_f32 v92, v92\n"
        "v_exp_f32 v93, v93\n"
        "v_exp_f32 v94, v94\n"
        "v_exp_f32 v95, v95\n"
        "v_exp_f32 v64, v64\n"
        "v_exp_f32 v65, v65\n"
        "v_exp_f32 v66, v66\n"
        "v_smfmac_f32_32x32x32_bf16 %[ob], %[v11], v[88:95], %[idx]\n"
        "ds_read_b128 %[k1], v99 offset:1024\n"
        "global_load_dwordx4 a[84:87], %[voff], s[88:89] offset:1024 sc1\n"
        "v_exp_f32 v67, v67\n"
        "v_exp_f32 v68, v68\n"
        "v_exp_f32 v69, v69\n"
        "v_exp_f32 v70, v70\n"
        "v_mfma_f32_32x32x16_bf16 v[80:95], %[k0], %[qb], %[mb]\n"
        "ds_read_b128 %[v10], v100 offset:1024\n"
        "global_load_dwordx4 a[96:99], %[voff], s[90:91] offset:-4096 sc1\n"
        "v_exp_f32 v71, v71\n"
        "v_exp_f32 v72, v72\n"
        "v_exp_f32 v73, v73\n"
        "v_exp_f32 v74, v74\n"
        "v_smfmac_f32_32x32x32_bf16 %[oa], %[v00], v[64:71], %[idx]\n"
        "ds_read_b128 %[v11], v100 offset:1536\n"
        "global_load_dwordx4 a[88:91], %[voff], s[88:89] offset:2048 sc1\n"
        "v_exp_f32 v75, v75\n"
        "v_exp_f32 v76, v76\n"
        "v_exp_f32 v77, v77\n"
        "v_exp_f32 v78, v78\n"
        "v_exp_f32 v79, v79\n"
        "v_exp_f32 v80, v80\n"
        "v_exp_f32 v81, v81\n"
        "v_exp_f32 v82, v82\n"
        "v_smfmac_f32_32x32x32_bf16 %[oa], %[v01], v[72:79], %[idx]\n"
        "s_waitcnt lgkmcnt(2)\n"
        "s_add_u32 s76, s74, 65536\n"
        "v_exp_f32 v83, v83\n"
        "v_exp_f32 v84, v84\n"
        "v_exp_f32 v85, v85\n"
        "v_exp_f32 v86, v86\n"
        "v_mfma_f32_32x32x16_bf16 v[64:79], %[k1], %[qa], %[ma]\n"
        "s_addc_u32 s77, s75, 0\n"
        "v_exp_f32 v87, v87\n"
        "v_exp_f32 v88, v88\n"
        "v_exp_f32 v89, v89\n"
        "v_exp_f32 v90, v90\n"
        "v_smfmac_f32_32x32x32_bf16 %[ob], %[v00], v[80:87], %[idx]\n"
        "s_waitcnt lgkmcnt(0)\n"
        "global_load_dwordx4 a[92:95], %[voff], s[88:89] offset:3072 sc1\n"
        "v_exp_f32 v91, v91\n"
        "v_exp_f32 v92, v92\n"
        "v_exp_f32 v93, v93\n"
        "v_exp_f32 v94, v94\n"
        "v_exp_f32 v95, v95\n"
        "v_exp_f32 v64, v64\n"
        "v_exp_f32 v65, v65\n"
        "v_exp_f32 v66, v66\n"
        "v_smfmac_f32_32x32x32_bf16 %[ob], %[v01], v[88:95], %[idx]\n"
        "ds_read_b128 %[k0], v99 offset:2048\n"
        "global_load_dwordx4 a[100:103], %[voff], s[90:91] offset:-3072 sc1\n"
        "v_exp_f32 v67, v67\n"
        "v_exp_f32 v68, v68\n"
        "v_exp_f32 v69, v69\n"
        "v_exp_f32 v70, v70\n"
        "v_mfma_f32_32x32x16_bf16 v[80:95], %[k1], %[qb], %[mb]\n"
        "ds_read_b128 %[v00], v100 offset:2048\n"
        "v_exp_f32 v71, v71\n"
        "v_exp_f32 v72, v72\n"
        "v_exp_f32 v73, v73\n"
        "v_exp_f32 v74, v74\n"
        "v_smfmac_f32_32x32x32_bf16 %[oa], %[v10], v[64:71], %[idx]\n"
        "ds_read_b128 %[v01], v100 offset:2560\n"
        "global_load_dwordx4 a[104:107], %[voff], s[90:91] offset:-2048 sc1\n"
        "v_exp_f32 v75, v75\n"
        "v_exp_f32 v76, v76\n"
        "v_exp_f32 v77, v77\n"
        "v_exp_f32 v78, v78\n"
        "v_exp_f32 v79, v79\n"
        "v_exp_f32 v80, v80\n"
        "v_exp_f32 v81, v81\n"
        "v_exp_f32 v82, v82\n"
        "v_smfmac_f32_32x32x32_bf16 %[oa], %[v11], v[72:79], %[idx]\n"
        "s_waitcnt lgkmcnt(2)\n"
        "global_load_dwordx4 a[108:111], %[voff], s[90:91] offset:-1024 sc1\n"
        "v_exp_f32 v83, v83\n"
        "v_exp_f32 v84, v84\n"
        "v_exp_f32 v85, v85\n"
        "v_exp_f32 v86, v86\n"
        "v_mfma_f32_32x32x16_bf16 v[64:79], %[k0], %[qa], %[ma]\n"
        "global_load_dwordx4 a[112:115], %[voff], s[90:91] offset:0 sc1\n"
        "v_exp_f32 v87, v87\n"
        "v_exp_f32 v88, v88\n"
        "v_exp_f32 v89, v89\n"
        "v_exp_f32 v90, v90\n"
        "v_smfmac_f32_32x32x32_bf16 %[ob], %[v10], v[80:87], %[idx]\n"
        "s_waitcnt lgkmcnt(0)\n"
        "v_exp_f32 v91, v91\n"
        "v_exp_f32 v92, v92\n"
        "v_exp_f32 v93, v93\n"
        "v_exp_f32 v94, v94\n"
        "v_exp_f32 v95, v95\n"
        "v_exp_f32 v64, v64\n"
        "v_exp_f32 v65, v65\n"
        "v_exp_f32 v66, v66\n"
        "v_smfmac_f32_32x32x32_bf16 %[ob], %[v11], v[88:95], %[idx]\n"
        "ds_read_b128 %[k1], v99 offset:3072\n"
        "global_load_dwordx4 a[116:119], %[voff], s[90:91] offset:1024 sc1\n"
        "v_exp_f32 v67, v67\n"
        "v_exp_f32 v68, v68\n"
        "v_exp_f32 v69, v69\n"
        "v_exp_f32 v70, v70\n"
        "v_mfma_f32_32x32x16_bf16 v[80:95], %[k0], %[qb], %[mb]\n"
        "ds_read_b128 %[v10], v100 offset:3072\n"
        "v_exp_f32 v71, v71\n"
        "v_exp_f32 v72, v72\n"
        "v_exp_f32 v73, v73\n"
        "v_exp_f32 v74, v74\n"
        "v_smfmac_f32_32x32x32_bf16 %[oa], %[v00], v[64:71], %[idx]\n"
        "ds_read_b128 %[v11], v100 offset:3584\n"
        "global_load_dwordx4 a[120:123], %[voff], s[90:91] offset:2048 sc1\n"
        "v_exp_f32 v75, v75\n"
        "v_exp_f32 v76, v76\n"
        "v_exp_f32 v77, v77\n"
        "v_exp_f32 v78, v78\n"
        "v_exp_f32 v79, v79\n"
        "v_exp_f32 v80, v80\n"
        "v_exp_f32 v81, v81\n"
        "v_exp_f32 v82, v82\n"
        "v_smfmac_f32_32x32x32_bf16 %[oa], %[v01], v[72:79], %[idx]\n"
        "s_waitcnt lgkmcnt(2)\n"
        "global_load_dwordx4 a[124:127], %[voff], s[90:91] offset:3072 sc1\n"
        "v_exp_f32 v83, v83\n"
        "v_exp_f32 v84, v84\n"
        "v_exp_f32 v85, v85\n"
        "v_exp_f32 v86, v86\n"
        "v_mfma_f32_32x32x16_bf16 v[64:79], %[k1], %[qa], %[ma]\n"
        "v_exp_f32 v87, v87\n"
        "v_exp_f32 v88, v88\n"
        "v_exp_f32 v89, v89\n"
        "v_exp_f32 v90, v90\n"
        "v_smfmac_f32_32x32x32_bf16 %[ob], %[v00], v[80:87], %[idx]\n"
        "s_waitcnt lgkmcnt(0)\n"
        "s_nop 0\n"
        "v_exp_f32 v91, v91\n"
        "v_exp_f32 v92, v92\n"
        "v_exp_f32 v93, v93\n"
        "v_exp_f32 v94, v94\n"
        "v_exp_f32 v95, v95\n"
        "v_exp_f32 v64, v64\n"
        "v_exp_f32 v65, v65\n"
        "v_exp_f32 v66, v66\n"
        "v_smfmac_f32_32x32x32_bf16 %[ob], %[v01], v[88:95], %[idx]\n"
        "ds_read_b128 %[k0], v99 offset:4096\n"
        "v_exp_f32 v67, v67\n"
        "v_exp_f32 v68, v68\n"
        "v_exp_f32 v69, v69\n"
        "v_exp_f32 v70, v70\n"
        "v_mfma_f32_32x32x16_bf16 v[80:95], %[k1], %[qb], %[mb]\n"
        "ds_read_b128 %[v00], v100 offset:4096\n"
        "v_exp_f32 v71, v71\n"
        "v_exp_f32 v72, v72\n"
        "v_exp_f32 v73, v73\n"
        "v_exp_f32 v74, v74\n"
        "v_smfmac_f32_32x32x32_bf16 %[oa], %[v10], v[64:71], %[idx]\n"
        "ds_read_b128 %[v01], v100 offset:4608\n"
        "v_exp_f32 v75, v75\n"
        "v_exp_f32 v76, v76\n"
        "v_exp_f32 v77, v77\n"
        "v_exp_f32 v78, v78\n"
        "v_exp_f32 v79, v79\n"
        "v_exp_f32 v80, v80\n"
        "v_exp_f32 v81, v81\n"
        "v_exp_f32 v82, v82\n"
        "v_smfmac_f32_32x32x32_bf16 %[oa], %[v11], v[72:79], %[idx]\n"
        "s_waitcnt lgkmcnt(2)\n"
        "v_exp_f32 v83, v83\n"
        "v_exp_f32 v84, v84\n"
        "v_exp_f32 v85, v85\n"
        "v_exp_f32 v86, v86\n"
        "v_mfma_f32_32x32x16_bf16 v[64:79], %[k0], %[qa], %[ma]\n"
        "s_waitcnt vmcnt(18)\n"
        "v_exp_f32 v87, v87\n"
        "v_exp_f32 v88, v88\n"
        "v_exp_f32 v89, v89\n"
        "v_exp_f32 v90, v90\n"
        "v_smfmac_f32_32x32x32_bf16 %[ob], %[v10], v[80:87], %[idx]\n"
        "s_waitcnt lgkmcnt(0)\n"
        "s_barrier\n"
        "v_exp_f32 v91, v91\n"
        "v_exp_f32 v92, v92\n"
        "v_exp_f32 v93, v93\n"
        "v_exp_f32 v94, v94\n"
        "v_exp_f32 v95, v95\n"
        "v_exp_f32 v64, v64\n"
        "v_exp_f32 v65, v65\n"
        "v_exp_f32 v66, v66\n"
        "v_smfmac_f32_32x32x32_bf16 %[ob], %[v11], v[88:95], %[idx]\n"
        "ds_read_b128 %[k1], v99 offset:5120\n"
        "s_add_u32 m0, %[m13], 65536\n"
        "s_add_u32 s82, %[w13l], 65536\n"
        "s_addc_u32 s83, %[w13h], 0\n"
        "global_load_lds_dwordx4 %[voff], s[82:83] offset:0 sc1\n"
        "v_exp_f32 v67, v67\n"
        "v_exp_f32 v68, v68\n"
        "v_exp_f32 v69, v69\n"
        "v_exp_f32 v70, v70\n"
        "v_mfma_f32_32x32x16_bf16 v[80:95], %[k0], %[qb], %[mb]\n"
        "ds_read_b128 %[v10], v100 offset:5120\n"
        "global_load_lds_dwordx4 %[voff], s[82:83] offset:1024 sc1\n"
        "v_exp_f32 v71, v71\n"
        "v_exp_f32 v72, v72\n"
        "v_exp_f32 v73, v73\n"
        "v_exp_f32 v74, v74\n"
        "v_smfmac_f32_32x32x32_bf16 %[oa], %[v00], v[64:71], %[idx]\n"
        "ds_read_b128 %[v11], v100 offset:5632\n"
        "global_load_lds_dwordx4 %[voff], s[82:83] offset:2048 sc1\n"
        "v_exp_f32 v75, v75\n"
        "v_exp_f32 v76, v76\n"
        "v_exp_f32 v77, v77\n"
        "v_exp_f32 v78, v78\n"
        "v_exp_f32 v79, v79\n"
        "v_exp_f32 v80, v80\n"
        "v_exp_f32 v81, v81\n"
        "v_exp_f32 v82, v82\n"
        "v_smfmac_f32_32x32x32_bf16 %[oa], %[v01], v[72:79], %[idx]\n"
        "s_waitcnt lgkmcnt(2)\n"
        "global_load_lds_dwordx4 %[voff], s[82:83] offset:3072 sc1\n"
        "v_exp_f32 v83, v83\n"
        "v_exp_f32 v84, v84\n"
        "v_exp_f32 v85, v85\n"
        "v_exp_f32 v86, v86\n"
        "v_mfma_f32_32x32x16_bf16 v[64:79], %[k1], %[qa], %[ma]\n"
        "v_exp_f32 v87, v87\n"
        "v_exp_f32 v88, v88\n"
        "v_exp_f32 v89, v89\n"
        "v_exp_f32 v90, v90\n"
        "v_smfmac_f32_32x32x32_bf16 %[ob], %[v00], v[80:87], %[idx]\n"
        "s_waitcnt lgkmcnt(0)\n"
        "s_nop 0\n"
        "v_exp_f32 v91, v91\n"
        "v_exp_f32 v92, v92\n"
        "v_exp_f32 v93, v93\n"
        "v_exp_f32 v94, v94\n"
        "v_exp_f32 v95, v95\n"
        "v_exp_f32 v64, v64\n"
        "v_exp_f32 v65, v65\n"
        "v_exp_f32 v66, v66\n"
        "v_smfmac_f32_32x32x32_bf16 %[ob], %[v01], v[88:95], %[idx]\n"
        "ds_read_b128 %[k0], v99 offset:6144\n"
        "s_add_u32 m0, %[m13], 69632\n"
        "s_add_u32 s82, %[w13l], 69632\n"
        "s_addc_u32 s83, %[w13h], 0\n"
        "global_load_lds_dwordx4 %[voff], s[82:83] offset:0 sc1\n"
        "v_exp_f32 v67, v67\n"
        "v_exp_f32 v68, v68\n"
        "v_exp_f32 v69, v69\n"
        "v_exp_f32 v70, v70\n"
        "v_mfma_f32_32x32x16_bf16 v[80:95], %[k1], %[qb], %[mb]\n"
        "ds_read_b128 %[v00], v100 offset:6144\n"
        "global_load_lds_dwordx4 %[voff], s[82:83] offset:1024 sc1\n"
        "v_exp_f32 v71, v71\n"
        "v_exp_f32 v72, v72\n"
        "v_exp_f32 v73, v73\n"
        "v_exp_f32 v74, v74\n"
        "v_smfmac_f32_32x32x32_bf16 %[oa], %[v10], v[64:71], %[idx]\n"
        "ds_read_b128 %[v01], v100 offset:6656\n"
        "global_load_lds_dwordx4 %[voff], s[82:83] offset:2048 sc1\n"
        "v_exp_f32 v75, v75\n"
        "v_exp_f32 v76, v76\n"
        "v_exp_f32 v77, v77\n"
        "v_exp_f32 v78, v78\n"
        "v_exp_f32 v79, v79\n"
        "v_exp_f32 v80, v80\n"
        "v_exp_f32 v81, v81\n"
        "v_exp_f32 v82, v82\n"
        "v_smfmac_f32_32x32x32_bf16 %[oa], %[v11], v[72:79], %[idx]\n"
        "s_waitcnt lgkmcnt(2)\n"
        "global_load_lds_dwordx4 %[voff], s[82:83] offset:3072 sc1\n"
        "v_exp_f32 v83, v83\n"
        "v_exp_f32 v84, v84\n"
        "v_exp_f32 v85, v85\n"
        "v_exp_f32 v86, v86\n"
        "v_mfma_f32_32x32x16_bf16 v[64:79], %[k0], %[qa], %[ma]\n"
        "v_exp_f32 v87, v87\n"
        "v_exp_f32 v88, v88\n"
        "v_exp_f32 v89, v89\n"
        "v_exp_f32 v90, v90\n"
        "v_smfmac_f32_32x32x32_bf16 %[ob], %[v10], v[80:87], %[idx]\n"
        "s_waitcnt lgkmcnt(0)\n"
        "s_nop 0\n"
        "v_exp_f32 v91, v91\n"
        "v_exp_f32 v92, v92\n"
        "v_exp_f32 v93, v93\n"
        "v_exp_f32 v94, v94\n"
        "v_exp_f32 v95, v95\n"
        "v_exp_f32 v64, v64\n"
        "v_exp_f32 v65, v65\n"
        "v_exp_f32 v66, v66\n"
        "v_smfmac_f32_32x32x32_bf16 %[ob], %[v11], v[88:95], %[idx]\n"
        "ds_read_b128 %[k1], v99 offset:7168\n"
        "s_add_u32 s73, s73, 8192\n"
        "v_exp_f32 v67, v67\n"
        "v_exp_f32 v68, v68\n"
        "v_exp_f32 v69, v69\n"
        "v_exp_f32 v70, v70\n"
        "v_mfma_f32_32x32x16_bf16 v[80:95], %[k0], %[qb], %[mb]\n"
        "ds_read_b128 %[v10], v100 offset:7168\n"
        "v_exp_f32 v71, v71\n"
        "v_exp_f32 v72, v72\n"
        "v_exp_f32 v73, v73\n"
        "v_exp_f32 v74, v74\n"
        "v_smfmac_f32_32x32x32_bf16 %[oa], %[v00], v[64:71], %[idx]\n"
        "ds_read_b128 %[v11], v100 offset:7680\n"
        "s_add_u32 s74, s74, 8192\n"
        "v_exp_f32 v75, v75\n"
        "v_exp_f32 v76, v76\n"
        "v_exp_f32 v77, v77\n"
        "v_exp_f32 v78, v78\n"
        "v_exp_f32 v79, v79\n"
        "v_exp_f32 v80, v80\n"
        "v_exp_f32 v81, v81\n"
        "v_exp_f32 v82, v82\n"
        "v_smfmac_f32_32x32x32_bf16 %[oa], %[v01], v[72:79], %[idx]\n"
        "s_waitcnt lgkmcnt(2)\n"
        "s_addc_u32 s75, s75, 0\n"
        "v_exp_f32 v83, v83\n"
        "v_exp_f32 v84, v84\n"
        "v_exp_f32 v85, v85\n"
        "v_exp_f32 v86, v86\n"
        "v_mfma_f32_32x32x16_bf16 v[64:79], %[k1], %[qa], %[ma]\n"
        "v_exp_f32 v87, v87\n"
        "v_exp_f32 v88, v88\n"
        "v_exp_f32 v89, v89\n"
        "v_exp_f32 v90, v90\n"
        "v_smfmac_f32_32x32x32_bf16 %[ob], %[v00], v[80:87], %[idx]\n"
        "s_waitcnt lgkmcnt(0)\n"
        "s_nop 0\n"
        "v_exp_f32 v91, v91\n"
        "v_exp_f32 v92, v92\n"
        "v_exp_f32 v93, v93\n"
        "v_exp_f32 v94, v94\n"
        "v_exp_f32 v95, v95\n"
        "v_exp_f32 v64, v64\n"
        "v_exp_f32 v65, v65\n"
        "v_exp_f32 v66, v66\n"
        "v_smfmac_f32_32x32x32_bf16 %[ob], %[v01], v[88:95], %[idx]\n"
        "ds_read_b128 %[k0], v99 offset:8192\n"
        "v_exp_f32 v67, v67\n"
        "v_exp_f32 v68, v68\n"
        "v_exp_f32 v69, v69\n"
        "v_exp_f32 v70, v70\n"
        "v_mfma_f32_32x32x16_bf16 v[80:95], %[k1], %[qb], %[mb]\n"
        "ds_read_b128 %[v00], v100 offset:8192\n"
        "v_exp_f32 v71, v71\n"
        "v_exp_f32 v72, v72\n"
        "v_exp_f32 v73, v73\n"
        "v_exp_f32 v74, v74\n"
        "v_smfmac_f32_32x32x32_bf16 %[oa], %[v10], v[64:71], %[idx]\n"
        "ds_read_b128 %[v01], v100 offset:8704\n"
        "v_exp_f32 v75, v75\n"
        "v_exp_f32 v76, v76\n"
        "v_exp_f32 v77, v77\n"
        "v_exp_f32 v78, v78\n"
        "v_exp_f32 v79, v79\n"
        "v_exp_f32 v80, v80\n"
        "v_exp_f32 v81, v81\n"
        "v_exp_f32 v82, v82\n"
        "v_smfmac_f32_32x32x32_bf16 %[oa], %[v11], v[72:79], %[idx]\n"
        "s_waitcnt lgkmcnt(2)\n"
        "v_exp_f32 v83, v83\n"
        "v_exp_f32 v84, v84\n"
        "v_exp_f32 v85, v85\n"
        "v_exp_f32 v86, v86\n"
        "v_mfma_f32_32x32x16_bf16 v[64:79], %[k0], %[qa], %[ma]\n"
        "v_exp_f32 v87, v87\n"
        "v_exp_f32 v88, v88\n"
        "v_exp_f32 v89, v89\n"
        "v_exp_f32 v90, v90\n"
        "v_smfmac_f32_32x32x32_bf16 %[ob], %[v10], v[80:87], %[idx]\n"
        "s_waitcnt lgkmcnt(0)\n"
        "v_add_u32 v99, 8192, v99\n"
        "v_add_u32 v100, %[vinc], v100\n"
        "v_exp_f32 v91, v91\n"
        "v_exp_f32 v92, v92\n"
        "v_exp_f32 v93, v93\n"
        "v_exp_f32 v94, v94\n"
        "v_exp_f32 v95, v95\n"
        "v_exp_f32 v64, v64\n"
        "v_exp_f32 v65, v65\n"
        "v_exp_f32 v66, v66\n"
        "v_smfmac_f32_32x32x32_bf16 %[ob], %[v11], v[88:95], %[idx]\n"
        "ds_read_b128 %[k1], v99 offset:1024\n"
        "v_exp_f32 v67, v67\n"
        "v_exp_f32 v68, v68\n"
        "v_exp_f32 v69, v69\n"
        "v_exp_f32 v70, v70\n"
        "v_mfma_f32_32x32x16_bf16 v[80:95], %[k0], %[qb], %[mb]\n"
        "ds_read_b128 %[v10], v100 offset:1024\n"
        "v_exp_f32 v71, v71\n"
        "v_exp_f32 v72, v72\n"
        "v_exp_f32 v73, v73\n"
        "v_exp_f32 v74, v74\n"
        "v_smfmac_f32_32x32x32_bf16 %[oa], %[v00], v[64:71], %[idx]\n"
        "ds_read_b128 %[v11], v100 offset:1536\n"
        "v_exp_f32 v75, v75\n"
        "v_exp_f32 v76, v76\n"
        "v_exp_f32 v77, v77\n"
        "v_exp_f32 v78, v78\n"
        "v_exp_f32 v79, v79\n"
        "v_exp_f32 v80, v80\n"
        "v_exp_f32 v81, v81\n"
        "v_exp_f32 v82, v82\n"
        "v_smfmac_f32_32x32x32_bf16 %[oa], %[v01], v[72:79], %[idx]\n"
        "s_waitcnt lgkmcnt(2)\n"
        "s_add_u32 s76, s74, 65536\n"
        "v_exp_f32 v83, v83\n"
        "v_exp_f32 v84, v84\n"
        "v_exp_f32 v85, v85\n"
        "v_exp_f32 v86, v86\n"
        "v_mfma_f32_32x32x16_bf16 v[64:79], %[k1], %[qa], %[ma]\n"
        "s_addc_u32 s77, s75, 0\n"
        "v_exp_f32 v87, v87\n"
        "v_exp_f32 v88, v88\n"
        "v_exp_f32 v89, v89\n"
        "v_exp_f32 v90, v90\n"
        "v_smfmac_f32_32x32x32_bf16 %[ob], %[v00], v[80:87], %[idx]\n"
        "s_waitcnt lgkmcnt(0)\n"
        "v_exp_f32 v91, v91\n"
        "v_exp_f32 v92, v92\n"
        "v_exp_f32 v93, v93\n"
        "v_exp_f32 v94, v94\n"
        "v_exp_f32 v95, v95\n"
        "v_exp_f32 v64, v64\n"
        "v_exp_f32 v65, v65\n"
        "v_exp_f32 v66, v66\n"
        "v_smfmac_f32_32x32x32_bf16 %[ob], %[v01], v[88:95], %[idx]\n"
        "ds_read_b128 %[k0], v99 offset:2048\n"
        "v_exp_f32 v67, v67\n"
        "v_exp_f32 v68, v68\n"
        "v_exp_f32 v69, v69\n"
        "v_exp_f32 v70, v70\n"
        "v_mfma_f32_32x32x16_bf16 v[80:95], %[k1], %[qb], %[mb]\n"
        "ds_read_b128 %[v00], v100 offset:2048\n"
        "v_exp_f32 v71, v71\n"
        "v_exp_f32 v72, v72\n"
        "v_exp_f32 v73, v73\n"
        "v_exp_f32 v74, v74\n"
        "v_smfmac_f32_32x32x32_bf16 %[oa], %[v10], v[64:71], %[idx]\n"
        "ds_read_b128 %[v01], v100 offset:2560\n"
        "v_exp_f32 v75, v75\n"
        "v_exp_f32 v76, v76\n"
        "v_exp_f32 v77, v77\n"
        "v_exp_f32 v78, v78\n"
        "v_exp_f32 v79, v79\n"
        "v_exp_f32 v80, v80\n"
        "v_exp_f32 v81, v81\n"
        "v_exp_f32 v82, v82\n"
        "v_smfmac_f32_32x32x32_bf16 %[oa], %[v11], v[72:79], %[idx]\n"
        "s_waitcnt lgkmcnt(2)\n"
        "v_exp_f32 v83, v83\n"
        "v_exp_f32 v84, v84\n"
        "v_exp_f32 v85, v85\n"
        "v_exp_f32 v86, v86\n"
        "v_mfma_f32_32x32x16_bf16 v[64:79], %[k0], %[qa], %[ma]\n"
        "v_exp_f32 v87, v87\n"
        "v_exp_f32 v88, v88\n"
        "v_exp_f32 v89, v89\n"
        "v_exp_f32 v90, v90\n"
        "v_smfmac_f32_32x32x32_bf16 %[ob], %[v10], v[80:87], %[idx]\n"
        "s_waitcnt lgkmcnt(0)\n"
        "s_nop 0\n"
        "v_exp_f32 v91, v91\n"
        "v_exp_f32 v92, v92\n"
        "v_exp_f32 v93, v93\n"
        "v_exp_f32 v94, v94\n"
        "v_exp_f32 v95, v95\n"
        "v_exp_f32 v64, v64\n"
        "v_exp_f32 v65, v65\n"
        "v_exp_f32 v66, v66\n"
        "v_smfmac_f32_32x32x32_bf16 %[ob], %[v11], v[88:95], %[idx]\n"
        "ds_read_b128 %[k1], v99 offset:3072\n"
        "v_exp_f32 v67, v67\n"
        "v_exp_f32 v68, v68\n"
        "v_exp_f32 v69, v69\n"
        "v_exp_f32 v70, v70\n"
        "v_mfma_f32_32x32x16_bf16 v[80:95], %[k0], %[qb], %[mb]\n"
        "ds_read_b128 %[v10], v100 offset:3072\n"
        "v_exp_f32 v71, v71\n"
        "v_exp_f32 v72, v72\n"
        "v_exp_f32 v73, v73\n"
        "v_exp_f32 v74, v74\n"
        "v_smfmac_f32_32x32x32_bf16 %[oa], %[v00], v[64:71], %[idx]\n"
        "ds_read_b128 %[v11], v100 offset:3584\n"
        "v_exp_f32 v75, v75\n"
        "v_exp_f32 v76, v76\n"
        "v_exp_f32 v77, v77\n"
        "v_exp_f32 v78, v78\n"
        "v_exp_f32 v79, v79\n"
        "v_exp_f32 v80, v80\n"
        "v_exp_f32 v81, v81\n"
        "v_exp_f32 v82, v82\n"
        "v_smfmac_f32_32x32x32_bf16 %[oa], %[v01], v[72:79], %[idx]\n"
        "s_waitcnt lgkmcnt(2)\n"
        "v_exp_f32 v83, v83\n"
        "v_exp_f32 v84, v84\n"
        "v_exp_f32 v85, v85\n"
        "v_exp_f32 v86, v86\n"
        "v_mfma_f32_32x32x16_bf16 v[64:79], %[k1], %[qa], %[ma]\n"
        "v_exp_f32 v87, v87\n"
        "v_exp_f32 v88, v88\n"
        "v_exp_f32 v89, v89\n"
        "v_exp_f32 v90, v90\n"
        "v_smfmac_f32_32x32x32_bf16 %[ob], %[v00], v[80:87], %[idx]\n"
        "s_waitcnt lgkmcnt(0)\n"
        "s_nop 0\n"
        "v_exp_f32 v91, v91\n"
        "v_exp_f32 v92, v92\n"
        "v_exp_f32 v93, v93\n"
        "v_exp_f32 v94, v94\n"
        "v_exp_f32 v95, v95\n"
        "v_exp_f32 v64, v64\n"
        "v_exp_f32 v65, v65\n"
        "v_exp_f32 v66, v66\n"
        "v_smfmac_f32_32x32x32_bf16 %[ob], %[v01], v[88:95], %[idx]\n"
        "ds_read_b128 %[k0], v99 offset:4096\n"
        "v_exp_f32 v67, v67\n"
        "v_exp_f32 v68, v68\n"
        "v_exp_f32 v69, v69\n"
        "v_exp_f32 v70, v70\n"
        "v_mfma_f32_32x32x16_bf16 v[80:95], %[k1], %[qb], %[mb]\n"
        "ds_read_b128 %[v00], v100 offset:4096\n"
        "v_exp_f32 v71, v71\n"
        "v_exp_f32 v72, v72\n"
        "v_exp_f32 v73, v73\n"
        "v_exp_f32 v74, v74\n"
        "v_smfmac_f32_32x32x32_bf16 %[oa], %[v10], v[64:71], %[idx]\n"
        "ds_read_b128 %[v01], v100 offset:4608\n"
        "v_exp_f32 v75, v75\n"
        "v_exp_f32 v76, v76\n"
        "v_exp_f32 v77, v77\n"
        "v_exp_f32 v78, v78\n"
        "v_exp_f32 v79, v79\n"
        "v_exp_f32 v80, v80\n"
        "v_exp_f32 v81, v81\n"
        "v_exp_f32 v82, v82\n"
        "v_smfmac_f32_32x32x32_bf16 %[oa], %[v11], v[72:79], %[idx]\n"
        "s_waitcnt lgkmcnt(2)\n"
        "v_exp_f32 v83, v83\n"
        "v_exp_f32 v84, v84\n"
        "v_exp_f32 v85, v85\n"
        "v_exp_f32 v86, v86\n"
        "v_mfma_f32_32x32x16_bf16 v[64:79], %[k0], %[qa], %[ma]\n"
        "v_exp_f32 v87, v87\n"
        "v_exp_f32 v88, v88\n"
        "v_exp_f32 v89, v89\n"
        "v_exp_f32 v90, v90\n"
        "v_smfmac_f32_32x32x32_bf16 %[ob], %[v10], v[80:87], %[idx]\n"
        "s_waitcnt lgkmcnt(0)\n"
        "s_nop 0\n"
        "v_exp_f32 v91, v91\n"
        "v_exp_f32 v92, v92\n"
        "v_exp_f32 v93, v93\n"
        "v_exp_f32 v94, v94\n"
        "v_exp_f32 v95, v95\n"
        "v_exp_f32 v64, v64\n"
        "v_exp_f32 v65, v65\n"
        "v_exp_f32 v66, v66\n"
        "v_smfmac_f32_32x32x32_bf16 %[ob], %[v11], v[88:95], %[idx]\n"
        "ds_read_b128 %[k1], v99 offset:5120\n"
        "v_exp_f32 v67, v67\n"
        "v_exp_f32 v68, v68\n"
        "v_exp_f32 v69, v69\n"
        "v_exp_f32 v70, v70\n"
        "v_mfma_f32_32x32x16_bf16 v[80:95], %[k0], %[qb], %[mb]\n"
        "ds_read_b128 %[v10], v100 offset:5120\n"
        "v_exp_f32 v71, v71\n"
        "v_exp_f32 v72, v72\n"
        "v_exp_f32 v73, v73\n"
        "v_exp_f32 v74, v74\n"
        "v_smfmac_f32_32x32x32_bf16 %[oa], %[v00], v[64:71], %[idx]\n"
        "ds_read_b128 %[v11], v100 offset:5632\n"
        "v_exp_f32 v75, v75\n"
        "v_exp_f32 v76, v76\n"
        "v_exp_f32 v77, v77\n"
        "v_exp_f32 v78, v78\n"
        "v_exp_f32 v79, v79\n"
        "v_exp_f32 v80, v80\n"
        "v_exp_f32 v81, v81\n"
        "v_exp_f32 v82, v82\n"
        "v_smfmac_f32_32x32x32_bf16 %[oa], %[v01], v[72:79], %[idx]\n"
        "s_waitcnt lgkmcnt(2)\n"
        "v_exp_f32 v83, v83\n"
        "v_exp_f32 v84, v84\n"
        "v_exp_f32 v85, v85\n"
        "v_exp_f32 v86, v86\n"
        "v_mfma_f32_32x32x16_bf16 v[64:79], %[k1], %[qa], %[ma]\n"
        "v_exp_f32 v87, v87\n"
        "v_exp_f32 v88, v88\n"
        "v_exp_f32 v89, v89\n"
        "v_exp_f32 v90, v90\n"
        "v_smfmac_f32_32x32x32_bf16 %[ob], %[v00], v[80:87], %[idx]\n"
        "s_waitcnt lgkmcnt(0)\n"
        "s_nop 0\n"
        "v_exp_f32 v91, v91\n"
        "v_exp_f32 v92, v92\n"
        "v_exp_f32 v93, v93\n"
        "v_exp_f32 v94, v94\n"
        "v_exp_f32 v95, v95\n"
        "v_exp_f32 v64, v64\n"
        "v_exp_f32 v65, v65\n"
        "v_exp_f32 v66, v66\n"
        "v_smfmac_f32_32x32x32_bf16 %[ob], %[v01], v[88:95], %[idx]\n"
        "ds_read_b128 %[k0], v99 offset:6144\n"
        "v_exp_f32 v67, v67\n"
        "v_exp_f32 v68, v68\n"
        "v_exp_f32 v69, v69\n"
        "v_exp_f32 v70, v70\n"
        "v_mfma_f32_32x32x16_bf16 v[80:95], %[k1], %[qb], %[mb]\n"
        "ds_read_b128 %[v00], v100 offset:6144\n"
        "v_exp_f32 v71, v71\n"
        "v_exp_f32 v72, v72\n"
        "v_exp_f32 v73, v73\n"
        "v_exp_f32 v74, v74\n"
        "v_smfmac_f32_32x32x32_bf16 %[oa], %[v10], v[64:71], %[idx]\n"
        "ds_read_b128 %[v01], v100 offset:6656\n"
        "v_exp_f32 v75, v75\n"
        "v_exp_f32 v76, v76\n"
        "v_exp_f32 v77, v77\n"
        "v_exp_f32 v78, v78\n"
        "v_exp_f32 v79, v79\n"
        "v_exp_f32 v80, v80\n"
        "v_exp_f32 v81, v81\n"
        "v_exp_f32 v82, v82\n"
        "v_smfmac_f32_32x32x32_bf16 %[oa], %[v11], v[72:79], %[idx]\n"
        "s_waitcnt lgkmcnt(2)\n"
        "v_exp_f32 v83, v83\n"
        "v_exp_f32 v84, v84\n"
        "v_exp_f32 v85, v85\n"
        "v_exp_f32 v86, v86\n"
        "v_mfma_f32_32x32x16_bf16 v[64:79], %[k0], %[qa], %[ma]\n"
        "v_exp_f32 v87, v87\n"
        "v_exp_f32 v88, v88\n"
        "v_exp_f32 v89, v89\n"
        "v_exp_f32 v90, v90\n"
        "v_smfmac_f32_32x32x32_bf16 %[ob], %[v10], v[80:87], %[idx]\n"
        "s_waitcnt lgkmcnt(0)\n"
        "s_nop 0\n"
        "v_exp_f32 v91, v91\n"
        "v_exp_f32 v92, v92\n"
        "v_exp_f32 v93, v93\n"
        "v_exp_f32 v94, v94\n"
        "v_exp_f32 v95, v95\n"
        "v_exp_f32 v64, v64\n"
        "v_exp_f32 v65, v65\n"
        "v_exp_f32 v66, v66\n"
        "v_smfmac_f32_32x32x32_bf16 %[ob], %[v11], v[88:95], %[idx]\n"
        "ds_read_b128 %[k1], v99 offset:7168\n"
        "s_add_u32 s73, s73, 8192\n"
        "v_exp_f32 v67, v67\n"
        "v_exp_f32 v68, v68\n"
        "v_exp_f32 v69, v69\n"
        "v_exp_f32 v70, v70\n"
        "v_mfma_f32_32x32x16_bf16 v[80:95], %[k0], %[qb], %[mb]\n"
        "ds_read_b128 %[v10], v100 offset:7168\n"
        "v_exp_f32 v71, v71\n"
        "v_exp_f32 v72, v72\n"
        "v_exp_f32 v73, v73\n"
        "v_exp_f32 v74, v74\n"
        "v_smfmac_f32_32x32x32_bf16 %[oa], %[v00], v[64:71], %[idx]\n"
        "ds_read_b128 %[v11], v100 offset:7680\n"
        "s_add_u32 s74, s74, 8192\n"
        "v_exp_f32 v75, v75\n"
        "v_exp_f32 v76, v76\n"
        "v_exp_f32 v77, v77\n"
        "v_exp_f32 v78, v78\n"
        "v_exp_f32 v79, v79\n"
        "v_exp_f32 v80, v80\n"
        "v_exp_f32 v81, v81\n"
        "v_exp_f32 v82, v82\n"
        "v_smfmac_f32_32x32x32_bf16 %[oa], %[v01], v[72:79], %[idx]\n"
        "s_waitcnt lgkmcnt(2)\n"
        "s_addc_u32 s75, s75, 0\n"
        "v_exp_f32 v83, v83\n"
        "v_exp_f32 v84, v84\n"
        "v_exp_f32 v85, v85\n"
        "v_exp_f32 v86, v86\n"
        "v_mfma_f32_32x32x16_bf16 v[64:79], %[k1], %[qa], %[ma]\n"
        "v_exp_f32 v87, v87\n"
        "v_exp_f32 v88, v88\n"
        "v_exp_f32 v89, v89\n"
        "v_exp_f32 v90, v90\n"
        "v_smfmac_f32_32x32x32_bf16 %[ob], %[v00], v[80:87], %[idx]\n"
        "s_waitcnt lgkmcnt(0)\n"
        "s_nop 0\n"
        "v_exp_f32 v91, v91\n"
        "v_exp_f32 v92, v92\n"
        "v_exp_f32 v93, v93\n"
        "v_exp_f32 v94, v94\n"
        "v_exp_f32 v95, v95\n"
        "v_exp_f32 v64, v64\n"
        "v_exp_f32 v65, v65\n"
        "v_exp_f32 v66, v66\n"
        "v_smfmac_f32_32x32x32_bf16 %[ob], %[v01], v[88:95], %[idx]\n"
        "ds_read_b128 %[k0], v99 offset:8192\n"
        "v_exp_f32 v67, v67\n"
        "v_exp_f32 v68, v68\n"
        "v_exp_f32 v69, v69\n"
        "v_exp_f32 v70, v70\n"
        "v_mfma_f32_32x32x16_bf16 v[80:95], %[k1], %[qb], %[mb]\n"
        "ds_read_b128 %[v00], v100 offset:8192\n"
        "v_exp_f32 v71, v71\n"
        "v_exp_f32 v72, v72\n"
        "v_exp_f32 v73, v73\n"
        "v_exp_f32 v74, v74\n"
        "v_smfmac_f32_32x32x32_bf16 %[oa], %[v10], v[64:71], %[idx]\n"
        "ds_read_b128 %[v01], v100 offset:8704\n"
        "v_exp_f32 v75, v75\n"
        "v_exp_f32 v76, v76\n"
        "v_exp_f32 v77, v77\n"
        "v_exp_f32 v78, v78\n"
        "v_exp_f32 v79, v79\n"
        "v_exp_f32 v80, v80\n"
        "v_exp_f32 v81, v81\n"
        "v_exp_f32 v82, v82\n"
        "v_smfmac_f32_32x32x32_bf16 %[oa], %[v11], v[72:79], %[idx]\n"
        "s_waitcnt lgkmcnt(2)\n"
        "v_exp_f32 v83, v83\n"
        "v_exp_f32 v84, v84\n"
        "v_exp_f32 v85, v85\n"
        "v_exp_f32 v86, v86\n"
        "v_mfma_f32_32x32x16_bf16 v[64:79], %[k0], %[qa], %[ma]\n"
        "v_exp_f32 v87, v87\n"
        "v_exp_f32 v88, v88\n"
        "v_exp_f32 v89, v89\n"
        "v_exp_f32 v90, v90\n"
        "v_smfmac_f32_32x32x32_bf16 %[ob], %[v10], v[80:87], %[idx]\n"
        "s_waitcnt lgkmcnt(0)\n"
        "s_nop 0\n"
        "v_exp_f32 v91, v91\n"
        "v_exp_f32 v92, v92\n"
        "v_exp_f32 v93, v93\n"
        "v_exp_f32 v94, v94\n"
        "v_exp_f32 v95, v95\n"
        "s_nop 1\n"
        "v_smfmac_f32_32x32x32_bf16 %[ob], %[v11], v[88:95], %[idx]\n"
        "s_nop 7\n"
        "s_nop 7\n"
                    ATT_OUT : [ma] "v"(ma), [mb] "v"(mb), ATT_IN);
                SBAR();
            }
            const float ra = __builtin_amdgcn_rcpf(oa[8]), rb = __builtin_amdgcn_rcpf(ob[8]);
            st16x2(o_st, voff, u32x4{pk2(oa[0] * ra, oa[1] * ra), pk2(oa[2] * ra, oa[3] * ra), pk2(oa[4] * ra, oa[5] * ra), pk2(oa[6] * ra, oa[7] * ra)},
                   u32x4{pk2(ob[0] * rb, ob[1] * rb), pk2(ob[2] * rb, ob[3] * rb), pk2(ob[4] * rb, ob[5] * rb), pk2(ob[6] * rb, ob[7] * rb)});
        }
        }
        {
            CTX();
            o_src = ws + (unsigned)(WS_O + (l * LS) * 2097152 + ((T64 * 2 + ot) * 32 + wid * 2) * 1024);
            o_own = ws + (unsigned)(WS_O + (l * LS) * 2097152 + ((T64 * 2 + (wid >> 1)) * 32 + c8 * 4) * 1024); o_ln = lane * 128;
            o_lds = lds0 + 98304 + wid * 2048; o_rd = lds0 + 98304 + voff;
            x1_st = ws + (unsigned)(WS_X + (2 * l * LS + 1) * 2097152 + ((T64 * 2 + ot) * 32 + on * 2) * 1024);
            x1_ps = ws + (unsigned)(WS_PST + (2 * l + 1) * 262144 + (T64 * 2 + ot) * 4096 + on * 256);
            x1_fl = ws + (unsigned)(FL_X1 + l * 8192 + G * 512); cs_src = ws + (unsigned)(WS_CS13 + ng * 896 + wid * 256);
            voff1 = voff; lane1 = lane; wid1 = wid;
            asm volatile("" : "+s"(o_own), "+s"(o_src), "+s"(o_lds), "+s"(x1_st), "+s"(x1_ps), "+s"(x1_fl), "+s"(cs_src), "+s"(wid1));
            asm volatile("" : "+v"(o_ln), "+v"(o_rd), "+v"(voff1), "+v"(lane1));
            arrive_block(o_fl, wid1);
        }
        float c0_, c1_, c2_, c3_;
        {
            const int l = l0; (void)l;
            f32x16 c0;
            SBAR();
            asm volatile(
        "s_sub_u32 s72, %[wid], 1\n"
        "s_cmp_lt_u32 s72, 2\n"
        "s_cbranch_scc0 7f\n"
        "s_mov_b32 exec_hi, 0\n"
        "global_load_dword v127, %[vln], %[own] sc1\n"
        "s_mov_b32 exec_hi, -1\n"
        "7:\n"
        "s_cmp_lg_u32 %[wid], 0\n"
        "s_cbranch_scc1 2f\n"
        "s_mov_b32 s72, 0x100000\n"
        "v_mov_b32 v65, 0\n"
        "1:\n"
        "global_load_dword v64, v65, %[sfl] sc1\n"
        "s_waitcnt vmcnt(0)\n"
        "v_readfirstlane_b32 s73, v64\n"
        "s_cmp_ge_u32 s73, 8\n"
        "s_cbranch_scc1 2f\n"
        "s_sub_u32 s72, s72, 1\n"
        "s_cbranch_scc0 1b\n"
        "2:\n"
        "s_waitcnt lgkmcnt(0)\n"
        "s_barrier\n"
        "s_mov_b64 s[74:75], %[so]\n"
        "s_mov_b32 m0, %[mo]\n"
        "s_nop 0\n"
        "global_load_lds_dwordx4 %[voff], s[74:75] offset:0 sc1\n"
        "global_load_lds_dwordx4 %[voff], s[74:75] offset:1024 sc1\n"
        "s_add_u32 s74, s74, 8192\n"
        "s_addc_u32 s75, s75, 0\n"
        "s_add_u32 m0, m0, 8192\n"
        "s_nop 0\n"
        "global_load_lds_dwordx4 %[voff], s[74:75] offset:0 sc1\n"
        "global_load_lds_dwordx4 %[voff], s[74:75] offset:1024 sc1\n"
        "s_add_u32 s74, s74, 8192\n"
        "s_addc_u32 s75, s75, 0\n"
        "s_add_u32 m0, m0, 8192\n"
        "s_nop 0\n"
        "global_load_lds_dwordx4 %[voff], s[74:75] offset:0 sc1\n"
        "global_load_lds_dwordx4 %[voff], s[74:75] offset:1024 sc1\n"
        "s_add_u32 s74, s74, 8192\n"
        "s_addc_u32 s75, s75, 0\n"
        "s_add_u32 m0, m0, 8192\n"
        "s_nop 0\n"
        "global_load_lds_dwordx4 %[voff], s[74:75] offset:0 sc1\n"
        "global_load_lds_dwordx4 %[voff], s[74:75] offset:1024 sc1\n"
        "s_add_u32 s76, %[w13l], 32768\n"
        "s_addc_u32 s77, %[w13h], 0\n"
        "s_add_u32 m0, %[m13], 32768\n"
        "s_waitcnt vmcnt(6)\n"
        "s_barrier\n"
        "ds_read_b128 v[96:99], %[vo] offset:0\n"
        "ds_read_b128 v[100:103], %[vo] offset:1024\n"
        "ds_read_b128 v[104:107], %[vo] offset:2048\n"
        "ds_read_b128 v[108:111], %[vo] offset:3072\n"
        "ds_read_b128 v[112:115], %[vo] offset:4096\n"
        "ds_read_b128 v[116:119], %[vo] offset:5120\n"
        "ds_read_b128 v[120:123], %[vo] offset:6144\n"
        "ds_read_b128 v[124:127], %[vo] offset:7168\n"
        "s_waitcnt lgkmcnt(7)\n"
        "v_mfma_f32_32x32x16_bf16 v[64:79], a[0:3], v[96:99], 0\n"
        "s_waitcnt lgkmcnt(6)\n"
        "v_mfma_f32_32x32x16_bf16 v[80:95], a[4:7], v[100:103], 0\n"
        "s_waitcnt lgkmcnt(5)\n"
        "v_mfma_f32_32x32x16_bf16 v[64:79], a[8:11], v[104:107], v[64:79]\n"
        "s_waitcnt vmcnt(4)\n"
        "s_barrier\n"
        "ds_read_b128 v[160:163], %[vo] offset:8192\n"
        "ds_read_b128 v[164:167], %[vo] offset:9216\n"
        "ds_read_b128 v[168:171], %[vo] offset:10240\n"
        "ds_read_b128 v[172:175], %[vo] offset:11264\n"
        "ds_read_b128 v[176:179], %[vo] offset:12288\n"
        "ds_read_b128 v[180:183], %[vo] offset:13312\n"
        "ds_read_b128 v[184:187], %[vo] offset:14336\n"
        "ds_read_b128 v[188:191], %[vo] offset:15360\n"
        "s_waitcnt lgkmcnt(12)\n"
        "v_mfma_f32_32x32x16_bf16 v[80:95], a[12:15], v[108:111], v[80:95]\n"
        "s_waitcnt lgkmcnt(11)\n"
        "v_mfma_f32_32x32x16_bf16 v[64:79], a[16:19], v[112:115], v[64:79]\n"
        "s_waitcnt lgkmcnt(10)\n"
        "v_mfma_f32_32x32x16_bf16 v[80:95], a[20:23], v[116:119], v[80:95]\n"
        "s_waitcnt lgkmcnt(9)\n"
        "v_mfma_f32_32x32x16_bf16 v[64:79], a[24:27], v[120:123], v[64:79]\n"
        "s_waitcnt lgkmcnt(8)\n"
        "v_mfma_f32_32x32x16_bf16 v[80:95], a[28:31], v[124:127], v[80:95]\n"
        "s_waitcnt lgkmcnt(7)\n"
        "v_mfma_f32_32x32x16_bf16 v[64:79], a[32:35], v[160:163], v[64:79]\n"
        "s_waitcnt lgkmcnt(6)\n"
        "v_mfma_f32_32x32x16_bf16 v[80:95], a[36:39], v[164:167], v[80:95]\n"
        "s_waitcnt lgkmcnt(5)\n"
        "v_mfma_f32_32x32x16_bf16 v[64:79], a[40:43], v[168:171], v[64:79]\n"
        "s_waitcnt vmcnt(2)\n"
        "s_barrier\n"
        "ds_read_b128 v[96:99], %[vo] offset:16384\n"
        "ds_read_b128 v[100:103], %[vo] offset:17408\n"
        "ds_read_b128 v[104:107], %[vo] offset:18432\n"
        "ds_read_b128 v[108:111], %[vo] offset:19456\n"
        "ds_read_b128 v[112:115], %[vo] offset:20480\n"
        "ds_read_b128 v[116:119], %[vo] offset:21504\n"
        "ds_read_b128 v[120:123], %[vo] offset:22528\n"
        "ds_read_b128 v[124:127], %[vo] offset:23552\n"
        "s_waitcnt lgkmcnt(12)\n"
        "v_mfma_f32_32x32x16_bf16 v[80:95], a[44:47], v[172:175], v[80:95]\n"
        "s_waitcnt lgkmcnt(11)\n"
        "v_mfma_f32_32x32x16_bf16 v[64:79], a[48:51], v[176:179], v[64:79]\n"
        "s_waitcnt lgkmcnt(10)\n"
        "v_mfma_f32_32x32x16_bf16 v[80:95], a[52:55], v[180:183], v[80:95]\n"
        "s_waitcnt lgkmcnt(9)\n"
        "v_mfma_f32_32x32x16_bf16 v[64:79], a[56:59], v[184:187], v[64:79]\n"
        "s_waitcnt lgkmcnt(8)\n"
        "v_mfma_f32_32x32x16_bf16 v[80:95], a[60:63], v[188:191], v[80:95]\n"
        "s_waitcnt lgkmcnt(7)\n"
        "v_mfma_f32_32x32x16_bf16 v[64:79], a[64:67], v[96:99], v[64:79]\n"
        "s_waitcnt lgkmcnt(6)\n"
        "v_mfma_f32_32x32x16_bf16 v[80:95], a[68:71], v[100:103], v[80:95]\n"
        "global_load_lds_dwordx4 %[voff], s[76:77] offset:0 sc1\n"
        "s_waitcnt lgkmcnt(5)\n"
        "v_mfma_f32_32x32x16_bf16 v[64:79], a[72:75], v[104:107], v[64:79]\n"
        "s_waitcnt vmcnt(1)\n"
        "s_barrier\n"
        "ds_read_b128 v[160:163], %[vo] offset:24576\n"
        "ds_read_b128 v[164:167], %[vo] offset:25600\n"
        "ds_read_b128 v[168:171], %[vo] offset:26624\n"
        "ds_read_b128 v[172:175], %[vo] offset:27648\n"
        "ds_read_b128 v[176:179], %[vo] offset:28672\n"
        "ds_read_b128 v[180:183], %[vo] offset:29696\n"
        "ds_read_b128 v[184:187], %[vo] offset:30720\n"
        "ds_read_b128 v[188:191], %[vo] offset:31744\n"
        "s_waitcnt lgkmcnt(12)\n"
        "v_mfma_f32_32x32x16_bf16 v[80:95], a[76:79], v[108:111], v[80:95]\n"
        "global_load_lds_dwordx4 %[voff], s[76:77] offset:1024 sc1\n"
        "s_waitcnt lgkmcnt(11)\n"
        "v_mfma_f32_32x32x16_bf16 v[64:79], a[80:83], v[112:115], v[64:79]\n"
        "s_waitcnt lgkmcnt(10)\n"
        "v_mfma_f32_32x32x16_bf16 v[80:95], a[84:87], v[116:119], v[80:95]\n"
        "global_load_lds_dwordx4 %[voff], s[76:77] offset:2048 sc1\n"
        "s_waitcnt lgkmcnt(9)\n"
        "v_mfma_f32_32x32x16_bf16 v[64:79], a[88:91], v[120:123], v[64:79]\n"
        "s_waitcnt lgkmcnt(8)\n"
        "v_mfma_f32_32x32x16_bf16 v[80:95], a[92:95], v[124:127], v[80:95]\n"
        "global_load_lds_dwordx4 %[voff], s[76:77] offset:3072 sc1\n"
        "s_waitcnt lgkmcnt(7)\n"
        "v_mfma_f32_32x32x16_bf16 v[64:79], a[96:99], v[160:163], v[64:79]\n"
        "s_waitcnt lgkmcnt(6)\n"
        "v_mfma_f32_32x32x16_bf16 v[80:95], a[100:103], v[164:167], v[80:95]\n"
        "s_add_u32 s76, s76, 4096\n"
        "s_addc_u32 s77, s77, 0\n"
        "s_add_u32 m0, m0, 4096\n"
        "s_nop 0\n"
        "global_load_lds_dwordx4 %[voff], s[76:77] offset:0 sc1\n"
        "s_waitcnt lgkmcnt(5)\n"
        "v_mfma_f32_32x32x16_bf16 v[64:79], a[104:107], v[168:171], v[64:79]\n"
        "s_waitcnt lgkmcnt(4)\n"
        "v_mfma_f32_32x32x16_bf16 v[80:95], a[108:111], v[172:175], v[80:95]\n"
        "global_load_lds_dwordx4 %[voff], s[76:77] offset:1024 sc1\n"
        "s_waitcnt lgkmcnt(3)\n"
        "v_mfma_f32_32x32x16_bf16 v[64:79], a[112:115], v[176:179], v[64:79]\n"
        "s_waitcnt lgkmcnt(2)\n"
        "v_mfma_f32_32x32x16_bf16 v[80:95], a[116:119], v[180:183], v[80:95]\n"
        "global_load_lds_dwordx4 %[voff], s[76:77] offset:2048 sc1\n"
        "s_waitcnt lgkmcnt(1)\n"
        "v_mfma_f32_32x32x16_bf16 v[64:79], a[120:123], v[184:187], v[64:79]\n"
        "s_waitcnt lgkmcnt(0)\n"
        "v_mfma_f32_32x32x16_bf16 v[80:95], a[124:127], v[188:191], v[80:95]\n"
        "global_load_lds_dwordx4 %[voff], s[76:77] offset:3072 sc1\n"
        "s_nop 7\n"
        "s_nop 3\n"
        "v_pk_add_f32 v[64:65], v[64:65], v[80:81]\n"
        "v_pk_add_f32 v[66:67], v[66:67], v[82:83]\n"
        "v_pk_add_f32 v[68:69], v[68:69], v[84:85]\n"
        "v_pk_add_f32 v[70:71], v[70:71], v[86:87]\n"
        "v_pk_add_f32 v[72:73], v[72:73], v[88:89]\n"
        "v_pk_add_f32 v[74:75], v[74:75], v[90:91]\n"
        "v_pk_add_f32 v[76:77], v[76:77], v[92:93]\n"
        "v_pk_add_f32 v[78:79], v[78:79], v[94:95]\n"
                : "=&{v[64:79]}"(c0)
                : "{a[0:31]}"(woa), "{a[32:63]}"(wob), "{a[64:95]}"(woc), "{a[96:127]}"(wod), [voff] "v"(voff1), [wid] "s"(wid1),
                  [sfl] "s"(o_fl), [vln] "v"(o_ln), [own] "s"(o_own), [vo] "v"(o_rd), [so] "s"(o_src), [mo] "s"(o_lds), [w13l] "s"(w13l), [w13h] "s"(w13h), [m13] "s"(m13)
                : "memory", "scc", "vcc", "s72", "s73", "s74", "s75", "s76", "s77", "s78", "s79", "s80", "s81", "s82", "s83", "s84", "s85", "v80", "v81", "v82", "v83", "v84", "v85", "v86", "v87", "v88", "v89", "v90", "v91", "v92", "v93", "v94", "v95", "v96", "v97", "v98", "v99", "v100", "v101", "v102", "v103", "v104", "v105", "v106", "v107", "v108", "v109", "v110", "v111", "v112", "v113", "v114", "v115", "v116", "v117", "v118", "v119", "v120", "v121", "v122", "v123", "v124", "v125", "v126", "v127", "v160", "v161", "v162", "v163", "v164", "v165", "v166", "v167", "v168", "v169", "v170", "v171", "v172", "v173", "v174", "v175", "v176", "v177", "v178", "v179", "v180", "v181", "v182", "v183", "v184", "v185", "v186", "v187", "v188", "v189", "v190", "v191");
            SBAR();
            float y[16];
#pragma unroll
            for (int i = 0; i < 16; i++) y[i] = c0[i];
            res_add(y, xr);
            st16x2(x1_st, voff1, u32x4{xr[0], xr[1], xr[2], xr[3]}, u32x4{xr[4], xr[5], xr[6], xr[7]});
            float s1 = 0.f, s2 = 0.f;
#pragma unroll
            for (int i = 0; i < 8; i++) { const float a = bflo(xr[i]), b = bfhi(xr[i]); s1 += a + b; s2 += a * a + b * b; }
            s1 = xsum(s1); s2 = xsum(s2);
            asm volatile("" : "+v"(xr[0]), "+v"(xr[1]), "+v"(xr[2]), "+v"(xr[3]), "+v"(xr[4]), "+v"(xr[5]), "+v"(xr[6]), "+v"(xr[7]));
            asm volatile("s_mov_b32 exec_hi, 0\n\tglobal_store_dwordx2 %4, %5, %6 sc1\n\ts_mov_b32 exec_hi, -1\n\t"
                         "global_load_dword %0, %7, %8 sc1\n\tglobal_load_dword %1, %7, %9 sc1\n\tglobal_load_dword %2, %7, %10 sc1\n\tglobal_load_dword %3, %7, %11 sc1"
                         : "=&v"(c0_), "=&v"(c1_), "=&v"(c2_), "=&v"(c3_)
                         : "v"(lane1 * 8), "v"(u32x2{__float_as_uint(s1), __float_as_uint(s2)}), "s"(x1_ps), "v"(lane1 * 4),
                           "s"(cs_src), "s"(cs_src + 14336), "s"(cs_src + 28672), "s"(cs_src + 43008) : "memory");
        }
        f32x16 c0d;
        u32x8 xd;
        const char* fin_st;
        const char* fin_fl;
        unsigned fin_r1, fin_r2, fin_r3, fin_v;
        {
            CTX();
            const unsigned gt = Gg * 4 + wid;
            const uint64_t w2u0 = (uint64_t)(w2p + nsub * 114688 + 16384), w2u1 = (uint64_t)(w2p + (nsub ^ 1) * 114688 + 16384);
            const uint64_t hh = (uint64_t)(ws + (unsigned)(WS_H + (l * LS) * 7340032 + ((T64 * 2) * 112 + wid * 28) * 1024));
            const uint64_t h0 = hh + tsub * 114688, h1 = hh + (tsub ^ 1) * 114688;
            unsigned t0l = (unsigned)w2u0, t0h = (unsigned)(w2u0 >> 32), t1l = (unsigned)w2u1, t1h = (unsigned)(w2u1 >> 32);
            unsigned a0l = (unsigned)h0, a0h = (unsigned)(h0 >> 32), a1l = (unsigned)h1, a1h = (unsigned)(h1 >> 32);
            unsigned mwa = lds0 + 131072 + wid * 7168, mwb = lds0 + 98304 + wid * 8192;
            const char* sfl = ws + (unsigned)(FL_X1 + l * 8192 + Gg * 512 + (GU_MAP == 1 ? (xcd >> 1) * 128 : GU_MAP == 2 ? (xcd >> 2) * 128 : 0));
            const char* sst = ws + (unsigned)(WS_PST + (2 * l + 1) * 262144 + gt * 4096);
            const char* sx = ws + (unsigned)(WS_X + (2 * l * LS + 1) * 2097152 + gt * 32768 + 4096);
            const char* sh = ws + (unsigned)(WS_H + (l * LS) * 7340032 + (gt * 112 + ng * 7 + 3) * 1024);
            const char* sxr = ws + (unsigned)(WS_X + (2 * l * LS + 1) * 2097152 + xoff);
            const char *sfh = ws + (unsigned)(FL_H + l * 2048 + Gg * 128), *sfe = ws + (unsigned)(FL_H + l * 2048 + G * 128);
            fin_st = l ? (const char*)out + (tt * 32 * D_ + (c8 * 2 + nsub) * 32) * 2 : ws + (unsigned)(WS_X + (2 * LS) * 2097152 + xoff);
            fin_st = uni(fin_st); fin_fl = ws + (unsigned)(FL_X2 + T64 * 128);
            unsigned vl0 = lds0 + voff, vl1 = lds0 + 65536 + voff, vcs = lds0 + L_CS + (lane & 48), vx = h * 1024 + ((lane & 15) + 2 * (lane & 16)) * 16;
            unsigned vh = ((lane & 15) + 16 * h + 2 * (lane & 16)) * 16, vbp = ((lane & 15) >> 1) * 4, vodd = lane & 1, vcw = lds0 + L_CS + tid * 4;
            unsigned vla = mwa + voff, vlb = mwb + voff, vlc = m13 + voff;
            unsigned vwq = (lane >> 3) * 1024 + (((lane & 7) ^ (((lane >> 4) & 1) << 2)) * 16), mwq = lds0 + 32768 + wid * 16384;
            const char* swq = (const char*)wq + c8 * 128 + wid * 131072;
            fin_r1 = lds0 + (wid ^ 1) * 8192 + voff; fin_r2 = lds0 + (wid ^ 2) * 8192 + 4096 + voff; fin_r3 = lds0 + 98304 + (wid ^ 3) * 8192 + voff;
            fin_v = r * (D_ * 2) + h * 8;
            asm volatile("" : "+s"(t0l), "+s"(t0h), "+s"(t1l), "+s"(t1h), "+s"(a0l), "+s"(a0h), "+s"(a1l), "+s"(a1h), "+s"(mwa),
                              "+s"(mwb), "+s"(sfl), "+s"(sst), "+s"(sx), "+s"(sh), "+s"(sfh), "+s"(sfe), "+s"(fin_st), "+s"(fin_fl), "+s"(swq), "+s"(mwq), "+s"(sxr));
            asm volatile("" : "+v"(vl0), "+v"(vl1), "+v"(vcs), "+v"(vx), "+v"(vh), "+v"(vbp), "+v"(vodd), "+v"(vcw), "+v"(vla), "+v"(vlb), "+v"(vlc),
                              "+v"(vwq), "+v"(fin_r1), "+v"(fin_r2), "+v"(fin_r3), "+v"(fin_v));
            if (GU_MAP) arrive_block4(x1_fl, wid, lane1); else arrive_block(x1_fl, wid);
            asm volatile("" : "+v"(c0_), "+v"(c1_), "+v"(c2_), "+v"(c3_));
            *(LAS float*)(uintptr_t)vcw = (c0_ + c1_) + (c2_ + c3_);
            u32x4 w2e;
            unsigned early;
            SBAR();
            asm volatile(
        "s_cmp_lg_u32 %[wid], 0\n"
        "s_cbranch_scc1 2f\n"
        "s_mov_b32 s72, 0x100000\n"
        "v_mov_b32 v65, 0\n"
        "1:\n"
        "global_load_dword v64, v65, %[sfl] sc1\n"
        "s_waitcnt vmcnt(0)\n"
        "v_readfirstlane_b32 s73, v64\n"
        "s_cmp_ge_u32 s73, 16\n"
        "s_cbranch_scc1 2f\n"
        "s_sub_u32 s72, s72, 1\n"
        "s_cbranch_scc0 1b\n"
        "2:\n"
        "s_waitcnt vmcnt(0) lgkmcnt(0)\n"
        "s_barrier\n"
        "s_mov_b64 s[74:75], %[sx]\n"
        "global_load_dwordx4 a[0:3], %[vx], s[74:75] offset:-4096 sc1\n"
        "global_load_dwordx4 a[4:7], %[vx], s[74:75] offset:-3840 sc1\n"
        "global_load_dwordx4 a[8:11], %[vx], s[74:75] offset:-2048 sc1\n"
        "global_load_dwordx4 a[12:15], %[vx], s[74:75] offset:-1792 sc1\n"
        "global_load_dwordx4 a[16:19], %[vx], s[74:75] offset:0 sc1\n"
        "global_load_dwordx4 a[20:23], %[vx], s[74:75] offset:256 sc1\n"
        "global_load_dwordx4 a[24:27], %[vx], s[74:75] offset:2048 sc1\n"
        "global_load_dwordx4 a[28:31], %[vx], s[74:75] offset:2304 sc1\n"
        "s_add_u32 s74, s74, 8192\n"
        "s_addc_u32 s75, s75, 0\n"
        "global_load_dwordx4 a[32:35], %[vx], s[74:75] offset:-4096 sc1\n"
        "global_load_dwordx4 a[36:39], %[vx], s[74:75] offset:-3840 sc1\n"
        "global_load_dwordx4 v[64:67], %[voff], %[sst] offset:0 sc1\n"
        "global_load_dwordx4 v[68:71], %[voff], %[sst] offset:1024 sc1\n"
        "global_load_dwordx4 v[72:75], %[voff], %[sst] offset:2048 sc1\n"
        "global_load_dwordx4 v[76:79], %[voff], %[sst] offset:3072 sc1\n"
        "ds_read_b128 v[96:99], %[vl0] offset:0\n"
        "ds_read_b128 v[100:103], %[vl0] offset:1024\n"
        "ds_read_b128 v[104:107], %[vl0] offset:2048\n"
        "ds_read_b128 v[108:111], %[vl0] offset:3072\n"
        "ds_read_b128 v[112:115], %[vl0] offset:4096\n"
        "ds_read_b128 v[116:119], %[vl0] offset:5120\n"
        "ds_read_b128 v[120:123], %[vl0] offset:6144\n"
        "ds_read_b128 v[124:127], %[vl0] offset:7168\n"
        "s_waitcnt vmcnt(12)\n"
        "s_waitcnt lgkmcnt(5)\n"
        "v_mfma_f32_16x16x32_bf16 v[128:131], v[96:99], a[0:3], 0\n"
        "ds_read_b128 v[56:59], %[vl0] offset:8192\n"
        "ds_read_b128 v[60:63], %[vl0] offset:9216\n"
        "v_mfma_f32_16x16x32_bf16 v[132:135], v[96:99], a[4:7], 0\n"
        "global_load_dwordx4 a[40:43], %[vx], s[74:75] offset:-2048 sc1\n"
        "v_mfma_f32_16x16x32_bf16 v[136:139], v[100:103], a[0:3], 0\n"
        "ds_read_b128 v[96:99], %[vl0] offset:10240\n"
        "v_mfma_f32_16x16x32_bf16 v[140:143], v[100:103], a[4:7], 0\n"
        "v_mfma_f32_16x16x32_bf16 v[144:147], v[104:107], a[0:3], 0\n"
        "ds_read_b128 v[100:103], %[vl0] offset:11264\n"
        "v_mfma_f32_16x16x32_bf16 v[148:151], v[104:107], a[4:7], 0\n"
        "s_waitcnt lgkmcnt(6)\n"
        "v_mfma_f32_16x16x32_bf16 v[152:155], v[108:111], a[0:3], 0\n"
        "ds_read_b128 v[104:107], %[vl0] offset:12288\n"
        "v_mfma_f32_16x16x32_bf16 v[156:159], v[108:111], a[4:7], 0\n"
        "global_load_dwordx4 a[44:47], %[vx], s[74:75] offset:-1792 sc1\n"
        "v_mfma_f32_16x16x32_bf16 v[160:163], v[112:115], a[0:3], 0\n"
        "ds_read_b128 v[108:111], %[vl0] offset:13312\n"
        "v_mfma_f32_16x16x32_bf16 v[164:167], v[112:115], a[4:7], 0\n"
        "v_mfma_f32_16x16x32_bf16 v[168:171], v[116:119], a[0:3], 0\n"
        "ds_read_b128 v[112:115], %[vl0] offset:14336\n"
        "v_mfma_f32_16x16x32_bf16 v[172:175], v[116:119], a[4:7], 0\n"
        "s_waitcnt vmcnt(12)\n"
        "s_waitcnt lgkmcnt(6)\n"
        "v_mfma_f32_16x16x32_bf16 v[128:131], v[120:123], a[8:11], v[128:131]\n"
        "ds_read_b128 v[116:119], %[vl0] offset:15360\n"
        "v_mfma_f32_16x16x32_bf16 v[132:135], v[120:123], a[12:15], v[132:135]\n"
        "global_load_dwordx4 a[48:51], %[vx], s[74:75] offset:0 sc1\n"
        "v_mfma_f32_16x16x32_bf16 v[136:139], v[124:127], a[8:11], v[136:139]\n"
        "ds_read_b128 v[120:123], %[vl0] offset:16384\n"
        "v_mfma_f32_16x16x32_bf16 v[140:143], v[124:127], a[12:15], v[140:143]\n"
        "v_mfma_f32_16x16x32_bf16 v[144:147], v[56:59], a[8:11], v[144:147]\n"
        "ds_read_b128 v[124:127], %[vl0] offset:17408\n"
        "v_mfma_f32_16x16x32_bf16 v[148:151], v[56:59], a[12:15], v[148:151]\n"
        "s_waitcnt lgkmcnt(6)\n"
        "v_mfma_f32_16x16x32_bf16 v[152:155], v[60:63], a[8:11], v[152:155]\n"
        "ds_read_b128 v[56:59], %[vl0] offset:18432\n"
        "v_mfma_f32_16x16x32_bf16 v[156:159], v[60:63], a[12:15], v[156:159]\n"
        "global_load_dwordx4 a[52:55], %[vx], s[74:75] offset:256 sc1\n"
        "v_mfma_f32_16x16x32_bf16 v[160:163], v[96:99], a[8:11], v[160:163]\n"
        "ds_read_b128 v[60:63], %[vl0] offset:19456\n"
        "v_mfma_f32_16x16x32_bf16 v[164:167], v[96:99], a[12:15], v[164:167]\n"
        "s_add_u32 s76, %[sw_lo], 98304\n"
        "s_addc_u32 s77, %[sw_hi], 0\n"
        "s_add_u32 m0, %[sm], 98304\n"
        "s_nop 0\n"
        "global_load_lds_dwordx4 %[voff], s[76:77] offset:0 sc1\n"
        "v_mfma_f32_16x16x32_bf16 v[168:171], v[100:103], a[8:11], v[168:171]\n"
        "ds_read_b128 v[96:99], %[vl0] offset:20480\n"
        "v_mfma_f32_16x16x32_bf16 v[172:175], v[100:103], a[12:15], v[172:175]\n"
        "s_waitcnt vmcnt(13)\n"
        "s_waitcnt lgkmcnt(6)\n"
        "v_mfma_f32_16x16x32_bf16 v[128:131], v[104:107], a[16:19], v[128:131]\n"
        "ds_read_b128 v[100:103], %[vl0] offset:21504\n"
        "v_mfma_f32_16x16x32_bf16 v[132:135], v[104:107], a[20:23], v[132:135]\n"
        "global_load_dwordx4 a[56:59], %[vx], s[74:75] offset:2048 sc1\n"
        "v_mfma_f32_16x16x32_bf16 v[136:139], v[108:111], a[16:19], v[136:139]\n"
        "ds_read_b128 v[104:107], %[vl0] offset:22528\n"
        "v_mfma_f32_16x16x32_bf16 v[140:143], v[108:111], a[20:23], v[140:143]\n"
        "v_mfma_f32_16x16x32_bf16 v[144:147], v[112:115], a[16:19], v[144:147]\n"
        "ds_read_b128 v[108:111], %[vl0] offset:23552\n"
        "v_mfma_f32_16x16x32_bf16 v[148:151], v[112:115], a[20:23], v[148:151]\n"
        "s_waitcnt lgkmcnt(6)\n"
        "v_mfma_f32_16x16x32_bf16 v[152:155], v[116:119], a[16:19], v[152:155]\n"
        "ds_read_b128 v[112:115], %[vl0] offset:24576\n"
        "v_mfma_f32_16x16x32_bf16 v[156:159], v[116:119], a[20:23], v[156:159]\n"
        "global_load_dwordx4 a[60:63], %[vx], s[74:75] offset:2304 sc1\n"
        "v_mfma_f32_16x16x32_bf16 v[160:163], v[120:123], a[16:19], v[160:163]\n"
        "ds_read_b128 v[116:119], %[vl0] offset:25600\n"
        "v_mfma_f32_16x16x32_bf16 v[164:167], v[120:123], a[20:23], v[164:167]\n"
        "global_load_lds_dwordx4 %[voff], s[76:77] offset:1024 sc1\n"
        "v_mfma_f32_16x16x32_bf16 v[168:171], v[124:127], a[16:19], v[168:171]\n"
        "ds_read_b128 v[120:123], %[vl0] offset:26624\n"
        "v_mfma_f32_16x16x32_bf16 v[172:175], v[124:127], a[20:23], v[172:175]\n"
        "s_waitcnt vmcnt(14)\n"
        "s_waitcnt lgkmcnt(6)\n"
        "v_mfma_f32_16x16x32_bf16 v[128:131], v[56:59], a[24:27], v[128:131]\n"
        "ds_read_b128 v[124:127], %[vl0] offset:27648\n"
        "v_mfma_f32_16x16x32_bf16 v[132:135], v[56:59], a[28:31], v[132:135]\n"
        "s_add_u32 s74, s74, 8192\n"
        "s_addc_u32 s75, s75, 0\n"
        "global_load_dwordx4 a[64:67], %[vx], s[74:75] offset:-4096 sc1\n"
        "v_mfma_f32_16x16x32_bf16 v[136:139], v[60:63], a[24:27], v[136:139]\n"
        "ds_read_b128 v[56:59], %[vl0] offset:28672\n"
        "v_mfma_f32_16x16x32_bf16 v[140:143], v[60:63], a[28:31], v[140:143]\n"
        "v_mfma_f32_16x16x32_bf16 v[144:147], v[96:99], a[24:27], v[144:147]\n"
        "ds_read_b128 v[60:63], %[vl0] offset:29696\n"
        "v_mfma_f32_16x16x32_bf16 v[148:151], v[96:99], a[28:31], v[148:151]\n"
        "s_waitcnt lgkmcnt(6)\n"
        "v_mfma_f32_16x16x32_bf16 v[152:155], v[100:103], a[24:27], v[152:155]\n"
        "ds_read_b128 v[96:99], %[vl0] offset:30720\n"
        "v_mfma_f32_16x16x32_bf16 v[156:159], v[100:103], a[28:31], v[156:159]\n"
        "global_load_dwordx4 a[68:71], %[vx], s[74:75] offset:-3840 sc1\n"
        "v_mfma_f32_16x16x32_bf16 v[160:163], v[104:107], a[24:27], v[160:163]\n"
        "ds_read_b128 v[100:103], %[vl0] offset:31744\n"
        "v_mfma_f32_16x16x32_bf16 v[164:167], v[104:107], a[28:31], v[164:167]\n"
        "global_load_lds_dwordx4 %[voff], s[76:77] offset:2048 sc1\n"
        "v_mfma_f32_16x16x32_bf16 v[168:171], v[108:111], a[24:27], v[168:171]\n"
        "ds_read_b128 v[104:107], %[vl0] offset:32768\n"
        "v_mfma_f32_16x16x32_bf16 v[172:175], v[108:111], a[28:31], v[172:175]\n"
        "s_waitcnt vmcnt(15)\n"
        "s_waitcnt lgkmcnt(6)\n"
        "v_mfma_f32_16x16x32_bf16 v[128:131], v[112:115], a[32:35], v[128:131]\n"
        "ds_read_b128 v[108:111], %[vl0] offset:33792\n"
        "v_mfma_f32_16x16x32_bf16 v[132:135], v[112:115], a[36:39], v[132:135]\n"
        "global_load_dwordx4 a[72:75], %[vx], s[74:75] offset:-2048 sc1\n"
        "v_mfma_f32_16x16x32_bf16 v[136:139], v[116:119], a[32:35], v[136:139]\n"
        "ds_read_b128 v[112:115], %[vl0] offset:34816\n"
        "v_mfma_f32_16x16x32_bf16 v[140:143], v[116:119], a[36:39], v[140:143]\n"
        "v_mfma_f32_16x16x32_bf16 v[144:147], v[120:123], a[32:35], v[144:147]\n"
        "ds_read_b128 v[116:119], %[vl0] offset:35840\n"
        "v_mfma_f32_16x16x32_bf16 v[148:151], v[120:123], a[36:39], v[148:151]\n"
        "s_waitcnt lgkmcnt(6)\n"
        "v_mfma_f32_16x16x32_bf16 v[152:155], v[124:127], a[32:35], v[152:155]\n"
        "ds_read_b128 v[120:123], %[vl0] offset:36864\n"
        "v_mfma_f32_16x16x32_bf16 v[156:159], v[124:127], a[36:39], v[156:159]\n"
        "global_load_dwordx4 a[76:79], %[vx], s[74:75] offset:-1792 sc1\n"
        "v_mfma_f32_16x16x32_bf16 v[160:163], v[56:59], a[32:35], v[160:163]\n"
        "ds_read_b128 v[124:127], %[vl0] offset:37888\n"
        "v_mfma_f32_16x16x32_bf16 v[164:167], v[56:59], a[36:39], v[164:167]\n"
        "global_load_lds_dwordx4 %[voff], s[76:77] offset:3072 sc1\n"
        "v_mfma_f32_16x16x32_bf16 v[168:171], v[60:63], a[32:35], v[168:171]\n"
        "ds_read_b128 v[56:59], %[vl0] offset:38912\n"
        "v_mfma_f32_16x16x32_bf16 v[172:175], v[60:63], a[36:39], v[172:175]\n"
        "s_waitcnt vmcnt(12)\n"
        "s_waitcnt lgkmcnt(6)\n"
        "v_mfma_f32_16x16x32_bf16 v[128:131], v[96:99], a[40:43], v[128:131]\n"
        "ds_read_b128 v[60:63], %[vl0] offset:39936\n"
        "s_waitcnt vmcnt(14)\n"
        "v_add_f32 v64, v64, v68\n"
        "v_mfma_f32_16x16x32_bf16 v[132:135], v[96:99], a[44:47], v[132:135]\n"
        "global_load_dwordx4 a[80:83], %[vx], s[74:75] offset:0 sc1\n"
        "v_add_f32 v72, v72, v76\n"
        "v_mfma_f32_16x16x32_bf16 v[136:139], v[100:103], a[40:43], v[136:139]\n"
        "ds_read_b128 v[96:99], %[vl0] offset:40960\n"
        "v_add_f32 v65, v65, v69\n"
        "v_add_f32 v73, v73, v77\n"
        "v_mfma_f32_16x16x32_bf16 v[140:143], v[100:103], a[44:47], v[140:143]\n"
        "v_add_f32 v66, v66, v70\n"
        "v_add_f32 v74, v74, v78\n"
        "v_mfma_f32_16x16x32_bf16 v[144:147], v[104:107], a[40:43], v[144:147]\n"
        "ds_read_b128 v[100:103], %[vl0] offset:41984\n"
        "v_add_f32 v67, v67, v71\n"
        "v_add_f32 v75, v75, v79\n"
        "v_mfma_f32_16x16x32_bf16 v[148:151], v[104:107], a[44:47], v[148:151]\n"
        "v_add_f32 v64, v64, v72\n"
        "s_waitcnt lgkmcnt(6)\n"
        "v_mfma_f32_16x16x32_bf16 v[152:155], v[108:111], a[40:43], v[152:155]\n"
        "ds_read_b128 v[104:107], %[vl0] offset:43008\n"
        "v_add_f32 v65, v65, v73\n"
        "v_add_f32 v66, v66, v74\n"
        "v_mfma_f32_16x16x32_bf16 v[156:159], v[108:111], a[44:47], v[156:159]\n"
        "global_load_dwordx4 a[84:87], %[vx], s[74:75] offset:256 sc1\n"
        "v_add_f32 v67, v67, v75\n"
        "v_mfma_f32_16x16x32_bf16 v[160:163], v[112:115], a[40:43], v[160:163]\n"
        "ds_read_b128 v[108:111], %[vl0] offset:44032\n"
        "ds_swizzle_b32 v68, v64 offset:swizzle(SWAP,16)\n"
        "ds_swizzle_b32 v69, v65 offset:swizzle(SWAP,16)\n"
        "v_mfma_f32_16x16x32_bf16 v[164:167], v[112:115], a[44:47], v[164:167]\n"
        "s_add_u32 s76, s76, 4096\n"
        "s_addc_u32 s77, s77, 0\n"
        "s_add_u32 m0, m0, 4096\n"
        "s_nop 0\n"
        "global_load_lds_dwordx4 %[voff], s[76:77] offset:0 sc1\n"
        "ds_swizzle_b32 v70, v66 offset:swizzle(SWAP,16)\n"
        "v_mfma_f32_16x16x32_bf16 v[168:171], v[116:119], a[40:43], v[168:171]\n"
        "ds_read_b128 v[112:115], %[vl0] offset:45056\n"
        "ds_swizzle_b32 v71, v67 offset:swizzle(SWAP,16)\n"
        "v_mfma_f32_16x16x32_bf16 v[172:175], v[116:119], a[44:47], v[172:175]\n"
        "s_waitcnt vmcnt(13)\n"
        "s_waitcnt lgkmcnt(10)\n"
        "v_mfma_f32_16x16x32_bf16 v[128:131], v[120:123], a[48:51], v[128:131]\n"
        "ds_read_b128 v[116:119], %[vl0] offset:46080\n"
        "v_mfma_f32_16x16x32_bf16 v[132:135], v[120:123], a[52:55], v[132:135]\n"
        "s_barrier\n"
        "global_load_dwordx4 a[88:91], %[vx], s[74:75] offset:2048 sc1\n"
        "s_waitcnt lgkmcnt(1)\n"
        "v_mfma_f32_16x16x32_bf16 v[136:139], v[124:127], a[48:51], v[136:139]\n"
        "ds_read_b128 v[120:123], %[vl0] offset:47104\n"
        "v_add_f32 v64, v64, v68\n"
        "v_add_f32 v65, v65, v69\n"
        "v_mfma_f32_16x16x32_bf16 v[140:143], v[124:127], a[52:55], v[140:143]\n"
        "v_add_f32 v66, v66, v70\n"
        "v_add_f32 v67, v67, v71\n"
        "v_mfma_f32_16x16x32_bf16 v[144:147], v[56:59], a[48:51], v[144:147]\n"
        "ds_read_b128 v[124:127], %[vl0] offset:48128\n"
        "v_mov_b32 v68, v64\n"
        "v_mov_b32 v69, v65\n"
        "v_mfma_f32_16x16x32_bf16 v[148:151], v[56:59], a[52:55], v[148:151]\n"
        "v_mov_b32 v70, v66\n"
        "s_waitcnt lgkmcnt(10)\n"
        "v_mfma_f32_16x16x32_bf16 v[152:155], v[60:63], a[48:51], v[152:155]\n"
        "ds_read_b128 v[56:59], %[vl0] offset:49152\n"
        "v_mov_b32 v71, v67\n"
        "v_mfma_f32_16x16x32_bf16 v[156:159], v[60:63], a[52:55], v[156:159]\n"
        "global_load_dwordx4 a[92:95], %[vx], s[74:75] offset:2304 sc1\n"
        "v_mfma_f32_16x16x32_bf16 v[160:163], v[96:99], a[48:51], v[160:163]\n"
        "ds_read_b128 v[60:63], %[vl0] offset:50176\n"
        "s_nop 1\n"
        "v_mfma_f32_16x16x32_bf16 v[164:167], v[96:99], a[52:55], v[164:167]\n"
        "global_load_lds_dwordx4 %[voff], s[76:77] offset:1024 sc1\n"
        "v_permlane32_swap_b32 v64, v68\n"
        "v_mfma_f32_16x16x32_bf16 v[168:171], v[100:103], a[48:51], v[168:171]\n"
        "ds_read_b128 v[96:99], %[vl0] offset:51200\n"
        "v_permlane32_swap_b32 v65, v69\n"
        "v_permlane32_swap_b32 v66, v70\n"
        "v_mfma_f32_16x16x32_bf16 v[172:175], v[100:103], a[52:55], v[172:175]\n"
        "v_permlane32_swap_b32 v67, v71\n"
        "s_waitcnt vmcnt(13)\n"
        "s_waitcnt lgkmcnt(7)\n"
        "v_mfma_f32_16x16x32_bf16 v[128:131], v[104:107], a[56:59], v[128:131]\n"
        "ds_read_b128 v[100:103], %[vl0] offset:52224\n"
        "s_nop 1\n"
        "v_mfma_f32_16x16x32_bf16 v[132:135], v[104:107], a[60:63], v[132:135]\n"
        "s_add_u32 s74, s74, 8192\n"
        "s_addc_u32 s75, s75, 0\n"
        "global_load_dwordx4 a[96:99], %[vx], s[74:75] offset:-4096 sc1\n"
        "v_add_f32 v64, v64, v68\n"
        "v_mfma_f32_16x16x32_bf16 v[136:139], v[108:111], a[56:59], v[136:139]\n"
        "ds_read_b128 v[104:107], %[vl0] offset:53248\n"
        "v_add_f32 v65, v65, v69\n"
        "v_add_f32 v66, v66, v70\n"
        "v_mfma_f32_16x16x32_bf16 v[140:143], v[108:111], a[60:63], v[140:143]\n"
        "v_add_f32 v67, v67, v71\n"
        "ds_bpermute_b32 v68, %[vbp], v64 offset:0\n"
        "v_mfma_f32_16x16x32_bf16 v[144:147], v[112:115], a[56:59], v[144:147]\n"
        "ds_read_b128 v[108:111], %[vl0] offset:54272\n"
        "ds_bpermute_b32 v69, %[vbp], v65 offset:0\n"
        "ds_bpermute_b32 v70, %[vbp], v66 offset:0\n"
        "v_mfma_f32_16x16x32_bf16 v[148:151], v[112:115], a[60:63], v[148:151]\n"
        "ds_bpermute_b32 v71, %[vbp], v67 offset:0\n"
        "s_waitcnt lgkmcnt(10)\n"
        "v_mfma_f32_16x16x32_bf16 v[152:155], v[116:119], a[56:59], v[152:155]\n"
        "ds_read_b128 v[112:115], %[vl0] offset:55296\n"
        "ds_bpermute_b32 v72, %[vbp], v64 offset:32\n"
        "ds_bpermute_b32 v73, %[vbp], v65 offset:32\n"
        "v_mfma_f32_16x16x32_bf16 v[156:159], v[116:119], a[60:63], v[156:159]\n"
        "global_load_dwordx4 a[100:103], %[vx], s[74:75] offset:-3840 sc1\n"
        "ds_bpermute_b32 v74, %[vbp], v66 offset:32\n"
        "v_mfma_f32_16x16x32_bf16 v[160:163], v[120:123], a[56:59], v[160:163]\n"
        "ds_read_b128 v[116:119], %[vl0] offset:56320\n"
        "ds_bpermute_b32 v75, %[vbp], v67 offset:32\n"
        "v_cmp_ne_u32 vcc, 0, %[vodd]\n"
        "v_mfma_f32_16x16x32_bf16 v[164:167], v[120:123], a[60:63], v[164:167]\n"
        "global_load_lds_dwordx4 %[voff], s[76:77] offset:2048 sc1\n"
        "v_mfma_f32_16x16x32_bf16 v[168:171], v[124:127], a[56:59], v[168:171]\n"
        "ds_read_b128 v[120:123], %[vl0] offset:57344\n"
        "v_mfma_f32_16x16x32_bf16 v[172:175], v[124:127], a[60:63], v[172:175]\n"
        "s_waitcnt vmcnt(13)\n"
        "s_waitcnt lgkmcnt(14)\n"
        "v_mfma_f32_16x16x32_bf16 v[128:131], v[56:59], a[64:67], v[128:131]\n"
        "ds_read_b128 v[124:127], %[vl0] offset:58368\n"
        "s_waitcnt lgkmcnt(2)\n"
        "v_cndmask_b32 v64, v68, v70, vcc\n"
        "v_mfma_f32_16x16x32_bf16 v[132:135], v[56:59], a[68:71], v[132:135]\n"
        "global_load_dwordx4 a[104:107], %[vx], s[74:75] offset:-2048 sc1\n"
        "v_cndmask_b32 v65, v69, v71, vcc\n"
        "v_mfma_f32_16x16x32_bf16 v[136:139], v[60:63], a[64:67], v[136:139]\n"
        "ds_read_b128 v[56:59], %[vl0] offset:59392\n"
        "v_mul_f32 v40, 0xbb000000, v64\n"
        "v_mul_f32 v65, 0x3b000000, v65\n"
        "v_mfma_f32_16x16x32_bf16 v[140:143], v[60:63], a[68:71], v[140:143]\n"
        "v_fma_f32 v65, -v40, v40, v65\n"
        "v_max_f32 v65, 0, v65\n"
        "v_mfma_f32_16x16x32_bf16 v[144:147], v[96:99], a[64:67], v[144:147]\n"
        "ds_read_b128 v[60:63], %[vl0] offset:60416\n"
        "v_add_f32 v42, 0x3727c5ac, v65\n"
        "v_mfma_f32_16x16x32_bf16 v[148:151], v[96:99], a[68:71], v[148:151]\n"
        "s_waitcnt lgkmcnt(13)\n"
        "v_mfma_f32_16x16x32_bf16 v[152:155], v[100:103], a[64:67], v[152:155]\n"
        "ds_read_b128 v[96:99], %[vl0] offset:61440\n"
        "v_rsq_f32 v41, v42\n"
        "v_mfma_f32_16x16x32_bf16 v[156:159], v[100:103], a[68:71], v[156:159]\n"
        "global_load_dwordx4 a[108:111], %[vx], s[74:75] offset:-1792 sc1\n"
        "v_mfma_f32_16x16x32_bf16 v[160:163], v[104:107], a[64:67], v[160:163]\n"
        "ds_read_b128 v[100:103], %[vl0] offset:62464\n"
        "s_nop 0\n"
        "v_mul_f32 v41, 0xbfb8aa3b, v41\n"
        "v_mfma_f32_16x16x32_bf16 v[164:167], v[104:107], a[68:71], v[164:167]\n"
        "global_load_lds_dwordx4 %[voff], s[76:77] offset:3072 sc1\n"
        "v_cndmask_b32 v66, v72, v74, vcc\n"
        "v_mfma_f32_16x16x32_bf16 v[168:171], v[108:111], a[64:67], v[168:171]\n"
        "ds_read_b128 v[104:107], %[vl0] offset:63488\n"
        "v_cndmask_b32 v67, v73, v75, vcc\n"
        "v_mul_f32 v43, 0xbb000000, v66\n"
        "v_mfma_f32_16x16x32_bf16 v[172:175], v[108:111], a[68:71], v[172:175]\n"
        "v_mul_f32 v67, 0x3b000000, v67\n"
        "s_waitcnt vmcnt(13)\n"
        "s_waitcnt lgkmcnt(6)\n"
        "v_mfma_f32_16x16x32_bf16 v[128:131], v[112:115], a[72:75], v[128:131]\n"
        "ds_read_b128 v[108:111], %[vl0] offset:64512\n"
        "v_fma_f32 v67, -v43, v43, v67\n"
        "v_max_f32 v67, 0, v67\n"
        "v_mfma_f32_16x16x32_bf16 v[132:135], v[112:115], a[76:79], v[132:135]\n"
        "global_load_dwordx4 a[112:115], %[vx], s[74:75] offset:0 sc1\n"
        "v_add_f32 v45, 0x3727c5ac, v67\n"
        "v_mfma_f32_16x16x32_bf16 v[136:139], v[116:119], a[72:75], v[136:139]\n"
        "ds_read_b128 v[112:115], %[vl1] offset:0\n"
        "v_rsq_f32 v44, v45\n"
        "v_mfma_f32_16x16x32_bf16 v[140:143], v[116:119], a[76:79], v[140:143]\n"
        "s_nop 0\n"
        "v_mul_f32 v44, 0xbfb8aa3b, v44\n"
        "v_mfma_f32_16x16x32_bf16 v[144:147], v[120:123], a[72:75], v[144:147]\n"
        "ds_read_b128 v[116:119], %[vl1] offset:1024\n"
        "ds_read_b128 v[48:51], %[vcs] offset:0\n"
        "ds_read_b128 v[52:55], %[vcs] offset:64\n"
        "v_mfma_f32_16x16x32_bf16 v[148:151], v[120:123], a[76:79], v[148:151]\n"
        "s_waitcnt lgkmcnt(8)\n"
        "v_mfma_f32_16x16x32_bf16 v[152:155], v[124:127], a[72:75], v[152:155]\n"
        "ds_read_b128 v[120:123], %[vl1] offset:2048\n"
        "v_mfma_f32_16x16x32_bf16 v[156:159], v[124:127], a[76:79], v[156:159]\n"
        "global_load_dwordx4 a[116:119], %[vx], s[74:75] offset:256 sc1\n"
        "v_mfma_f32_16x16x32_bf16 v[160:163], v[56:59], a[72:75], v[160:163]\n"
        "ds_read_b128 v[124:127], %[vl1] offset:3072\n"
        "v_mfma_f32_16x16x32_bf16 v[164:167], v[56:59], a[76:79], v[164:167]\n"
        "v_mfma_f32_16x16x32_bf16 v[168:171], v[60:63], a[72:75], v[168:171]\n"
        "ds_read_b128 v[56:59], %[vl1] offset:4096\n"
        "v_mfma_f32_16x16x32_bf16 v[172:175], v[60:63], a[76:79], v[172:175]\n"
        "s_waitcnt vmcnt(12)\n"
        "s_waitcnt lgkmcnt(8)\n"
        "v_mfma_f32_16x16x32_bf16 v[128:131], v[96:99], a[80:83], v[128:131]\n"
        "ds_read_b128 v[60:63], %[vl1] offset:5120\n"
        "v_mfma_f32_16x16x32_bf16 v[132:135], v[96:99], a[84:87], v[132:135]\n"
        "global_load_dwordx4 a[120:123], %[vx], s[74:75] offset:2048 sc1\n"
        "v_mfma_f32_16x16x32_bf16 v[136:139], v[100:103], a[80:83], v[136:139]\n"
        "ds_read_b128 v[96:99], %[vl1] offset:6144\n"
        "v_mfma_f32_16x16x32_bf16 v[140:143], v[100:103], a[84:87], v[140:143]\n"
        "v_mfma_f32_16x16x32_bf16 v[144:147], v[104:107], a[80:83], v[144:147]\n"
        "ds_read_b128 v[100:103], %[vl1] offset:7168\n"
        "v_mfma_f32_16x16x32_bf16 v[148:151], v[104:107], a[84:87], v[148:151]\n"
        "s_waitcnt lgkmcnt(8)\n"
        "v_mfma_f32_16x16x32_bf16 v[152:155], v[108:111], a[80:83], v[152:155]\n"
        "ds_read_b128 v[104:107], %[vl1] offset:8192\n"
        "v_mfma_f32_16x16x32_bf16 v[156:159], v[108:111], a[84:87], v[156:159]\n"
        "global_load_dwordx4 a[124:127], %[vx], s[74:75] offset:2304 sc1\n"
        "v_mfma_f32_16x16x32_bf16 v[160:163], v[112:115], a[80:83], v[160:163]\n"
        "ds_read_b128 v[108:111], %[vl1] offset:9216\n"
        "v_mfma_f32_16x16x32_bf16 v[164:167], v[112:115], a[84:87], v[164:167]\n"
        "v_mfma_f32_16x16x32_bf16 v[168:171], v[116:119], a[80:83], v[168:171]\n"
        "ds_read_b128 v[112:115], %[vl1] offset:10240\n"
        "v_mfma_f32_16x16x32_bf16 v[172:175], v[116:119], a[84:87], v[172:175]\n"
        "s_add_u32 s76, %[sw_lo], 131072\n"
        "s_addc_u32 s77, %[sw_hi], 0\n"
        "s_add_u32 m0, %[sm], 0\n"
        "s_nop 0\n"
        "global_load_lds_dwordx4 %[voff], s[76:77] offset:0 sc1\n"
        "s_waitcnt vmcnt(12)\n"
        "s_waitcnt lgkmcnt(6)\n"
        "v_mfma_f32_16x16x32_bf16 v[128:131], v[120:123], a[88:91], v[128:131]\n"
        "ds_read_b128 v[116:119], %[vl1] offset:11264\n"
        "v_mfma_f32_16x16x32_bf16 v[132:135], v[120:123], a[92:95], v[132:135]\n"
        "v_mfma_f32_16x16x32_bf16 v[136:139], v[124:127], a[88:91], v[136:139]\n"
        "ds_read_b128 v[120:123], %[vl1] offset:12288\n"
        "v_mfma_f32_16x16x32_bf16 v[140:143], v[124:127], a[92:95], v[140:143]\n"
        "global_load_lds_dwordx4 %[voff], s[76:77] offset:1024 sc1\n"
        "v_mfma_f32_16x16x32_bf16 v[144:147], v[56:59], a[88:91], v[144:147]\n"
        "ds_read_b128 v[124:127], %[vl1] offset:13312\n"
        "v_mfma_f32_16x16x32_bf16 v[148:151], v[56:59], a[92:95], v[148:151]\n"
        "s_waitcnt lgkmcnt(6)\n"
        "v_mfma_f32_16x16x32_bf16 v[152:155], v[60:63], a[88:91], v[152:155]\n"
        "ds_read_b128 v[56:59], %[vl1] offset:14336\n"
        "v_mfma_f32_16x16x32_bf16 v[156:159], v[60:63], a[92:95], v[156:159]\n"
        "global_load_lds_dwordx4 %[voff], s[76:77] offset:2048 sc1\n"
        "v_mfma_f32_16x16x32_bf16 v[160:163], v[96:99], a[88:91], v[160:163]\n"
        "ds_read_b128 v[60:63], %[vl1] offset:15360\n"
        "v_mfma_f32_16x16x32_bf16 v[164:167], v[96:99], a[92:95], v[164:167]\n"
        "v_mfma_f32_16x16x32_bf16 v[168:171], v[100:103], a[88:91], v[168:171]\n"
        "ds_read_b128 v[96:99], %[vl1] offset:16384\n"
        "v_mfma_f32_16x16x32_bf16 v[172:175], v[100:103], a[92:95], v[172:175]\n"
        "global_load_lds_dwordx4 %[voff], s[76:77] offset:3072 sc1\n"
        "s_waitcnt vmcnt(12)\n"
        "s_waitcnt lgkmcnt(6)\n"
        "v_mfma_f32_16x16x32_bf16 v[128:131], v[104:107], a[96:99], v[128:131]\n"
        "ds_read_b128 v[100:103], %[vl1] offset:17408\n"
        "v_mfma_f32_16x16x32_bf16 v[132:135], v[104:107], a[100:103], v[132:135]\n"
        "v_mfma_f32_16x16x32_bf16 v[136:139], v[108:111], a[96:99], v[136:139]\n"
        "ds_read_b128 v[104:107], %[vl1] offset:18432\n"
        "v_mfma_f32_16x16x32_bf16 v[140:143], v[108:111], a[100:103], v[140:143]\n"
        "s_add_u32 s76, s76, 4096\n"
        "s_addc_u32 s77, s77, 0\n"
        "s_add_u32 m0, m0, 4096\n"
        "s_nop 0\n"
        "global_load_lds_dwordx4 %[voff], s[76:77] offset:0 sc1\n"
        "v_mfma_f32_16x16x32_bf16 v[144:147], v[112:115], a[96:99], v[144:147]\n"
        "ds_read_b128 v[108:111], %[vl1] offset:19456\n"
        "v_mfma_f32_16x16x32_bf16 v[148:151], v[112:115], a[100:103], v[148:151]\n"
        "s_waitcnt lgkmcnt(6)\n"
        "v_mfma_f32_16x16x32_bf16 v[152:155], v[116:119], a[96:99], v[152:155]\n"
        "ds_read_b128 v[112:115], %[vl1] offset:20480\n"
        "v_mfma_f32_16x16x32_bf16 v[156:159], v[116:119], a[100:103], v[156:159]\n"
        "global_load_lds_dwordx4 %[voff], s[76:77] offset:1024 sc1\n"
        "v_mfma_f32_16x16x32_bf16 v[160:163], v[120:123], a[96:99], v[160:163]\n"
        "ds_read_b128 v[116:119], %[vl1] offset:21504\n"
        "v_mfma_f32_16x16x32_bf16 v[164:167], v[120:123], a[100:103], v[164:167]\n"
        "s_waitcnt vmcnt(10)\n"
        "s_barrier\n"
        "v_mfma_f32_16x16x32_bf16 v[168:171], v[124:127], a[96:99], v[168:171]\n"
        "ds_read_b128 v[120:123], %[vl1] offset:22528\n"
        "v_mfma_f32_16x16x32_bf16 v[172:175], v[124:127], a[100:103], v[172:175]\n"
        "global_load_lds_dwordx4 %[voff], s[76:77] offset:2048 sc1\n"
        "s_waitcnt vmcnt(12)\n"
        "s_waitcnt lgkmcnt(6)\n"
        "v_mfma_f32_16x16x32_bf16 v[128:131], v[56:59], a[104:107], v[128:131]\n"
        "ds_read_b128 v[124:127], %[vl1] offset:23552\n"
        "v_mfma_f32_16x16x32_bf16 v[132:135], v[56:59], a[108:111], v[132:135]\n"
        "v_mfma_f32_16x16x32_bf16 v[136:139], v[60:63], a[104:107], v[136:139]\n"
        "ds_read_b128 v[56:59], %[vl1] offset:24576\n"
        "v_mfma_f32_16x16x32_bf16 v[140:143], v[60:63], a[108:111], v[140:143]\n"
        "global_load_lds_dwordx4 %[voff], s[76:77] offset:3072 sc1\n"
        "v_mfma_f32_16x16x32_bf16 v[144:147], v[96:99], a[104:107], v[144:147]\n"
        "ds_read_b128 v[60:63], %[vl1] offset:25600\n"
        "v_mfma_f32_16x16x32_bf16 v[148:151], v[96:99], a[108:111], v[148:151]\n"
        "s_waitcnt lgkmcnt(6)\n"
        "v_mfma_f32_16x16x32_bf16 v[152:155], v[100:103], a[104:107], v[152:155]\n"
        "ds_read_b128 v[96:99], %[vl1] offset:26624\n"
        "v_mfma_f32_16x16x32_bf16 v[156:159], v[100:103], a[108:111], v[156:159]\n"
        "s_add_u32 s76, %[sw_lo], 163840\n"
        "s_addc_u32 s77, %[sw_hi], 0\n"
        "s_add_u32 m0, %[sm], 32768\n"
        "s_nop 0\n"
        "global_load_lds_dwordx4 %[voff], s[76:77] offset:0 sc1\n"
        "v_mfma_f32_16x16x32_bf16 v[160:163], v[104:107], a[104:107], v[160:163]\n"
        "ds_read_b128 v[100:103], %[vl1] offset:27648\n"
        "v_mfma_f32_16x16x32_bf16 v[164:167], v[104:107], a[108:111], v[164:167]\n"
        "v_mfma_f32_16x16x32_bf16 v[168:171], v[108:111], a[104:107], v[168:171]\n"
        "ds_read_b128 v[104:107], %[vl1] offset:28672\n"
        "v_mfma_f32_16x16x32_bf16 v[172:175], v[108:111], a[108:111], v[172:175]\n"
        "s_waitcnt vmcnt(11)\n"
        "s_waitcnt lgkmcnt(6)\n"
        "v_mfma_f32_16x16x32_bf16 v[128:131], v[112:115], a[112:115], v[128:131]\n"
        "ds_read_b128 v[108:111], %[vl1] offset:29696\n"
        "v_mfma_f32_16x16x32_bf16 v[132:135], v[112:115], a[116:119], v[132:135]\n"
        "global_load_lds_dwordx4 %[voff], s[76:77] offset:1024 sc1\n"
        "v_mfma_f32_16x16x32_bf16 v[136:139], v[116:119], a[112:115], v[136:139]\n"
        "ds_read_b128 v[112:115], %[vl1] offset:30720\n"
        "v_mfma_f32_16x16x32_bf16 v[140:143], v[116:119], a[116:119], v[140:143]\n"
        "v_mfma_f32_16x16x32_bf16 v[144:147], v[120:123], a[112:115], v[144:147]\n"
        "ds_read_b128 v[116:119], %[vl1] offset:31744\n"
        "v_mfma_f32_16x16x32_bf16 v[148:151], v[120:123], a[116:119], v[148:151]\n"
        "s_waitcnt lgkmcnt(6)\n"
        "v_mfma_f32_16x16x32_bf16 v[152:155], v[124:127], a[112:115], v[152:155]\n"
        "ds_read_b128 v[120:123], %[vl1] offset:32768\n"
        "v_mfma_f32_16x16x32_bf16 v[156:159], v[124:127], a[116:119], v[156:159]\n"
        "global_load_lds_dwordx4 %[voff], s[76:77] offset:2048 sc1\n"
        "v_mfma_f32_16x16x32_bf16 v[160:163], v[56:59], a[112:115], v[160:163]\n"
        "ds_read_b128 v[124:127], %[vl1] offset:33792\n"
        "v_mfma_f32_16x16x32_bf16 v[164:167], v[56:59], a[116:119], v[164:167]\n"
        "v_mfma_f32_16x16x32_bf16 v[168:171], v[60:63], a[112:115], v[168:171]\n"
        "ds_read_b128 v[56:59], %[vl1] offset:34816\n"
        "v_mfma_f32_16x16x32_bf16 v[172:175], v[60:63], a[116:119], v[172:175]\n"
        "s_waitcnt vmcnt(11)\n"
        "s_waitcnt lgkmcnt(6)\n"
        "v_mfma_f32_16x16x32_bf16 v[128:131], v[96:99], a[120:123], v[128:131]\n"
        "ds_read_b128 v[60:63], %[vl1] offset:35840\n"
        "v_mfma_f32_16x16x32_bf16 v[132:135], v[96:99], a[124:127], v[132:135]\n"
        "global_load_lds_dwordx4 %[voff], s[76:77] offset:3072 sc1\n"
        "v_mfma_f32_16x16x32_bf16 v[136:139], v[100:103], a[120:123], v[136:139]\n"
        "ds_read_b128 v[96:99], %[vl1] offset:36864\n"
        "v_mfma_f32_16x16x32_bf16 v[140:143], v[100:103], a[124:127], v[140:143]\n"
        "v_mfma_f32_16x16x32_bf16 v[144:147], v[104:107], a[120:123], v[144:147]\n"
        "ds_read_b128 v[100:103], %[vl1] offset:37888\n"
        "v_mfma_f32_16x16x32_bf16 v[148:151], v[104:107], a[124:127], v[148:151]\n"
        "s_waitcnt lgkmcnt(6)\n"
        "v_mfma_f32_16x16x32_bf16 v[152:155], v[108:111], a[120:123], v[152:155]\n"
        "ds_read_b128 v[104:107], %[vl1] offset:38912\n"
        "v_mfma_f32_16x16x32_bf16 v[156:159], v[108:111], a[124:127], v[156:159]\n"
        "s_add_u32 s76, s76, 4096\n"
        "s_addc_u32 s77, s77, 0\n"
        "s_add_u32 m0, m0, 4096\n"
        "s_nop 0\n"
        "global_load_lds_dwordx4 %[voff], s[76:77] offset:0 sc1\n"
        "v_mfma_f32_16x16x32_bf16 v[160:163], v[112:115], a[120:123], v[160:163]\n"
        "ds_read_b128 v[108:111], %[vl1] offset:39936\n"
        "s_waitcnt lgkmcnt(15)\n"
        "v_fmac_f32 v128, v48, v40\n"
        "v_mfma_f32_16x16x32_bf16 v[164:167], v[112:115], a[124:127], v[164:167]\n"
        "v_fmac_f32 v129, v49, v40\n"
        "v_fmac_f32 v130, v50, v40\n"
        "v_mfma_f32_16x16x32_bf16 v[168:171], v[116:119], a[120:123], v[168:171]\n"
        "ds_read_b128 v[112:115], %[vl1] offset:40960\n"
        "v_fmac_f32 v131, v51, v40\n"
        "v_fmac_f32 v132, v48, v43\n"
        "v_mfma_f32_16x16x32_bf16 v[172:175], v[116:119], a[124:127], v[172:175]\n"
        "v_fmac_f32 v133, v49, v43\n"
        "s_waitcnt lgkmcnt(6)\n"
        "v_mfma_f32_16x16x32_bf16 v[176:179], v[120:123], a[0:3], 0\n"
        "ds_read_b128 v[116:119], %[vl1] offset:41984\n"
        "v_fmac_f32 v134, v50, v43\n"
        "v_fmac_f32 v135, v51, v43\n"
        "v_mfma_f32_16x16x32_bf16 v[180:183], v[120:123], a[4:7], 0\n"
        "global_load_lds_dwordx4 %[voff], s[76:77] offset:1024 sc1\n"
        "v_mul_f32 v64, v128, v41\n"
        "v_mfma_f32_16x16x32_bf16 v[184:187], v[124:127], a[0:3], 0\n"
        "ds_read_b128 v[120:123], %[vl1] offset:43008\n"
        "v_mul_f32 v65, v129, v41\n"
        "v_mul_f32 v66, v130, v41\n"
        "v_mfma_f32_16x16x32_bf16 v[188:191], v[124:127], a[4:7], 0\n"
        "v_mul_f32 v67, v131, v41\n"
        "v_mul_f32 v68, v132, v44\n"
        "v_mfma_f32_16x16x32_bf16 v[192:195], v[56:59], a[0:3], 0\n"
        "ds_read_b128 v[124:127], %[vl1] offset:44032\n"
        "v_mul_f32 v69, v133, v44\n"
        "v_mul_f32 v70, v134, v44\n"
        "v_mfma_f32_16x16x32_bf16 v[196:199], v[56:59], a[4:7], 0\n"
        "v_mul_f32 v71, v135, v44\n"
        "s_waitcnt lgkmcnt(6)\n"
        "v_mfma_f32_16x16x32_bf16 v[200:203], v[60:63], a[0:3], 0\n"
        "ds_read_b128 v[56:59], %[vl1] offset:45056\n"
        "v_exp_f32 v64, v64\n"
        "v_mfma_f32_16x16x32_bf16 v[204:207], v[60:63], a[4:7], 0\n"
        "global_load_lds_dwordx4 %[voff], s[76:77] offset:2048 sc1\n"
        "v_mfma_f32_16x16x32_bf16 v[176:179], v[96:99], a[8:11], v[176:179]\n"
        "ds_read_b128 v[60:63], %[vl1] offset:46080\n"
        "v_exp_f32 v65, v65\n"
        "v_mfma_f32_16x16x32_bf16 v[180:183], v[96:99], a[12:15], v[180:183]\n"
        "v_exp_f32 v66, v66\n"
        "v_mfma_f32_16x16x32_bf16 v[184:187], v[100:103], a[8:11], v[184:187]\n"
        "ds_read_b128 v[96:99], %[vl1] offset:47104\n"
        "v_exp_f32 v67, v67\n"
        "v_mfma_f32_16x16x32_bf16 v[188:191], v[100:103], a[12:15], v[188:191]\n"
        "s_waitcnt lgkmcnt(6)\n"
        "v_mfma_f32_16x16x32_bf16 v[192:195], v[104:107], a[8:11], v[192:195]\n"
        "ds_read_b128 v[100:103], %[vl1] offset:48128\n"
        "v_exp_f32 v68, v68\n"
        "v_mfma_f32_16x16x32_bf16 v[196:199], v[104:107], a[12:15], v[196:199]\n"
        "global_load_lds_dwordx4 %[voff], s[76:77] offset:3072 sc1\n"
        "v_mfma_f32_16x16x32_bf16 v[200:203], v[108:111], a[8:11], v[200:203]\n"
        "ds_read_b128 v[104:107], %[vl1] offset:49152\n"
        "v_exp_f32 v69, v69\n"
        "v_mfma_f32_16x16x32_bf16 v[204:207], v[108:111], a[12:15], v[204:207]\n"
        "v_exp_f32 v70, v70\n"
        "v_mfma_f32_16x16x32_bf16 v[176:179], v[112:115], a[16:19], v[176:179]\n"
        "ds_read_b128 v[108:111], %[vl1] offset:50176\n"
        "v_exp_f32 v71, v71\n"
        "v_mfma_f32_16x16x32_bf16 v[180:183], v[112:115], a[20:23], v[180:183]\n"
        "v_fmac_f32 v136, v52, v40\n"
        "s_waitcnt lgkmcnt(6)\n"
        "v_mfma_f32_16x16x32_bf16 v[184:187], v[116:119], a[16:19], v[184:187]\n"
        "ds_read_b128 v[112:115], %[vl1] offset:51200\n"
        "v_fmac_f32 v137, v53, v40\n"
        "v_fmac_f32 v138, v54, v40\n"
        "v_mfma_f32_16x16x32_bf16 v[188:191], v[116:119], a[20:23], v[188:191]\n"
        "v_fmac_f32 v139, v55, v40\n"
        "v_fmac_f32 v140, v52, v43\n"
        "v_mfma_f32_16x16x32_bf16 v[192:195], v[120:123], a[16:19], v[192:195]\n"
        "ds_read_b128 v[116:119], %[vl1] offset:52224\n"
        "v_fmac_f32 v141, v53, v43\n"
        "v_fmac_f32 v142, v54, v43\n"
        "v_mfma_f32_16x16x32_bf16 v[196:199], v[120:123], a[20:23], v[196:199]\n"
        "v_fmac_f32 v143, v55, v43\n"
        "ds_read_b128 v[48:51], %[vcs] offset:128\n"
        "v_mfma_f32_16x16x32_bf16 v[200:203], v[124:127], a[16:19], v[200:203]\n"
        "ds_read_b128 v[120:123], %[vl1] offset:53248\n"
        "ds_read_b128 v[52:55], %[vcs] offset:192\n"
        "v_fma_f32 v64, v64, v42, v42\n"
        "v_mfma_f32_16x16x32_bf16 v[204:207], v[124:127], a[20:23], v[204:207]\n"
        "v_fma_f32 v65, v65, v42, v42\n"
        "s_waitcnt lgkmcnt(8)\n"
        "v_mfma_f32_16x16x32_bf16 v[176:179], v[56:59], a[24:27], v[176:179]\n"
        "ds_read_b128 v[124:127], %[vl1] offset:54272\n"
        "v_fma_f32 v66, v66, v42, v42\n"
        "v_fma_f32 v67, v67, v42, v42\n"
        "v_mfma_f32_16x16x32_bf16 v[180:183], v[56:59], a[28:31], v[180:183]\n"
        "s_waitcnt vmcnt(8)\n"
        "s_barrier\n"
        "v_fma_f32 v68, v68, v45, v45\n"
        "v_fma_f32 v69, v69, v45, v45\n"
        "v_mfma_f32_16x16x32_bf16 v[184:187], v[60:63], a[24:27], v[184:187]\n"
        "ds_read_b128 v[56:59], %[vl1] offset:55296\n"
        "v_fma_f32 v70, v70, v45, v45\n"
        "v_fma_f32 v71, v71, v45, v45\n"
        "v_mfma_f32_16x16x32_bf16 v[188:191], v[60:63], a[28:31], v[188:191]\n"
        "v_rcp_f32 v64, v64\n"
        "v_mfma_f32_16x16x32_bf16 v[192:195], v[96:99], a[24:27], v[192:195]\n"
        "ds_read_b128 v[60:63], %[vl1] offset:56320\n"
        "v_rcp_f32 v65, v65\n"
        "v_mfma_f32_16x16x32_bf16 v[196:199], v[96:99], a[28:31], v[196:199]\n"
        "s_add_u32 s76, %[sw_lo], 196608\n"
        "s_addc_u32 s77, %[sw_hi], 0\n"
        "s_add_u32 m0, %[sm], 65536\n"
        "s_nop 0\n"
        "global_load_lds_dwordx4 %[voff], s[76:77] offset:0 sc1\n"
        "s_waitcnt lgkmcnt(8)\n"
        "v_mfma_f32_16x16x32_bf16 v[200:203], v[100:103], a[24:27], v[200:203]\n"
        "ds_read_b128 v[96:99], %[vl1] offset:57344\n"
        "v_rcp_f32 v66, v66\n"
        "v_mfma_f32_16x16x32_bf16 v[204:207], v[100:103], a[28:31], v[204:207]\n"
        "v_rcp_f32 v67, v67\n"
        "v_mfma_f32_16x16x32_bf16 v[176:179], v[104:107], a[32:35], v[176:179]\n"
        "ds_read_b128 v[100:103], %[vl1] offset:58368\n"
        "v_rcp_f32 v68, v68\n"
        "v_mfma_f32_16x16x32_bf16 v[180:183], v[104:107], a[36:39], v[180:183]\n"
        "v_rcp_f32 v69, v69\n"
        "v_mfma_f32_16x16x32_bf16 v[184:187], v[108:111], a[32:35], v[184:187]\n"
        "ds_read_b128 v[104:107], %[vl1] offset:59392\n"
        "v_rcp_f32 v70, v70\n"
        "v_mfma_f32_16x16x32_bf16 v[188:191], v[108:111], a[36:39], v[188:191]\n"
        "global_load_lds_dwordx4 %[voff], s[76:77] offset:1024 sc1\n"
        "s_waitcnt lgkmcnt(7)\n"
        "v_mfma_f32_16x16x32_bf16 v[192:195], v[112:115], a[32:35], v[192:195]\n"
        "ds_read_b128 v[108:111], %[vl1] offset:60416\n"
        "v_rcp_f32 v71, v71\n"
        "v_mfma_f32_16x16x32_bf16 v[196:199], v[112:115], a[36:39], v[196:199]\n"
        "v_mul_f32 v128, v128, v136\n"
        "v_mul_f32 v129, v129, v137\n"
        "v_mfma_f32_16x16x32_bf16 v[200:203], v[116:119], a[32:35], v[200:203]\n"
        "ds_read_b128 v[112:115], %[vl1] offset:61440\n"
        "v_mul_f32 v130, v130, v138\n"
        "v_mul_f32 v131, v131, v139\n"
        "v_mfma_f32_16x16x32_bf16 v[204:207], v[116:119], a[36:39], v[204:207]\n"
        "v_mul_f32 v132, v132, v140\n"
        "v_mul_f32 v133, v133, v141\n"
        "v_mfma_f32_16x16x32_bf16 v[176:179], v[120:123], a[40:43], v[176:179]\n"
        "ds_read_b128 v[116:119], %[vl1] offset:62464\n"
        "v_mul_f32 v134, v134, v142\n"
        "v_mul_f32 v135, v135, v143\n"
        "v_mfma_f32_16x16x32_bf16 v[180:183], v[120:123], a[44:47], v[180:183]\n"
        "global_load_lds_dwordx4 %[voff], s[76:77] offset:2048 sc1\n"
        "s_waitcnt lgkmcnt(6)\n"
        "v_mfma_f32_16x16x32_bf16 v[184:187], v[124:127], a[40:43], v[184:187]\n"
        "ds_read_b128 v[120:123], %[vl1] offset:63488\n"
        "v_mul_f32 v128, v128, v64\n"
        "v_mul_f32 v129, v129, v65\n"
        "v_mfma_f32_16x16x32_bf16 v[188:191], v[124:127], a[44:47], v[188:191]\n"
        "v_mul_f32 v130, v130, v66\n"
        "v_mul_f32 v131, v131, v67\n"
        "v_mfma_f32_16x16x32_bf16 v[192:195], v[56:59], a[40:43], v[192:195]\n"
        "ds_read_b128 v[124:127], %[vl1] offset:64512\n"
        "v_mul_f32 v132, v132, v68\n"
        "v_mul_f32 v133, v133, v69\n"
        "v_mfma_f32_16x16x32_bf16 v[196:199], v[56:59], a[44:47], v[196:199]\n"
        "v_mul_f32 v134, v134, v70\n"
        "v_mul_f32 v135, v135, v71\n"
        "v_mfma_f32_16x16x32_bf16 v[200:203], v[60:63], a[40:43], v[200:203]\n"
        "ds_read_b128 v[56:59], %[vl0] offset:0\n"
        "v_cvt_pk_bf16_f32 v72, v128, v129\n"
        "v_cvt_pk_bf16_f32 v73, v130, v131\n"
        "v_mfma_f32_16x16x32_bf16 v[204:207], v[60:63], a[44:47], v[204:207]\n"
        "global_load_lds_dwordx4 %[voff], s[76:77] offset:3072 sc1\n"
        "s_waitcnt lgkmcnt(6)\n"
        "v_mfma_f32_16x16x32_bf16 v[176:179], v[96:99], a[48:51], v[176:179]\n"
        "ds_read_b128 v[60:63], %[vl0] offset:1024\n"
        "v_cvt_pk_bf16_f32 v74, v132, v133\n"
        "v_cvt_pk_bf16_f32 v75, v134, v135\n"
        "v_mfma_f32_16x16x32_bf16 v[180:183], v[96:99], a[52:55], v[180:183]\n"
        "s_nop 1\n"
        "v_permlane32_swap_b32 v72, v74\n"
        "v_mfma_f32_16x16x32_bf16 v[184:187], v[100:103], a[48:51], v[184:187]\n"
        "ds_read_b128 v[96:99], %[vl0] offset:2048\n"
        "v_permlane32_swap_b32 v73, v75\n"
        "global_store_dwordx4 %[vh], v[72:75], %[sh] offset:-3072 sc1\n"
        "v_mfma_f32_16x16x32_bf16 v[188:191], v[100:103], a[52:55], v[188:191]\n"
        "s_waitcnt lgkmcnt(14)\n"
        "v_fmac_f32 v144, v48, v40\n"
        "v_mfma_f32_16x16x32_bf16 v[192:195], v[104:107], a[48:51], v[192:195]\n"
        "ds_read_b128 v[100:103], %[vl0] offset:3072\n"
        "v_fmac_f32 v145, v49, v40\n"
        "v_fmac_f32 v146, v50, v40\n"
        "v_mfma_f32_16x16x32_bf16 v[196:199], v[104:107], a[52:55], v[196:199]\n"
        "s_add_u32 s76, s76, 4096\n"
        "s_addc_u32 s77, s77, 0\n"
        "s_add_u32 m0, m0, 4096\n"
        "s_nop 0\n"
        "global_load_lds_dwordx4 %[voff], s[76:77] offset:0 sc1\n"
        "s_waitcnt lgkmcnt(6)\n"
        "v_mfma_f32_16x16x32_bf16 v[200:203], v[108:111], a[48:51], v[200:203]\n"
        "ds_read_b128 v[104:107], %[vl0] offset:4096\n"
        "v_fmac_f32 v147, v51, v40\n"
        "v_fmac_f32 v148, v48, v43\n"
        "v_mfma_f32_16x16x32_bf16 v[204:207], v[108:111], a[52:55], v[204:207]\n"
        "v_fmac_f32 v149, v49, v43\n"
        "v_fmac_f32 v150, v50, v43\n"
        "v_mfma_f32_16x16x32_bf16 v[176:179], v[112:115], a[56:59], v[176:179]\n"
        "ds_read_b128 v[108:111], %[vl0] offset:5120\n"
        "v_fmac_f32 v151, v51, v43\n"
        "v_mul_f32 v64, v144, v41\n"
        "v_mfma_f32_16x16x32_bf16 v[180:183], v[112:115], a[60:63], v[180:183]\n"
        "v_mul_f32 v65, v145, v41\n"
        "v_mul_f32 v66, v146, v41\n"
        "v_mfma_f32_16x16x32_bf16 v[184:187], v[116:119], a[56:59], v[184:187]\n"
        "ds_read_b128 v[112:115], %[vl0] offset:6144\n"
        "v_mul_f32 v67, v147, v41\n"
        "v_mul_f32 v68, v148, v44\n"
        "v_mfma_f32_16x16x32_bf16 v[188:191], v[116:119], a[60:63], v[188:191]\n"
        "global_load_lds_dwordx4 %[voff], s[76:77] offset:1024 sc1\n"
        "s_waitcnt lgkmcnt(6)\n"
        "v_mfma_f32_16x16x32_bf16 v[192:195], v[120:123], a[56:59], v[192:195]\n"
        "ds_read_b128 v[116:119], %[vl0] offset:7168\n"
        "v_mul_f32 v69, v149, v44\n"
        "v_mul_f32 v70, v150, v44\n"
        "v_mfma_f32_16x16x32_bf16 v[196:199], v[120:123], a[60:63], v[196:199]\n"
        "v_mul_f32 v71, v151, v44\n"
        "v_mfma_f32_16x16x32_bf16 v[200:203], v[124:127], a[56:59], v[200:203]\n"
        "ds_read_b128 v[120:123], %[vl0] offset:8192\n"
        "v_exp_f32 v64, v64\n"
        "v_mfma_f32_16x16x32_bf16 v[204:207], v[124:127], a[60:63], v[204:207]\n"
        "v_exp_f32 v65, v65\n"
        "v_mfma_f32_16x16x32_bf16 v[176:179], v[56:59], a[64:67], v[176:179]\n"
        "ds_read_b128 v[124:127], %[vl0] offset:9216\n"
        "v_exp_f32 v66, v66\n"
        "v_mfma_f32_16x16x32_bf16 v[180:183], v[56:59], a[68:71], v[180:183]\n"
        "global_load_lds_dwordx4 %[voff], s[76:77] offset:2048 sc1\n"
        "s_waitcnt lgkmcnt(6)\n"
        "v_mfma_f32_16x16x32_bf16 v[184:187], v[60:63], a[64:67], v[184:187]\n"
        "ds_read_b128 v[56:59], %[vl0] offset:10240\n"
        "v_exp_f32 v67, v67\n"
        "v_mfma_f32_16x16x32_bf16 v[188:191], v[60:63], a[68:71], v[188:191]\n"
        "v_exp_f32 v68, v68\n"
        "v_mfma_f32_16x16x32_bf16 v[192:195], v[96:99], a[64:67], v[192:195]\n"
        "ds_read_b128 v[60:63], %[vl0] offset:11264\n"
        "v_exp_f32 v69, v69\n"
        "v_mfma_f32_16x16x32_bf16 v[196:199], v[96:99], a[68:71], v[196:199]\n"
        "v_exp_f32 v70, v70\n"
        "v_mfma_f32_16x16x32_bf16 v[200:203], v[100:103], a[64:67], v[200:203]\n"
        "ds_read_b128 v[96:99], %[vl0] offset:12288\n"
        "v_exp_f32 v71, v71\n"
        "v_mfma_f32_16x16x32_bf16 v[204:207], v[100:103], a[68:71], v[204:207]\n"
        "global_load_lds_dwordx4 %[voff], s[76:77] offset:3072 sc1\n"
        "s_waitcnt lgkmcnt(6)\n"
        "v_mfma_f32_16x16x32_bf16 v[176:179], v[104:107], a[72:75], v[176:179]\n"
        "ds_read_b128 v[100:103], %[vl0] offset:13312\n"
        "v_fmac_f32 v152, v52, v40\n"
        "v_fmac_f32 v153, v53, v40\n"
        "v_mfma_f32_16x16x32_bf16 v[180:183], v[104:107], a[76:79], v[180:183]\n"
        "v_fmac_f32 v154, v54, v40\n"
        "v_fmac_f32 v155, v55, v40\n"
        "v_mfma_f32_16x16x32_bf16 v[184:187], v[108:111], a[72:75], v[184:187]\n"
        "ds_read_b128 v[104:107], %[vl0] offset:14336\n"
        "v_fmac_f32 v156, v52, v43\n"
        "v_fmac_f32 v157, v53, v43\n"
        "v_mfma_f32_16x16x32_bf16 v[188:191], v[108:111], a[76:79], v[188:191]\n"
        "s_add_u32 s78, %[w2t0l], 0\n"
        "s_addc_u32 s79, %[w2t0h], 0\n"
        "s_add_u32 m0, %[mwa], 0\n"
        "s_nop 0\n"
        "global_load_lds_dwordx4 %[voff], s[78:79] offset:0 sc1\n"
        "v_fmac_f32 v158, v54, v43\n"
        "v_mfma_f32_16x16x32_bf16 v[192:195], v[112:115], a[72:75], v[192:195]\n"
        "ds_read_b128 v[108:111], %[vl0] offset:15360\n"
        "v_fmac_f32 v159, v55, v43\n"
        "ds_read_b128 v[48:51], %[vcs] offset:256\n"
        "v_mfma_f32_16x16x32_bf16 v[196:199], v[112:115], a[76:79], v[196:199]\n"
        "ds_read_b128 v[52:55], %[vcs] offset:320\n"
        "s_waitcnt lgkmcnt(8)\n"
        "v_mfma_f32_16x16x32_bf16 v[200:203], v[116:119], a[72:75], v[200:203]\n"
        "ds_read_b128 v[112:115], %[vl0] offset:16384\n"
        "v_fma_f32 v64, v64, v42, v42\n"
        "v_fma_f32 v65, v65, v42, v42\n"
        "v_mfma_f32_16x16x32_bf16 v[204:207], v[116:119], a[76:79], v[204:207]\n"
        "global_load_lds_dwordx4 %[voff], s[78:79] offset:1024 sc1\n"
        "v_fma_f32 v66, v66, v42, v42\n"
        "v_mfma_f32_16x16x32_bf16 v[176:179], v[120:123], a[80:83], v[176:179]\n"
        "ds_read_b128 v[116:119], %[vl0] offset:17408\n"
        "v_fma_f32 v67, v67, v42, v42\n"
        "v_fma_f32 v68, v68, v45, v45\n"
        "v_mfma_f32_16x16x32_bf16 v[180:183], v[120:123], a[84:87], v[180:183]\n"
        "v_fma_f32 v69, v69, v45, v45\n"
        "v_fma_f32 v70, v70, v45, v45\n"
        "v_mfma_f32_16x16x32_bf16 v[184:187], v[124:127], a[80:83], v[184:187]\n"
        "ds_read_b128 v[120:123], %[vl0] offset:18432\n"
        "v_fma_f32 v71, v71, v45, v45\n"
        "v_mfma_f32_16x16x32_bf16 v[188:191], v[124:127], a[84:87], v[188:191]\n"
        "global_load_lds_dwordx4 %[voff], s[78:79] offset:2048 sc1\n"
        "s_waitcnt lgkmcnt(8)\n"
        "v_mfma_f32_16x16x32_bf16 v[192:195], v[56:59], a[80:83], v[192:195]\n"
        "ds_read_b128 v[124:127], %[vl0] offset:19456\n"
        "v_rcp_f32 v64, v64\n"
        "v_mfma_f32_16x16x32_bf16 v[196:199], v[56:59], a[84:87], v[196:199]\n"
        "v_rcp_f32 v65, v65\n"
        "v_mfma_f32_16x16x32_bf16 v[200:203], v[60:63], a[80:83], v[200:203]\n"
        "ds_read_b128 v[56:59], %[vl0] offset:20480\n"
        "v_rcp_f32 v66, v66\n"
        "v_mfma_f32_16x16x32_bf16 v[204:207], v[60:63], a[84:87], v[204:207]\n"
        "global_load_lds_dwordx4 %[voff], s[78:79] offset:3072 sc1\n"
        "v_mfma_f32_16x16x32_bf16 v[176:179], v[96:99], a[88:91], v[176:179]\n"
        "ds_read_b128 v[60:63], %[vl0] offset:21504\n"
        "v_rcp_f32 v67, v67\n"
        "v_mfma_f32_16x16x32_bf16 v[180:183], v[96:99], a[92:95], v[180:183]\n"
        "s_waitcnt lgkmcnt(8)\n"
        "v_mfma_f32_16x16x32_bf16 v[184:187], v[100:103], a[88:91], v[184:187]\n"
        "ds_read_b128 v[96:99], %[vl0] offset:22528\n"
        "v_rcp_f32 v68, v68\n"
        "v_mfma_f32_16x16x32_bf16 v[188:191], v[100:103], a[92:95], v[188:191]\n"
        "s_add_u32 s78, %[w2t0l], 4096\n"
        "s_addc_u32 s79, %[w2t0h], 0\n"
        "s_add_u32 m0, %[mwa], 4096\n"
        "s_nop 0\n"
        "global_load_lds_dwordx4 %[voff], s[78:79] offset:0 sc1\n"
        "v_mfma_f32_16x16x32_bf16 v[192:195], v[104:107], a[88:91], v[192:195]\n"
        "ds_read_b128 v[100:103], %[vl0] offset:23552\n"
        "v_rcp_f32 v69, v69\n"
        "v_mfma_f32_16x16x32_bf16 v[196:199], v[104:107], a[92:95], v[196:199]\n"
        "v_rcp_f32 v70, v70\n"
        "v_mfma_f32_16x16x32_bf16 v[200:203], v[108:111], a[88:91], v[200:203]\n"
        "ds_read_b128 v[104:107], %[vl0] offset:24576\n"
        "v_rcp_f32 v71, v71\n"
        "v_mfma_f32_16x16x32_bf16 v[204:207], v[108:111], a[92:95], v[204:207]\n"
        "global_load_lds_dwordx4 %[voff], s[78:79] offset:1024 sc1\n"
        "s_waitcnt lgkmcnt(6)\n"
        "v_mfma_f32_16x16x32_bf16 v[176:179], v[112:115], a[96:99], v[176:179]\n"
        "ds_read_b128 v[108:111], %[vl0] offset:25600\n"
        "v_mul_f32 v144, v144, v152\n"
        "v_mul_f32 v145, v145, v153\n"
        "v_mfma_f32_16x16x32_bf16 v[180:183], v[112:115], a[100:103], v[180:183]\n"
        "v_mul_f32 v146, v146, v154\n"
        "v_mul_f32 v147, v147, v155\n"
        "v_mfma_f32_16x16x32_bf16 v[184:187], v[116:119], a[96:99], v[184:187]\n"
        "ds_read_b128 v[112:115], %[vl0] offset:26624\n"
        "v_mul_f32 v148, v148, v156\n"
        "v_mul_f32 v149, v149, v157\n"
        "v_mfma_f32_16x16x32_bf16 v[188:191], v[116:119], a[100:103], v[188:191]\n"
        "global_load_lds_dwordx4 %[voff], s[78:79] offset:2048 sc1\n"
        "v_mul_f32 v150, v150, v158\n"
        "v_mfma_f32_16x16x32_bf16 v[192:195], v[120:123], a[96:99], v[192:195]\n"
        "ds_read_b128 v[116:119], %[vl0] offset:27648\n"
        "v_mul_f32 v151, v151, v159\n"
        "v_mul_f32 v144, v144, v64\n"
        "v_mfma_f32_16x16x32_bf16 v[196:199], v[120:123], a[100:103], v[196:199]\n"
        "s_waitcnt vmcnt(16)\n"
        "s_barrier\n"
        "v_mul_f32 v145, v145, v65\n"
        "s_waitcnt lgkmcnt(6)\n"
        "v_mfma_f32_16x16x32_bf16 v[200:203], v[124:127], a[96:99], v[200:203]\n"
        "ds_read_b128 v[120:123], %[vl0] offset:28672\n"
        "v_mul_f32 v146, v146, v66\n"
        "v_mul_f32 v147, v147, v67\n"
        "v_mfma_f32_16x16x32_bf16 v[204:207], v[124:127], a[100:103], v[204:207]\n"
        "v_mul_f32 v148, v148, v68\n"
        "v_mul_f32 v149, v149, v69\n"
        "v_mfma_f32_16x16x32_bf16 v[176:179], v[56:59], a[104:107], v[176:179]\n"
        "ds_read_b128 v[124:127], %[vl0] offset:29696\n"
        "v_mul_f32 v150, v150, v70\n"
        "v_mul_f32 v151, v151, v71\n"
        "v_mfma_f32_16x16x32_bf16 v[180:183], v[56:59], a[108:111], v[180:183]\n"
        "s_add_u32 s78, %[w2t0l], 7168\n"
        "s_addc_u32 s79, %[w2t0h], 0\n"
        "s_add_u32 m0, %[mwb], 0\n"
        "s_nop 0\n"
        "global_load_lds_dwordx4 %[voff], s[78:79] offset:0 sc1\n"
        "v_cvt_pk_bf16_f32 v76, v144, v145\n"
        "v_mfma_f32_16x16x32_bf16 v[184:187], v[60:63], a[104:107], v[184:187]\n"
        "ds_read_b128 v[56:59], %[vl0] offset:30720\n"
        "v_cvt_pk_bf16_f32 v77, v146, v147\n"
        "v_cvt_pk_bf16_f32 v78, v148, v149\n"
        "v_mfma_f32_16x16x32_bf16 v[188:191], v[60:63], a[108:111], v[188:191]\n"
        "v_cvt_pk_bf16_f32 v79, v150, v151\n"
        "s_waitcnt lgkmcnt(6)\n"
        "v_mfma_f32_16x16x32_bf16 v[192:195], v[96:99], a[104:107], v[192:195]\n"
        "ds_read_b128 v[60:63], %[vl0] offset:31744\n"
        "s_nop 1\n"
        "v_permlane32_swap_b32 v76, v78\n"
        "v_mfma_f32_16x16x32_bf16 v[196:199], v[96:99], a[108:111], v[196:199]\n"
        "v_permlane32_swap_b32 v77, v79\n"
        "global_store_dwordx4 %[vh], v[76:79], %[sh] offset:-2048 sc1\n"
        "v_mfma_f32_16x16x32_bf16 v[200:203], v[100:103], a[104:107], v[200:203]\n"
        "ds_read_b128 v[96:99], %[vl0] offset:32768\n"
        "s_waitcnt lgkmcnt(15)\n"
        "v_fmac_f32 v160, v48, v40\n"
        "v_mfma_f32_16x16x32_bf16 v[204:207], v[100:103], a[108:111], v[204:207]\n"
        "global_load_lds_dwordx4 %[voff], s[78:79] offset:1024 sc1\n"
        "v_fmac_f32 v161, v49, v40\n"
        "v_mfma_f32_16x16x32_bf16 v[176:179], v[104:107], a[112:115], v[176:179]\n"
        "ds_read_b128 v[100:103], %[vl0] offset:33792\n"
        "v_fmac_f32 v162, v50, v40\n"
        "v_fmac_f32 v163, v51, v40\n"
        "v_mfma_f32_16x16x32_bf16 v[180:183], v[104:107], a[116:119], v[180:183]\n"
        "v_fmac_f32 v164, v48, v43\n"
        "s_waitcnt lgkmcnt(6)\n"
        "v_mfma_f32_16x16x32_bf16 v[184:187], v[108:111], a[112:115], v[184:187]\n"
        "ds_read_b128 v[104:107], %[vl0] offset:34816\n"
        "v_fmac_f32 v165, v49, v43\n"
        "v_fmac_f32 v166, v50, v43\n"
        "v_mfma_f32_16x16x32_bf16 v[188:191], v[108:111], a[116:119], v[188:191]\n"
        "v_fmac_f32 v167, v51, v43\n"
        "v_mul_f32 v64, v160, v41\n"
        "v_mfma_f32_16x16x32_bf16 v[192:195], v[112:115], a[112:115], v[192:195]\n"
        "ds_read_b128 v[108:111], %[vl0] offset:35840\n"
        "v_mul_f32 v65, v161, v41\n"
        "v_mul_f32 v66, v162, v41\n"
        "v_mfma_f32_16x16x32_bf16 v[196:199], v[112:115], a[116:119], v[196:199]\n"
        "global_load_lds_dwordx4 %[voff], s[78:79] offset:2048 sc1\n"
        "v_mul_f32 v67, v163, v41\n"
        "v_mfma_f32_16x16x32_bf16 v[200:203], v[116:119], a[112:115], v[200:203]\n"
        "ds_read_b128 v[112:115], %[vl0] offset:36864\n"
        "v_mul_f32 v68, v164, v44\n"
        "v_mul_f32 v69, v165, v44\n"
        "v_mfma_f32_16x16x32_bf16 v[204:207], v[116:119], a[116:119], v[204:207]\n"
        "v_mul_f32 v70, v166, v44\n"
        "s_waitcnt lgkmcnt(6)\n"
        "v_mfma_f32_16x16x32_bf16 v[176:179], v[120:123], a[120:123], v[176:179]\n"
        "ds_read_b128 v[116:119], %[vl0] offset:37888\n"
        "v_mul_f32 v71, v167, v44\n"
        "v_mfma_f32_16x16x32_bf16 v[180:183], v[120:123], a[124:127], v[180:183]\n"
        "v_exp_f32 v64, v64\n"
        "v_mfma_f32_16x16x32_bf16 v[184:187], v[124:127], a[120:123], v[184:187]\n"
        "ds_read_b128 v[120:123], %[vl0] offset:38912\n"
        "v_exp_f32 v65, v65\n"
        "v_mfma_f32_16x16x32_bf16 v[188:191], v[124:127], a[124:127], v[188:191]\n"
        "global_load_lds_dwordx4 %[voff], s[78:79] offset:3072 sc1\n"
        "v_mfma_f32_16x16x32_bf16 v[192:195], v[56:59], a[120:123], v[192:195]\n"
        "ds_read_b128 v[124:127], %[vl0] offset:39936\n"
        "v_exp_f32 v66, v66\n"
        "v_mfma_f32_16x16x32_bf16 v[196:199], v[56:59], a[124:127], v[196:199]\n"
        "s_waitcnt lgkmcnt(6)\n"
        "v_mfma_f32_16x16x32_bf16 v[200:203], v[60:63], a[120:123], v[200:203]\n"
        "ds_read_b128 v[56:59], %[vl0] offset:40960\n"
        "v_exp_f32 v67, v67\n"
        "v_mfma_f32_16x16x32_bf16 v[204:207], v[60:63], a[124:127], v[204:207]\n"
        "v_exp_f32 v68, v68\n"
        "v_mfma_f32_16x16x32_bf16 v[208:211], v[96:99], a[0:3], 0\n"
        "ds_read_b128 v[60:63], %[vl0] offset:41984\n"
        "v_exp_f32 v69, v69\n"
        "v_mfma_f32_16x16x32_bf16 v[212:215], v[96:99], a[4:7], 0\n"
        "s_add_u32 s78, %[w2t0l], 11264\n"
        "s_addc_u32 s79, %[w2t0h], 0\n"
        "s_add_u32 m0, %[mwb], 4096\n"
        "s_nop 0\n"
        "global_load_lds_dwordx4 %[voff], s[78:79] offset:0 sc1\n"
        "v_mfma_f32_16x16x32_bf16 v[216:219], v[100:103], a[0:3], 0\n"
        "ds_read_b128 v[96:99], %[vl0] offset:43008\n"
        "v_exp_f32 v70, v70\n"
        "v_mfma_f32_16x16x32_bf16 v[220:223], v[100:103], a[4:7], 0\n"
        "s_waitcnt lgkmcnt(6)\n"
        "v_mfma_f32_16x16x32_bf16 v[224:227], v[104:107], a[0:3], 0\n"
        "ds_read_b128 v[100:103], %[vl0] offset:44032\n"
        "v_exp_f32 v71, v71\n"
        "v_mfma_f32_16x16x32_bf16 v[228:231], v[104:107], a[4:7], 0\n"
        "v_fmac_f32 v168, v52, v40\n"
        "v_fmac_f32 v169, v53, v40\n"
        "v_mfma_f32_16x16x32_bf16 v[232:235], v[108:111], a[0:3], 0\n"
        "ds_read_b128 v[104:107], %[vl0] offset:45056\n"
        "v_fmac_f32 v170, v54, v40\n"
        "v_fmac_f32 v171, v55, v40\n"
        "v_mfma_f32_16x16x32_bf16 v[236:239], v[108:111], a[4:7], 0\n"
        "s_add_u32 s78, %[w2t1l], 0\n"
        "s_addc_u32 s79, %[w2t1h], 0\n"
        "s_add_u32 m0, %[mwb], 5120\n"
        "s_nop 0\n"
        "global_load_lds_dwordx4 %[voff], s[78:79] offset:0 sc1\n"
        "v_fmac_f32 v172, v52, v43\n"
        "v_mfma_f32_16x16x32_bf16 v[208:211], v[112:115], a[8:11], v[208:211]\n"
        "ds_read_b128 v[108:111], %[vl0] offset:46080\n"
        "v_fmac_f32 v173, v53, v43\n"
        "v_fmac_f32 v174, v54, v43\n"
        "v_mfma_f32_16x16x32_bf16 v[212:215], v[112:115], a[12:15], v[212:215]\n"
        "v_fmac_f32 v175, v55, v43\n"
        "s_waitcnt lgkmcnt(6)\n"
        "v_mfma_f32_16x16x32_bf16 v[216:219], v[116:119], a[8:11], v[216:219]\n"
        "ds_read_b128 v[112:115], %[vl0] offset:47104\n"
        "ds_read_b128 v[48:51], %[vcs] offset:384\n"
        "ds_read_b128 v[52:55], %[vcs] offset:448\n"
        "v_mfma_f32_16x16x32_bf16 v[220:223], v[116:119], a[12:15], v[220:223]\n"
        "v_fma_f32 v64, v64, v42, v42\n"
        "v_fma_f32 v65, v65, v42, v42\n"
        "v_mfma_f32_16x16x32_bf16 v[224:227], v[120:123], a[8:11], v[224:227]\n"
        "ds_read_b128 v[116:119], %[vl0] offset:48128\n"
        "v_fma_f32 v66, v66, v42, v42\n"
        "v_fma_f32 v67, v67, v42, v42\n"
        "v_mfma_f32_16x16x32_bf16 v[228:231], v[120:123], a[12:15], v[228:231]\n"
        "global_load_lds_dwordx4 %[voff], s[78:79] offset:1024 sc1\n"
        "v_fma_f32 v68, v68, v45, v45\n"
        "v_mfma_f32_16x16x32_bf16 v[232:235], v[124:127], a[8:11], v[232:235]\n"
        "ds_read_b128 v[120:123], %[vl0] offset:49152\n"
        "v_fma_f32 v69, v69, v45, v45\n"
        "v_fma_f32 v70, v70, v45, v45\n"
        "v_mfma_f32_16x16x32_bf16 v[236:239], v[124:127], a[12:15], v[236:239]\n"
        "v_fma_f32 v71, v71, v45, v45\n"
        "s_waitcnt lgkmcnt(8)\n"
        "v_mfma_f32_16x16x32_bf16 v[208:211], v[56:59], a[16:19], v[208:211]\n"
        "ds_read_b128 v[124:127], %[vl0] offset:50176\n"
        "v_rcp_f32 v64, v64\n"
        "v_mfma_f32_16x16x32_bf16 v[212:215], v[56:59], a[20:23], v[212:215]\n"
        "v_rcp_f32 v65, v65\n"
        "v_mfma_f32_16x16x32_bf16 v[216:219], v[60:63], a[16:19], v[216:219]\n"
        "ds_read_b128 v[56:59], %[vl0] offset:51200\n"
        "v_rcp_f32 v66, v66\n"
        "v_mfma_f32_16x16x32_bf16 v[220:223], v[60:63], a[20:23], v[220:223]\n"
        "global_load_lds_dwordx4 %[voff], s[78:79] offset:2048 sc1\n"
        "v_mfma_f32_16x16x32_bf16 v[224:227], v[96:99], a[16:19], v[224:227]\n"
        "ds_read_b128 v[60:63], %[vl0] offset:52224\n"
        "v_rcp_f32 v67, v67\n"
        "v_mfma_f32_16x16x32_bf16 v[228:231], v[96:99], a[20:23], v[228:231]\n"
        "s_waitcnt lgkmcnt(8)\n"
        "v_mfma_f32_16x16x32_bf16 v[232:235], v[100:103], a[16:19], v[232:235]\n"
        "ds_read_b128 v[96:99], %[vl0] offset:53248\n"
        "v_rcp_f32 v68, v68\n"
        "v_mfma_f32_16x16x32_bf16 v[236:239], v[100:103], a[20:23], v[236:239]\n"
        "v_rcp_f32 v69, v69\n"
        "v_mfma_f32_16x16x32_bf16 v[208:211], v[104:107], a[24:27], v[208:211]\n"
        "ds_read_b128 v[100:103], %[vl0] offset:54272\n"
        "v_rcp_f32 v70, v70\n"
        "v_mfma_f32_16x16x32_bf16 v[212:215], v[104:107], a[28:31], v[212:215]\n"
        "v_rcp_f32 v71, v71\n"
        "v_mfma_f32_16x16x32_bf16 v[216:219], v[108:111], a[24:27], v[216:219]\n"
        "ds_read_b128 v[104:107], %[vl0] offset:55296\n"
        "v_mul_f32 v160, v160, v168\n"
        "v_mul_f32 v161, v161, v169\n"
        "v_mfma_f32_16x16x32_bf16 v[220:223], v[108:111], a[28:31], v[220:223]\n"
        "v_mul_f32 v162, v162, v170\n"
        "s_waitcnt lgkmcnt(6)\n"
        "v_mfma_f32_16x16x32_bf16 v[224:227], v[112:115], a[24:27], v[224:227]\n"
        "ds_read_b128 v[108:111], %[vl0] offset:56320\n"
        "v_mul_f32 v163, v163, v171\n"
        "v_mul_f32 v164, v164, v172\n"
        "v_mfma_f32_16x16x32_bf16 v[228:231], v[112:115], a[28:31], v[228:231]\n"
        "v_mul_f32 v165, v165, v173\n"
        "v_mul_f32 v166, v166, v174\n"
        "v_mfma_f32_16x16x32_bf16 v[232:235], v[116:119], a[24:27], v[232:235]\n"
        "ds_read_b128 v[112:115], %[vl0] offset:57344\n"
        "v_mul_f32 v167, v167, v175\n"
        "v_mul_f32 v160, v160, v64\n"
        "v_mfma_f32_16x16x32_bf16 v[236:239], v[116:119], a[28:31], v[236:239]\n"
        "v_mul_f32 v161, v161, v65\n"
        "v_mul_f32 v162, v162, v66\n"
        "v_mfma_f32_16x16x32_bf16 v[208:211], v[120:123], a[32:35], v[208:211]\n"
        "ds_read_b128 v[116:119], %[vl0] offset:58368\n"
        "v_mul_f32 v163, v163, v67\n"
        "v_mul_f32 v164, v164, v68\n"
        "v_mfma_f32_16x16x32_bf16 v[212:215], v[120:123], a[36:39], v[212:215]\n"
        "v_mul_f32 v165, v165, v69\n"
        "s_waitcnt lgkmcnt(6)\n"
        "v_mfma_f32_16x16x32_bf16 v[216:219], v[124:127], a[32:35], v[216:219]\n"
        "ds_read_b128 v[120:123], %[vl0] offset:59392\n"
        "v_mul_f32 v166, v166, v70\n"
        "v_mul_f32 v167, v167, v71\n"
        "v_mfma_f32_16x16x32_bf16 v[220:223], v[124:127], a[36:39], v[220:223]\n"
        "v_cvt_pk_bf16_f32 v72, v160, v161\n"
        "v_cvt_pk_bf16_f32 v73, v162, v163\n"
        "v_mfma_f32_16x16x32_bf16 v[224:227], v[56:59], a[32:35], v[224:227]\n"
        "ds_read_b128 v[124:127], %[vl0] offset:60416\n"
        "v_cvt_pk_bf16_f32 v74, v164, v165\n"
        "v_cvt_pk_bf16_f32 v75, v166, v167\n"
        "v_mfma_f32_16x16x32_bf16 v[228:231], v[56:59], a[36:39], v[228:231]\n"
        "s_waitcnt vmcnt(16)\n"
        "s_barrier\n"
        "s_nop 1\n"
        "v_permlane32_swap_b32 v72, v74\n"
        "v_mfma_f32_16x16x32_bf16 v[232:235], v[60:63], a[32:35], v[232:235]\n"
        "ds_read_b128 v[56:59], %[vl0] offset:61440\n"
        "v_permlane32_swap_b32 v73, v75\n"
        "global_store_dwordx4 %[vh], v[72:75], %[sh] offset:-1024 sc1\n"
        "v_mfma_f32_16x16x32_bf16 v[236:239], v[60:63], a[36:39], v[236:239]\n"
        "s_waitcnt lgkmcnt(14)\n"
        "s_waitcnt lgkmcnt(6)\n"
        "v_mfma_f32_16x16x32_bf16 v[208:211], v[96:99], a[40:43], v[208:211]\n"
        "ds_read_b128 v[60:63], %[vl0] offset:62464\n"
        "v_fmac_f32 v176, v48, v40\n"
        "v_fmac_f32 v177, v49, v40\n"
        "v_mfma_f32_16x16x32_bf16 v[212:215], v[96:99], a[44:47], v[212:215]\n"
        "s_add_u32 s78, %[w2t1l], 3072\n"
        "s_addc_u32 s79, %[w2t1h], 0\n"
        "s_add_u32 m0, %[sm], 0\n"
        "s_nop 0\n"
        "global_load_lds_dwordx4 %[voff], s[78:79] offset:0 sc1\n"
        "v_fmac_f32 v178, v50, v40\n"
        "v_mfma_f32_16x16x32_bf16 v[216:219], v[100:103], a[40:43], v[216:219]\n"
        "ds_read_b128 v[96:99], %[vl0] offset:63488\n"
        "v_fmac_f32 v179, v51, v40\n"
        "v_fmac_f32 v180, v48, v43\n"
        "v_mfma_f32_16x16x32_bf16 v[220:223], v[100:103], a[44:47], v[220:223]\n"
        "v_fmac_f32 v181, v49, v43\n"
        "v_fmac_f32 v182, v50, v43\n"
        "v_mfma_f32_16x16x32_bf16 v[224:227], v[104:107], a[40:43], v[224:227]\n"
        "ds_read_b128 v[100:103], %[vl0] offset:64512\n"
        "v_fmac_f32 v183, v51, v43\n"
        "v_mul_f32 v64, v176, v41\n"
        "v_mfma_f32_16x16x32_bf16 v[228:231], v[104:107], a[44:47], v[228:231]\n"
        "v_mul_f32 v65, v177, v41\n"
        "s_waitcnt lgkmcnt(6)\n"
        "v_mfma_f32_16x16x32_bf16 v[232:235], v[108:111], a[40:43], v[232:235]\n"
        "ds_read_b128 v[104:107], %[vl1] offset:0\n"
        "v_mul_f32 v66, v178, v41\n"
        "v_mul_f32 v67, v179, v41\n"
        "v_mfma_f32_16x16x32_bf16 v[236:239], v[108:111], a[44:47], v[236:239]\n"
        "global_load_lds_dwordx4 %[voff], s[78:79] offset:1024 sc1\n"
        "v_mul_f32 v68, v180, v44\n"
        "v_mfma_f32_16x16x32_bf16 v[208:211], v[112:115], a[48:51], v[208:211]\n"
        "ds_read_b128 v[108:111], %[vl1] offset:1024\n"
        "v_mul_f32 v69, v181, v44\n"
        "v_mul_f32 v70, v182, v44\n"
        "v_mfma_f32_16x16x32_bf16 v[212:215], v[112:115], a[52:55], v[212:215]\n"
        "v_mul_f32 v71, v183, v44\n"
        "v_mfma_f32_16x16x32_bf16 v[216:219], v[116:119], a[48:51], v[216:219]\n"
        "ds_read_b128 v[112:115], %[vl1] offset:2048\n"
        "v_exp_f32 v64, v64\n"
        "v_mfma_f32_16x16x32_bf16 v[220:223], v[116:119], a[52:55], v[220:223]\n"
        "s_waitcnt lgkmcnt(6)\n"
        "v_mfma_f32_16x16x32_bf16 v[224:227], v[120:123], a[48:51], v[224:227]\n"
        "ds_read_b128 v[116:119], %[vl1] offset:3072\n"
        "v_exp_f32 v65, v65\n"
        "v_mfma_f32_16x16x32_bf16 v[228:231], v[120:123], a[52:55], v[228:231]\n"
        "global_load_lds_dwordx4 %[voff], s[78:79] offset:2048 sc1\n"
        "v_mfma_f32_16x16x32_bf16 v[232:235], v[124:127], a[48:51], v[232:235]\n"
        "ds_read_b128 v[120:123], %[vl1] offset:4096\n"
        "v_exp_f32 v66, v66\n"
        "v_mfma_f32_16x16x32_bf16 v[236:239], v[124:127], a[52:55], v[236:239]\n"
        "v_exp_f32 v67, v67\n"
        "v_mfma_f32_16x16x32_bf16 v[208:211], v[56:59], a[56:59], v[208:211]\n"
        "ds_read_b128 v[124:127], %[vl1] offset:5120\n"
        "v_exp_f32 v68, v68\n"
        "v_mfma_f32_16x16x32_bf16 v[212:215], v[56:59], a[60:63], v[212:215]\n"
        "s_waitcnt lgkmcnt(6)\n"
        "v_mfma_f32_16x16x32_bf16 v[216:219], v[60:63], a[56:59], v[216:219]\n"
        "ds_read_b128 v[56:59], %[vl1] offset:6144\n"
        "v_exp_f32 v69, v69\n"
        "v_mfma_f32_16x16x32_bf16 v[220:223], v[60:63], a[60:63], v[220:223]\n"
        "global_load_lds_dwordx4 %[voff], s[78:79] offset:3072 sc1\n"
        "v_mfma_f32_16x16x32_bf16 v[224:227], v[96:99], a[56:59], v[224:227]\n"
        "ds_read_b128 v[60:63], %[vl1] offset:7168\n"
        "v_exp_f32 v70, v70\n"
        "v_mfma_f32_16x16x32_bf16 v[228:231], v[96:99], a[60:63], v[228:231]\n"
        "v_exp_f32 v71, v71\n"
        "v_mfma_f32_16x16x32_bf16 v[232:235], v[100:103], a[56:59], v[232:235]\n"
        "ds_read_b128 v[96:99], %[vl1] offset:8192\n"
        "v_fmac_f32 v184, v52, v40\n"
        "v_fmac_f32 v185, v53, v40\n"
        "v_mfma_f32_16x16x32_bf16 v[236:239], v[100:103], a[60:63], v[236:239]\n"
        "v_fmac_f32 v186, v54, v40\n"
        "s_waitcnt lgkmcnt(6)\n"
        "v_mfma_f32_16x16x32_bf16 v[208:211], v[104:107], a[64:67], v[208:211]\n"
        "ds_read_b128 v[100:103], %[vl1] offset:9216\n"
        "v_fmac_f32 v187, v55, v40\n"
        "v_fmac_f32 v188, v52, v43\n"
        "v_mfma_f32_16x16x32_bf16 v[212:215], v[104:107], a[68:71], v[212:215]\n"
        "s_add_u32 s78, %[w2t1l], 7168\n"
        "s_addc_u32 s79, %[w2t1h], 0\n"
        "s_add_u32 m0, %[sm], 4096\n"
        "s_nop 0\n"
        "global_load_lds_dwordx4 %[voff], s[78:79] offset:0 sc1\n"
        "v_fmac_f32 v189, v53, v43\n"
        "v_mfma_f32_16x16x32_bf16 v[216:219], v[108:111], a[64:67], v[216:219]\n"
        "ds_read_b128 v[104:107], %[vl1] offset:10240\n"
        "v_fmac_f32 v190, v54, v43\n"
        "v_fmac_f32 v191, v55, v43\n"
        "v_mfma_f32_16x16x32_bf16 v[220:223], v[108:111], a[68:71], v[220:223]\n"
        "ds_read_b128 v[48:51], %[vcs] offset:512\n"
        "ds_read_b128 v[52:55], %[vcs] offset:576\n"
        "v_mfma_f32_16x16x32_bf16 v[224:227], v[112:115], a[64:67], v[224:227]\n"
        "ds_read_b128 v[108:111], %[vl1] offset:11264\n"
        "v_fma_f32 v64, v64, v42, v42\n"
        "v_fma_f32 v65, v65, v42, v42\n"
        "v_mfma_f32_16x16x32_bf16 v[228:231], v[112:115], a[68:71], v[228:231]\n"
        "v_fma_f32 v66, v66, v42, v42\n"
        "s_waitcnt lgkmcnt(8)\n"
        "v_mfma_f32_16x16x32_bf16 v[232:235], v[116:119], a[64:67], v[232:235]\n"
        "ds_read_b128 v[112:115], %[vl1] offset:12288\n"
        "v_fma_f32 v67, v67, v42, v42\n"
        "v_fma_f32 v68, v68, v45, v45\n"
        "v_mfma_f32_16x16x32_bf16 v[236:239], v[116:119], a[68:71], v[236:239]\n"
        "global_load_lds_dwordx4 %[voff], s[78:79] offset:1024 sc1\n"
        "v_fma_f32 v69, v69, v45, v45\n"
        "v_mfma_f32_16x16x32_bf16 v[208:211], v[120:123], a[72:75], v[208:211]\n"
        "ds_read_b128 v[116:119], %[vl1] offset:13312\n"
        "v_fma_f32 v70, v70, v45, v45\n"
        "v_fma_f32 v71, v71, v45, v45\n"
        "v_mfma_f32_16x16x32_bf16 v[212:215], v[120:123], a[76:79], v[212:215]\n"
        "v_rcp_f32 v64, v64\n"
        "v_mfma_f32_16x16x32_bf16 v[216:219], v[124:127], a[72:75], v[216:219]\n"
        "ds_read_b128 v[120:123], %[vl1] offset:14336\n"
        "v_rcp_f32 v65, v65\n"
        "v_mfma_f32_16x16x32_bf16 v[220:223], v[124:127], a[76:79], v[220:223]\n"
        "s_waitcnt lgkmcnt(8)\n"
        "v_mfma_f32_16x16x32_bf16 v[224:227], v[56:59], a[72:75], v[224:227]\n"
        "ds_read_b128 v[124:127], %[vl1] offset:15360\n"
        "v_rcp_f32 v66, v66\n"
        "v_mfma_f32_16x16x32_bf16 v[228:231], v[56:59], a[76:79], v[228:231]\n"
        "global_load_lds_dwordx4 %[voff], s[78:79] offset:2048 sc1\n"
        "v_mfma_f32_16x16x32_bf16 v[232:235], v[60:63], a[72:75], v[232:235]\n"
        "ds_read_b128 v[56:59], %[vl1] offset:16384\n"
        "v_rcp_f32 v67, v67\n"
        "v_mfma_f32_16x16x32_bf16 v[236:239], v[60:63], a[76:79], v[236:239]\n"
        "v_rcp_f32 v68, v68\n"
        "v_mfma_f32_16x16x32_bf16 v[208:211], v[96:99], a[80:83], v[208:211]\n"
        "ds_read_b128 v[60:63], %[vl1] offset:17408\n"
        "v_rcp_f32 v69, v69\n"
        "v_mfma_f32_16x16x32_bf16 v[212:215], v[96:99], a[84:87], v[212:215]\n"
        "s_waitcnt lgkmcnt(6)\n"
        "v_mfma_f32_16x16x32_bf16 v[216:219], v[100:103], a[80:83], v[216:219]\n"
        "ds_read_b128 v[96:99], %[vl1] offset:18432\n"
        "v_rcp_f32 v70, v70\n"
        "v_mfma_f32_16x16x32_bf16 v[220:223], v[100:103], a[84:87], v[220:223]\n"
        "global_load_lds_dwordx4 %[voff], s[78:79] offset:3072 sc1\n"
        "v_mfma_f32_16x16x32_bf16 v[224:227], v[104:107], a[80:83], v[224:227]\n"
        "ds_read_b128 v[100:103], %[vl1] offset:19456\n"
        "v_rcp_f32 v71, v71\n"
        "v_mfma_f32_16x16x32_bf16 v[228:231], v[104:107], a[84:87], v[228:231]\n"
        "v_mul_f32 v176, v176, v184\n"
        "v_mul_f32 v177, v177, v185\n"
        "v_mfma_f32_16x16x32_bf16 v[232:235], v[108:111], a[80:83], v[232:235]\n"
        "ds_read_b128 v[104:107], %[vl1] offset:20480\n"
        "v_mul_f32 v178, v178, v186\n"
        "v_mul_f32 v179, v179, v187\n"
        "v_mfma_f32_16x16x32_bf16 v[236:239], v[108:111], a[84:87], v[236:239]\n"
        "v_mul_f32 v180, v180, v188\n"
        "s_waitcnt lgkmcnt(6)\n"
        "v_mfma_f32_16x16x32_bf16 v[208:211], v[112:115], a[88:91], v[208:211]\n"
        "ds_read_b128 v[108:111], %[vl1] offset:21504\n"
        "v_mul_f32 v181, v181, v189\n"
        "v_mul_f32 v182, v182, v190\n"
        "v_mfma_f32_16x16x32_bf16 v[212:215], v[112:115], a[92:95], v[212:215]\n"
        "v_mul_f32 v183, v183, v191\n"
        "v_mul_f32 v176, v176, v64\n"
        "v_mfma_f32_16x16x32_bf16 v[216:219], v[116:119], a[88:91], v[216:219]\n"
        "ds_read_b128 v[112:115], %[vl1] offset:22528\n"
        "v_mul_f32 v177, v177, v65\n"
        "v_mul_f32 v178, v178, v66\n"
        "v_mfma_f32_16x16x32_bf16 v[220:223], v[116:119], a[92:95], v[220:223]\n"
        "v_mul_f32 v179, v179, v67\n"
        "v_mul_f32 v180, v180, v68\n"
        "v_mfma_f32_16x16x32_bf16 v[224:227], v[120:123], a[88:91], v[224:227]\n"
        "ds_read_b128 v[116:119], %[vl1] offset:23552\n"
        "v_mul_f32 v181, v181, v69\n"
        "v_mul_f32 v182, v182, v70\n"
        "v_mfma_f32_16x16x32_bf16 v[228:231], v[120:123], a[92:95], v[228:231]\n"
        "s_waitcnt vmcnt(8)\n"
        "s_barrier\n"
        "s_cmp_lg_u32 %[wid], 0\n"
        "s_cbranch_scc1 3f\n"
        "v_mov_b32 v80, 1\n"
        "v_mov_b32 v81, 0\n"
        "s_mov_b64 s[74:75], exec\n"
        "s_mov_b64 exec, 1\n"
        "global_atomic_add v81, v80, %[sfh] offset:64 sc1\n"
        "s_mov_b64 exec, s[74:75]\n"
        "3:\n"
        "v_mul_f32 v183, v183, v71\n"
        "s_waitcnt lgkmcnt(6)\n"
        "v_mfma_f32_16x16x32_bf16 v[232:235], v[124:127], a[88:91], v[232:235]\n"
        "ds_read_b128 v[120:123], %[vl1] offset:24576\n"
        "v_cvt_pk_bf16_f32 v76, v176, v177\n"
        "v_cvt_pk_bf16_f32 v77, v178, v179\n"
        "v_mfma_f32_16x16x32_bf16 v[236:239], v[124:127], a[92:95], v[236:239]\n"
        "v_cvt_pk_bf16_f32 v78, v180, v181\n"
        "v_cvt_pk_bf16_f32 v79, v182, v183\n"
        "v_mfma_f32_16x16x32_bf16 v[208:211], v[56:59], a[96:99], v[208:211]\n"
        "ds_read_b128 v[124:127], %[vl1] offset:25600\n"
        "s_nop 1\n"
        "v_permlane32_swap_b32 v76, v78\n"
        "v_mfma_f32_16x16x32_bf16 v[212:215], v[56:59], a[100:103], v[212:215]\n"
        "v_permlane32_swap_b32 v77, v79\n"
        "global_store_dwordx4 %[vh], v[76:79], %[sh] offset:0 sc1\n"
        "v_mfma_f32_16x16x32_bf16 v[216:219], v[60:63], a[96:99], v[216:219]\n"
        "ds_read_b128 v[56:59], %[vl1] offset:26624\n"
        "s_waitcnt lgkmcnt(15)\n"
        "v_fmac_f32 v192, v48, v40\n"
        "v_mfma_f32_16x16x32_bf16 v[220:223], v[60:63], a[100:103], v[220:223]\n"
        "v_fmac_f32 v193, v49, v40\n"
        "s_waitcnt lgkmcnt(6)\n"
        "v_mfma_f32_16x16x32_bf16 v[224:227], v[96:99], a[96:99], v[224:227]\n"
        "ds_read_b128 v[60:63], %[vl1] offset:27648\n"
        "v_fmac_f32 v194, v50, v40\n"
        "v_fmac_f32 v195, v51, v40\n"
        "v_mfma_f32_16x16x32_bf16 v[228:231], v[96:99], a[100:103], v[228:231]\n"
        "v_fmac_f32 v196, v48, v43\n"
        "v_fmac_f32 v197, v49, v43\n"
        "v_mfma_f32_16x16x32_bf16 v[232:235], v[100:103], a[96:99], v[232:235]\n"
        "ds_read_b128 v[96:99], %[vl1] offset:28672\n"
        "v_fmac_f32 v198, v50, v43\n"
        "v_fmac_f32 v199, v51, v43\n"
        "v_mfma_f32_16x16x32_bf16 v[236:239], v[100:103], a[100:103], v[236:239]\n"
        "v_mul_f32 v64, v192, v41\n"
        "v_mul_f32 v65, v193, v41\n"
        "v_mfma_f32_16x16x32_bf16 v[208:211], v[104:107], a[104:107], v[208:211]\n"
        "ds_read_b128 v[100:103], %[vl1] offset:29696\n"
        "v_mul_f32 v66, v194, v41\n"
        "v_mul_f32 v67, v195, v41\n"
        "v_mfma_f32_16x16x32_bf16 v[212:215], v[104:107], a[108:111], v[212:215]\n"
        "v_mul_f32 v68, v196, v44\n"
        "s_waitcnt lgkmcnt(6)\n"
        "v_mfma_f32_16x16x32_bf16 v[216:219], v[108:111], a[104:107], v[216:219]\n"
        "ds_read_b128 v[104:107], %[vl1] offset:30720\n"
        "v_mul_f32 v69, v197, v44\n"
        "v_mul_f32 v70, v198, v44\n"
        "v_mfma_f32_16x16x32_bf16 v[220:223], v[108:111], a[108:111], v[220:223]\n"
        "v_mul_f32 v71, v199, v44\n"
        "v_mfma_f32_16x16x32_bf16 v[224:227], v[112:115], a[104:107], v[224:227]\n"
        "ds_read_b128 v[108:111], %[vl1] offset:31744\n"
        "v_exp_f32 v64, v64\n"
        "v_mfma_f32_16x16x32_bf16 v[228:231], v[112:115], a[108:111], v[228:231]\n"
        "v_exp_f32 v65, v65\n"
        "v_mfma_f32_16x16x32_bf16 v[232:235], v[116:119], a[104:107], v[232:235]\n"
        "v_exp_f32 v66, v66\n"
        "v_mfma_f32_16x16x32_bf16 v[236:239], v[116:119], a[108:111], v[236:239]\n"
        "s_waitcnt lgkmcnt(5)\n"
        "v_mfma_f32_16x16x32_bf16 v[208:211], v[120:123], a[112:115], v[208:211]\n"
        "v_exp_f32 v67, v67\n"
        "v_mfma_f32_16x16x32_bf16 v[212:215], v[120:123], a[116:119], v[212:215]\n"
        "v_exp_f32 v68, v68\n"
        "v_mfma_f32_16x16x32_bf16 v[216:219], v[124:127], a[112:115], v[216:219]\n"
        "v_exp_f32 v69, v69\n"
        "v_mfma_f32_16x16x32_bf16 v[220:223], v[124:127], a[116:119], v[220:223]\n"
        "v_exp_f32 v70, v70\n"
        "v_mfma_f32_16x16x32_bf16 v[224:227], v[56:59], a[112:115], v[224:227]\n"
        "v_exp_f32 v71, v71\n"
        "v_mfma_f32_16x16x32_bf16 v[228:231], v[56:59], a[116:119], v[228:231]\n"
        "v_fmac_f32 v200, v52, v40\n"
        "s_waitcnt lgkmcnt(2)\n"
        "v_mfma_f32_16x16x32_bf16 v[232:235], v[60:63], a[112:115], v[232:235]\n"
        "v_fmac_f32 v201, v53, v40\n"
        "v_fmac_f32 v202, v54, v40\n"
        "v_mfma_f32_16x16x32_bf16 v[236:239], v[60:63], a[116:119], v[236:239]\n"
        "v_fmac_f32 v203, v55, v40\n"
        "v_fmac_f32 v204, v52, v43\n"
        "v_mfma_f32_16x16x32_bf16 v[208:211], v[96:99], a[120:123], v[208:211]\n"
        "v_fmac_f32 v205, v53, v43\n"
        "v_fmac_f32 v206, v54, v43\n"
        "v_mfma_f32_16x16x32_bf16 v[212:215], v[96:99], a[124:127], v[212:215]\n"
        "v_fmac_f32 v207, v55, v43\n"
        "ds_read_b128 v[48:51], %[vcs] offset:640\n"
        "v_mfma_f32_16x16x32_bf16 v[216:219], v[100:103], a[120:123], v[216:219]\n"
        "ds_read_b128 v[52:55], %[vcs] offset:704\n"
        "v_fma_f32 v64, v64, v42, v42\n"
        "v_mfma_f32_16x16x32_bf16 v[220:223], v[100:103], a[124:127], v[220:223]\n"
        "v_fma_f32 v65, v65, v42, v42\n"
        "s_waitcnt lgkmcnt(2)\n"
        "v_mfma_f32_16x16x32_bf16 v[224:227], v[104:107], a[120:123], v[224:227]\n"
        "v_fma_f32 v66, v66, v42, v42\n"
        "v_fma_f32 v67, v67, v42, v42\n"
        "v_mfma_f32_16x16x32_bf16 v[228:231], v[104:107], a[124:127], v[228:231]\n"
        "v_fma_f32 v68, v68, v45, v45\n"
        "v_fma_f32 v69, v69, v45, v45\n"
        "v_mfma_f32_16x16x32_bf16 v[232:235], v[108:111], a[120:123], v[232:235]\n"
        "v_fma_f32 v70, v70, v45, v45\n"
        "v_fma_f32 v71, v71, v45, v45\n"
        "v_mfma_f32_16x16x32_bf16 v[236:239], v[108:111], a[124:127], v[236:239]\n"
        "v_rcp_f32 v64, v64\n"
        "v_rcp_f32 v65, v65\n"
        "v_rcp_f32 v66, v66\n"
        "v_rcp_f32 v67, v67\n"
        "v_rcp_f32 v68, v68\n"
        "v_rcp_f32 v69, v69\n"
        "v_rcp_f32 v70, v70\n"
        "v_rcp_f32 v71, v71\n"
        "v_mul_f32 v192, v192, v200\n"
        "v_mul_f32 v193, v193, v201\n"
        "v_mul_f32 v194, v194, v202\n"
        "v_mul_f32 v195, v195, v203\n"
        "v_mul_f32 v196, v196, v204\n"
        "v_mul_f32 v197, v197, v205\n"
        "v_mul_f32 v198, v198, v206\n"
        "v_mul_f32 v199, v199, v207\n"
        "v_mul_f32 v192, v192, v64\n"
        "v_mul_f32 v193, v193, v65\n"
        "v_mul_f32 v194, v194, v66\n"
        "v_mul_f32 v195, v195, v67\n"
        "v_mul_f32 v196, v196, v68\n"
        "v_mul_f32 v197, v197, v69\n"
        "v_mul_f32 v198, v198, v70\n"
        "v_mul_f32 v199, v199, v71\n"
        "v_cvt_pk_bf16_f32 v72, v192, v193\n"
        "v_cvt_pk_bf16_f32 v73, v194, v195\n"
        "v_cvt_pk_bf16_f32 v74, v196, v197\n"
        "v_cvt_pk_bf16_f32 v75, v198, v199\n"
        "s_nop 1\n"
        "v_permlane32_swap_b32 v72, v74\n"
        "v_permlane32_swap_b32 v73, v75\n"
        "global_store_dwordx4 %[vh], v[72:75], %[sh] offset:1024 sc1\n"
        "s_add_u32 s78, s78, 4096\n"
        "s_addc_u32 s79, s79, 0\n"
        "global_load_dwordx4 a[96:99], %[voff], s[78:79] sc1\n"
        "v_mov_b32 v80, v40\n"
        "v_mov_b32 v81, v40\n"
        "v_mov_b32 v82, v41\n"
        "v_mov_b32 v83, v41\n"
        "v_mov_b32 v84, v42\n"
        "v_mov_b32 v85, v42\n"
        "v_mov_b32 v86, v43\n"
        "v_mov_b32 v87, v43\n"
        "v_mov_b32 v88, v44\n"
        "v_mov_b32 v89, v44\n"
        "v_mov_b32 v90, v45\n"
        "v_mov_b32 v91, v45\n"
        "ds_read_b128 v[96:99], %[vcs] offset:768\n"
        "ds_read_b128 v[100:103], %[vcs] offset:832\n"
        "s_waitcnt lgkmcnt(2)\n"
        "v_pk_fma_f32 v[208:209], v[48:49], v[80:81], v[208:209]\n"
        "v_pk_fma_f32 v[210:211], v[50:51], v[80:81], v[210:211]\n"
        "v_pk_fma_f32 v[212:213], v[48:49], v[86:87], v[212:213]\n"
        "v_pk_fma_f32 v[214:215], v[50:51], v[86:87], v[214:215]\n"
        "v_pk_mul_f32 v[64:65], v[208:209], v[82:83]\n"
        "v_pk_mul_f32 v[66:67], v[210:211], v[82:83]\n"
        "v_pk_mul_f32 v[68:69], v[212:213], v[88:89]\n"
        "v_pk_mul_f32 v[70:71], v[214:215], v[88:89]\n"
        "v_exp_f32 v64, v64\n"
        "v_exp_f32 v65, v65\n"
        "v_exp_f32 v66, v66\n"
        "v_exp_f32 v67, v67\n"
        "v_exp_f32 v68, v68\n"
        "v_exp_f32 v69, v69\n"
        "v_exp_f32 v70, v70\n"
        "v_exp_f32 v71, v71\n"
        "v_pk_fma_f32 v[216:217], v[52:53], v[80:81], v[216:217]\n"
        "v_pk_fma_f32 v[218:219], v[54:55], v[80:81], v[218:219]\n"
        "v_pk_fma_f32 v[220:221], v[52:53], v[86:87], v[220:221]\n"
        "v_pk_fma_f32 v[222:223], v[54:55], v[86:87], v[222:223]\n"
        "v_pk_fma_f32 v[64:65], v[64:65], v[84:85], v[84:85]\n"
        "v_pk_fma_f32 v[66:67], v[66:67], v[84:85], v[84:85]\n"
        "v_pk_fma_f32 v[68:69], v[68:69], v[90:91], v[90:91]\n"
        "v_pk_fma_f32 v[70:71], v[70:71], v[90:91], v[90:91]\n"
        "v_rcp_f32 v64, v64\n"
        "v_rcp_f32 v65, v65\n"
        "v_rcp_f32 v66, v66\n"
        "v_rcp_f32 v67, v67\n"
        "v_rcp_f32 v68, v68\n"
        "v_rcp_f32 v69, v69\n"
        "v_rcp_f32 v70, v70\n"
        "v_rcp_f32 v71, v71\n"
        "v_pk_mul_f32 v[208:209], v[208:209], v[216:217]\n"
        "v_pk_mul_f32 v[210:211], v[210:211], v[218:219]\n"
        "v_pk_mul_f32 v[212:213], v[212:213], v[220:221]\n"
        "v_pk_mul_f32 v[214:215], v[214:215], v[222:223]\n"
        "v_pk_mul_f32 v[208:209], v[208:209], v[64:65]\n"
        "v_pk_mul_f32 v[210:211], v[210:211], v[66:67]\n"
        "v_pk_mul_f32 v[212:213], v[212:213], v[68:69]\n"
        "v_pk_mul_f32 v[214:215], v[214:215], v[70:71]\n"
        "v_cvt_pk_bf16_f32 v72, v208, v209\n"
        "v_cvt_pk_bf16_f32 v73, v210, v211\n"
        "v_cvt_pk_bf16_f32 v74, v212, v213\n"
        "v_cvt_pk_bf16_f32 v75, v214, v215\n"
        "s_nop 1\n"
        "v_permlane32_swap_b32 v72, v74\n"
        "v_permlane32_swap_b32 v73, v75\n"
        "global_store_dwordx4 %[vh], v[72:75], %[sh] offset:2048 sc1\n"
        "s_waitcnt lgkmcnt(0)\n"
        "v_pk_fma_f32 v[224:225], v[96:97], v[80:81], v[224:225]\n"
        "v_pk_fma_f32 v[226:227], v[98:99], v[80:81], v[226:227]\n"
        "v_pk_fma_f32 v[228:229], v[96:97], v[86:87], v[228:229]\n"
        "v_pk_fma_f32 v[230:231], v[98:99], v[86:87], v[230:231]\n"
        "v_pk_mul_f32 v[112:113], v[224:225], v[82:83]\n"
        "v_pk_mul_f32 v[114:115], v[226:227], v[82:83]\n"
        "v_pk_mul_f32 v[116:117], v[228:229], v[88:89]\n"
        "v_pk_mul_f32 v[118:119], v[230:231], v[88:89]\n"
        "v_exp_f32 v112, v112\n"
        "v_exp_f32 v113, v113\n"
        "v_exp_f32 v114, v114\n"
        "v_exp_f32 v115, v115\n"
        "v_exp_f32 v116, v116\n"
        "v_exp_f32 v117, v117\n"
        "v_exp_f32 v118, v118\n"
        "v_exp_f32 v119, v119\n"
        "v_pk_fma_f32 v[232:233], v[100:101], v[80:81], v[232:233]\n"
        "v_pk_fma_f32 v[234:235], v[102:103], v[80:81], v[234:235]\n"
        "v_pk_fma_f32 v[236:237], v[100:101], v[86:87], v[236:237]\n"
        "v_pk_fma_f32 v[238:239], v[102:103], v[86:87], v[238:239]\n"
        "v_pk_fma_f32 v[112:113], v[112:113], v[84:85], v[84:85]\n"
        "v_pk_fma_f32 v[114:115], v[114:115], v[84:85], v[84:85]\n"
        "v_pk_fma_f32 v[116:117], v[116:117], v[90:91], v[90:91]\n"
        "v_pk_fma_f32 v[118:119], v[118:119], v[90:91], v[90:91]\n"
        "v_rcp_f32 v112, v112\n"
        "v_rcp_f32 v113, v113\n"
        "v_rcp_f32 v114, v114\n"
        "v_rcp_f32 v115, v115\n"
        "v_rcp_f32 v116, v116\n"
        "v_rcp_f32 v117, v117\n"
        "v_rcp_f32 v118, v118\n"
        "v_rcp_f32 v119, v119\n"
        "v_pk_mul_f32 v[224:225], v[224:225], v[232:233]\n"
        "v_pk_mul_f32 v[226:227], v[226:227], v[234:235]\n"
        "v_pk_mul_f32 v[228:229], v[228:229], v[236:237]\n"
        "v_pk_mul_f32 v[230:231], v[230:231], v[238:239]\n"
        "v_pk_mul_f32 v[224:225], v[224:225], v[112:113]\n"
        "v_pk_mul_f32 v[226:227], v[226:227], v[114:115]\n"
        "v_pk_mul_f32 v[228:229], v[228:229], v[116:117]\n"
        "v_pk_mul_f32 v[230:231], v[230:231], v[118:119]\n"
        "v_cvt_pk_bf16_f32 v120, v224, v225\n"
        "v_cvt_pk_bf16_f32 v121, v226, v227\n"
        "v_cvt_pk_bf16_f32 v122, v228, v229\n"
        "v_cvt_pk_bf16_f32 v123, v230, v231\n"
        "s_nop 1\n"
        "v_permlane32_swap_b32 v120, v122\n"
        "v_permlane32_swap_b32 v121, v123\n"
        "global_store_dwordx4 %[vh], v[120:123], %[sh] offset:3072 sc1\n"
        "v_mov_b32 v81, 0\n"
        "global_load_dword v80, v81, %[sfe] offset:64 sc1\n"
        "s_waitcnt vmcnt(0)\n"
        "s_barrier\n"
        "s_cmp_lg_u32 %[wid], 0\n"
        "s_cbranch_scc1 4f\n"
        "v_mov_b32 v64, 1\n"
        "v_mov_b32 v65, 0\n"
        "s_mov_b64 s[74:75], exec\n"
        "s_mov_b64 exec, 1\n"
        "global_atomic_add v65, v64, %[sfh] sc1\n"
        "s_mov_b64 exec, s[74:75]\n"
        "4:\n"
        "v_readfirstlane_b32 %[early], v80\n"
                : [early] "=s"(early), "={a[96:99]}"(w2e)
                : [voff] "v"(voff1), [vl0] "v"(vl0), [vl1] "v"(vl1), [vcs] "v"(vcs), [vx] "v"(vx), [vh] "v"(vh), [vbp] "v"(vbp), [vodd] "v"(vodd),
                  [wid] "s"(wid), [sfl] "s"(sfl), [sst] "s"(sst), [sx] "s"(sx), [sw_lo] "s"(w13l), [sw_hi] "s"(w13h), [sm] "s"(m13), [sh] "s"(sh),
                  [w2t0l] "s"(t0l), [w2t0h] "s"(t0h), [w2t1l] "s"(t1l), [w2t1h] "s"(t1h), [mwa] "s"(mwa), [mwb] "s"(mwb), [sfh] "s"(sfh), [sfe] "s"(sfe)
                : "memory", "scc", "vcc", "s72", "s73", "s74", "s75", "s76", "s77", "s78", "s79", "s80", "s81", "s82", "s83", "s84", "s85", "v40", "v41", "v42", "v43", "v44", "v45", "v46", "v47", "v48", "v49", "v50", "v51", "v52", "v53", "v54", "v55", "v56", "v57", "v58", "v59", "v60", "v61", "v62", "v63", "v64", "v65", "v66", "v67", "v68", "v69", "v70", "v71", "v72", "v73", "v74", "v75", "v76", "v77", "v78", "v79", "v80", "v81", "v82", "v83", "v84", "v85", "v86", "v87", "v88", "v89", "v90", "v91", "v92", "v93", "v94", "v95", "v96", "v97", "v98", "v99", "v100", "v101", "v102", "v103", "v104", "v105", "v106", "v107", "v108", "v109", "v110", "v111", "v112", "v113", "v114", "v115", "v116", "v117", "v118", "v119", "v120", "v121", "v122", "v123", "v124", "v125", "v126", "v127", "v128", "v129", "v130", "v131", "v132", "v133", "v134", "v135", "v136", "v137", "v138", "v139", "v140", "v141", "v142", "v143", "v144", "v145", "v146", "v147", "v148", "v149", "v150", "v151", "v152", "v153", "v154", "v155", "v156", "v157", "v158", "v159", "v160", "v161", "v162", "v163", "v164", "v165", "v166", "v167", "v168", "v169", "v170", "v171", "v172", "v173", "v174", "v175", "v176", "v177", "v178", "v179", "v180", "v181", "v182", "v183", "v184", "v185", "v186", "v187", "v188", "v189", "v190", "v191", "v192", "v193", "v194", "v195", "v196", "v197", "v198", "v199", "v200", "v201", "v202", "v203", "v204", "v205", "v206", "v207", "v208", "v209", "v210", "v211", "v212", "v213", "v214", "v215", "v216", "v217", "v218", "v219", "v220", "v221", "v222", "v223", "v224", "v225", "v226", "v227", "v228", "v229", "v230", "v231", "v232", "v233", "v234", "v235", "v236", "v237", "v238", "v239", "a0", "a1", "a2", "a3", "a4", "a5", "a6", "a7", "a8", "a9", "a10", "a11", "a12", "a13", "a14", "a15", "a16", "a17", "a18", "a19", "a20", "a21", "a22", "a23", "a24", "a25", "a26", "a27", "a28", "a29", "a30", "a31", "a32", "a33", "a34", "a35", "a36", "a37", "a38", "a39", "a40", "a41", "a42", "a43", "a44", "a45", "a46", "a47", "a48", "a49", "a50", "a51", "a52", "a53", "a54", "a55", "a56", "a57", "a58", "a59", "a60", "a61", "a62", "a63", "a64", "a65", "a66", "a67", "a68", "a69", "a70", "a71", "a72", "a73", "a74", "a75", "a76", "a77", "a78", "a79", "a80", "a81", "a82", "a83", "a84", "a85", "a86", "a87", "a88", "a89", "a90", "a91", "a92", "a93", "a94", "a95", "a100", "a101", "a102", "a103", "a104", "a105", "a106", "a107", "a108", "a109", "a110", "a111", "a112", "a113", "a114", "a115", "a116", "a117", "a118", "a119", "a120", "a121", "a122", "a123", "a124", "a125", "a126", "a127");
            SBAR();
#define RESD_OPS \
                : "=&{v[64:79]}"(c0d), "=&{v[24:31]}"(xd) \
                : "{a[128:159]}"(w2a), "{a[160:191]}"(w2b), "{a[192:223]}"(w2c), "{a[224:255]}"(w2d), "{a[96:99]}"(w2e), [early] "s"(early), \
                  [voff] "v"(voff1), [vla] "v"(vla), [vlb] "v"(vlb), [vlc] "v"(vlc), [sfl] "s"(sfe), \
                  [a0l] "s"(a0l), [a0h] "s"(a0h), [a1l] "s"(a1l), [a1h] "s"(a1h), [vwq] "v"(vwq), [swq] "s"(swq), [mwq] "s"(mwq), [sxr] "s"(sxr) \
                : "memory", "scc", "vcc", "s72", "s73", "s74", "s75", "s76", "s77", "s78", "s79", "s80", "s81", "s82", "s83", "s84", "s85", "v32", "v33", "v34", "v35", "v36", "v37", "v38", "v39", "v40", "v41", "v42", "v43", "v44", "v45", "v46", "v47", "v48", "v49", "v50", "v51", "v52", "v53", "v54", "v55", "v56", "v57", "v58", "v59", "v60", "v61", "v62", "v63", "v80", "v81", "v82", "v83", "v84", "v85", "v86", "v87", "v88", "v89", "v90", "v91", "v92", "v93", "v94", "v95", "v96", "v97", "v98", "v99", "v100", "v101", "v102", "v103", "v104", "v105", "v106", "v107", "v108", "v109", "v110", "v111", "v112", "v113", "v114", "v115", "v116", "v117", "v118", "v119", "v120", "v121", "v122", "v123", "v124", "v125", "v126", "v127", "v128", "v129", "v130", "v131", "v132", "v133", "v134", "v135", "v136", "v137", "v138", "v139", "v140", "v141", "v142", "v143", "v144", "v145", "v146", "v147", "v148", "v149", "v150", "v151", "v152", "v153", "v154", "v155", "v156", "v157", "v158", "v159", "v160", "v161", "v162", "v163", "v164", "v165", "v166", "v167", "v168", "v169", "v170", "v171", "v172", "v173", "v174", "v175", "v176", "v177", "v178", "v179", "v180", "v181", "v182", "v183", "v184", "v185", "v186", "v187", "v188", "v189", "v190", "v191", "v192", "v193", "v194", "v195", "v196", "v197", "v198", "v199", "v200", "v201", "v202", "v203", "v204", "v205", "v206", "v207", "v208", "v209", "v210", "v211", "v212", "v213", "v214", "v215", "v216", "v217", "v218", "v219", "v220", "v221", "v222", "v223", "v224", "v225", "v226", "v227", "v228", "v229", "v230", "v231", "v232", "v233", "v234", "v235", "v236", "v237", "v238", "v239", "v240", "v241", "v242", "v243", "v244", "v245", "v246", "v247", "v248", "v249", "v250", "v251", "v252", "v253", "v254", "v255", "a0", "a1", "a2", "a3", "a4", "a5", "a6", "a7", "a8", "a9", "a10", "a11", "a12", "a13", "a14", "a15", "a16", "a17", "a18", "a19", "a20", "a21", "a22", "a23", "a24", "a25", "a26", "a27", "a28", "a29", "a30", "a31", "a32", "a33", "a34", "a35", "a36", "a37", "a38", "a39", "a40", "a41", "a42", "a43", "a44", "a45", "a46", "a47", "a48", "a49", "a50", "a51", "a52", "a53", "a54", "a55", "a56", "a57", "a58", "a59", "a60", "a61", "a62", "a63", "a64", "a65", "a66", "a67", "a68", "a69", "a70", "a71", "a72", "a73", "a74", "a75", "a76", "a77", "a78", "a79", "a80", "a81", "a82", "a83", "a84", "a85", "a86", "a87", "a88", "a89", "a90", "a91", "a92", "a93", "a94", "a95"
            if (l == 0) asm volatile(
        "s_mov_b32 s72, 0x100000\n"
        "v_mov_b32 v255, 0\n"
        "s_mov_b32 s73, %[early]\n"
        "1:\n"
        "s_cmp_ge_u32 s73, 16\n"
        "s_cbranch_scc1 2f\n"
        "global_load_dword v254, v255, %[sfl] offset:64 sc1\n"
        "s_waitcnt vmcnt(0)\n"
        "v_readfirstlane_b32 s73, v254\n"
        "s_sub_u32 s72, s72, 1\n"
        "s_cbranch_scc0 1b\n"
        "2:\n"
        "ds_read_b128 a[64:67], %[vla] offset:0\n"
        "ds_read_b128 a[68:71], %[vlb] offset:5120\n"
        "ds_read_b128 a[72:75], %[vla] offset:1024\n"
        "ds_read_b128 a[76:79], %[vlb] offset:6144\n"
        "ds_read_b128 a[80:83], %[vla] offset:2048\n"
        "ds_read_b128 a[84:87], %[vlb] offset:7168\n"
        "s_add_u32 s74, %[a0l], 3072\n"
        "s_addc_u32 s75, %[a0h], 0\n"
        "global_load_dwordx4 v[32:35], %[voff], s[74:75] offset:-3072 sc1\n"
        "s_add_u32 s76, %[a1l], 3072\n"
        "s_addc_u32 s77, %[a1h], 0\n"
        "global_load_dwordx4 v[36:39], %[voff], s[76:77] offset:-3072 sc1\n"
        "global_load_dwordx4 v[40:43], %[voff], s[74:75] offset:-2048 sc1\n"
        "global_load_dwordx4 v[44:47], %[voff], s[76:77] offset:-2048 sc1\n"
        "global_load_dwordx4 v[48:51], %[voff], s[74:75] offset:-1024 sc1\n"
        "global_load_dwordx4 v[52:55], %[voff], s[76:77] offset:-1024 sc1\n"
        "s_add_u32 s74, %[a0l], 10240\n"
        "s_addc_u32 s75, %[a0h], 0\n"
        "global_load_dwordx4 v[56:59], %[voff], s[74:75] offset:-3072 sc1\n"
        "s_add_u32 s76, %[a1l], 10240\n"
        "s_addc_u32 s77, %[a1h], 0\n"
        "global_load_dwordx4 v[60:63], %[voff], s[76:77] offset:-3072 sc1\n"
        "global_load_dwordx4 v[64:67], %[voff], s[74:75] offset:-2048 sc1\n"
        "global_load_dwordx4 v[68:71], %[voff], s[76:77] offset:-2048 sc1\n"
        "global_load_dwordx4 v[72:75], %[voff], s[74:75] offset:-1024 sc1\n"
        "global_load_dwordx4 v[76:79], %[voff], s[76:77] offset:-1024 sc1\n"
        "s_add_u32 s74, %[a0l], 17408\n"
        "s_addc_u32 s75, %[a0h], 0\n"
        "global_load_dwordx4 v[80:83], %[voff], s[74:75] offset:-3072 sc1\n"
        "s_add_u32 s76, %[a1l], 17408\n"
        "s_addc_u32 s77, %[a1h], 0\n"
        "global_load_dwordx4 v[84:87], %[voff], s[76:77] offset:-3072 sc1\n"
        "s_waitcnt vmcnt(12)\n"
        "v_mfma_f32_32x32x16_bf16 a[0:15], a[128:131], v[32:35], 0\n"
        "global_load_dwordx4 v[88:91], %[voff], s[74:75] offset:-2048 sc1\n"
        "v_mfma_f32_32x32x16_bf16 a[16:31], a[128:131], v[36:39], 0\n"
        "v_mfma_f32_32x32x16_bf16 a[32:47], a[132:135], v[32:35], 0\n"
        "global_load_dwordx4 v[92:95], %[voff], s[76:77] offset:-2048 sc1\n"
        "v_mfma_f32_32x32x16_bf16 a[48:63], a[132:135], v[36:39], 0\n"
        "s_waitcnt vmcnt(12)\n"
        "v_mfma_f32_32x32x16_bf16 a[0:15], a[136:139], v[40:43], a[0:15]\n"
        "global_load_dwordx4 v[96:99], %[voff], s[74:75] offset:-1024 sc1\n"
        "v_mfma_f32_32x32x16_bf16 a[16:31], a[136:139], v[44:47], a[16:31]\n"
        "v_mfma_f32_32x32x16_bf16 a[32:47], a[140:143], v[40:43], a[32:47]\n"
        "global_load_dwordx4 v[100:103], %[voff], s[76:77] offset:-1024 sc1\n"
        "v_mfma_f32_32x32x16_bf16 a[48:63], a[140:143], v[44:47], a[48:63]\n"
        "s_waitcnt vmcnt(12)\n"
        "v_mfma_f32_32x32x16_bf16 a[0:15], a[144:147], v[48:51], a[0:15]\n"
        "s_add_u32 s74, %[a0l], 24576\n"
        "s_addc_u32 s75, %[a0h], 0\n"
        "global_load_dwordx4 v[104:107], %[voff], s[74:75] offset:-3072 sc1\n"
        "v_mfma_f32_32x32x16_bf16 a[16:31], a[144:147], v[52:55], a[16:31]\n"
        "v_mfma_f32_32x32x16_bf16 a[32:47], a[148:151], v[48:51], a[32:47]\n"
        "s_add_u32 s76, %[a1l], 24576\n"
        "s_addc_u32 s77, %[a1h], 0\n"
        "global_load_dwordx4 v[108:111], %[voff], s[76:77] offset:-3072 sc1\n"
        "v_mfma_f32_32x32x16_bf16 a[48:63], a[148:151], v[52:55], a[48:63]\n"
        "s_waitcnt vmcnt(12)\n"
        "v_mfma_f32_32x32x16_bf16 a[0:15], a[152:155], v[56:59], a[0:15]\n"
        "global_load_dwordx4 v[112:115], %[voff], s[74:75] offset:-2048 sc1\n"
        "v_mfma_f32_32x32x16_bf16 a[16:31], a[152:155], v[60:63], a[16:31]\n"
        "v_mfma_f32_32x32x16_bf16 a[32:47], a[156:159], v[56:59], a[32:47]\n"
        "global_load_dwordx4 v[116:119], %[voff], s[76:77] offset:-2048 sc1\n"
        "v_mfma_f32_32x32x16_bf16 a[48:63], a[156:159], v[60:63], a[48:63]\n"
        "s_waitcnt vmcnt(12)\n"
        "v_mfma_f32_32x32x16_bf16 a[0:15], a[160:163], v[64:67], a[0:15]\n"
        "global_load_dwordx4 v[120:123], %[voff], s[74:75] offset:-1024 sc1\n"
        "v_mfma_f32_32x32x16_bf16 a[16:31], a[160:163], v[68:71], a[16:31]\n"
        "v_mfma_f32_32x32x16_bf16 a[32:47], a[164:167], v[64:67], a[32:47]\n"
        "global_load_dwordx4 v[124:127], %[voff], s[76:77] offset:-1024 sc1\n"
        "v_mfma_f32_32x32x16_bf16 a[48:63], a[164:167], v[68:71], a[48:63]\n"
        "global_load_dword v254, v255, %[sfl] sc1\n"
        "s_waitcnt vmcnt(13)\n"
        "v_mfma_f32_32x32x16_bf16 a[0:15], a[168:171], v[72:75], a[0:15]\n"
        "s_mov_b32 m0, %[mwq]\n"
        "s_mov_b64 s[78:79], %[swq]\n"
        "global_load_lds_dwordx4 %[vwq], s[78:79]\n"
        "s_add_u32 m0, m0, 1024\n"
        "s_add_u32 s78, s78, 8192\n"
        "s_addc_u32 s79, s79, 0\n"
        "global_load_lds_dwordx4 %[vwq], s[78:79]\n"
        "v_mfma_f32_32x32x16_bf16 a[16:31], a[168:171], v[76:79], a[16:31]\n"
        "s_add_u32 m0, m0, 1024\n"
        "s_add_u32 s78, s78, 8192\n"
        "s_addc_u32 s79, s79, 0\n"
        "global_load_lds_dwordx4 %[vwq], s[78:79]\n"
        "s_add_u32 m0, m0, 1024\n"
        "s_add_u32 s78, s78, 8192\n"
        "s_addc_u32 s79, s79, 0\n"
        "global_load_lds_dwordx4 %[vwq], s[78:79]\n"
        "v_mfma_f32_32x32x16_bf16 a[32:47], a[172:175], v[72:75], a[32:47]\n"
        "s_add_u32 m0, m0, 1024\n"
        "s_add_u32 s78, s78, 8192\n"
        "s_addc_u32 s79, s79, 0\n"
        "global_load_lds_dwordx4 %[vwq], s[78:79]\n"
        "s_add_u32 m0, m0, 1024\n"
        "s_add_u32 s78, s78, 8192\n"
        "s_addc_u32 s79, s79, 0\n"
        "global_load_lds_dwordx4 %[vwq], s[78:79]\n"
        "v_mfma_f32_32x32x16_bf16 a[48:63], a[172:175], v[76:79], a[48:63]\n"
        "s_add_u32 m0, m0, 1024\n"
        "s_add_u32 s78, s78, 8192\n"
        "s_addc_u32 s79, s79, 0\n"
        "global_load_lds_dwordx4 %[vwq], s[78:79]\n"
        "s_add_u32 m0, m0, 1024\n"
        "s_add_u32 s78, s78, 8192\n"
        "s_addc_u32 s79, s79, 0\n"
        "global_load_lds_dwordx4 %[vwq], s[78:79]\n"
        "s_waitcnt vmcnt(19)\n"
        "v_mfma_f32_32x32x16_bf16 a[0:15], a[176:179], v[80:83], a[0:15]\n"
        "s_add_u32 m0, m0, 1024\n"
        "s_add_u32 s78, s78, 8192\n"
        "s_addc_u32 s79, s79, 0\n"
        "global_load_lds_dwordx4 %[vwq], s[78:79]\n"
        "s_add_u32 m0, m0, 1024\n"
        "s_add_u32 s78, s78, 8192\n"
        "s_addc_u32 s79, s79, 0\n"
        "global_load_lds_dwordx4 %[vwq], s[78:79]\n"
        "v_mfma_f32_32x32x16_bf16 a[16:31], a[176:179], v[84:87], a[16:31]\n"
        "s_add_u32 m0, m0, 1024\n"
        "s_add_u32 s78, s78, 8192\n"
        "s_addc_u32 s79, s79, 0\n"
        "global_load_lds_dwordx4 %[vwq], s[78:79]\n"
        "s_add_u32 m0, m0, 1024\n"
        "s_add_u32 s78, s78, 8192\n"
        "s_addc_u32 s79, s79, 0\n"
        "global_load_lds_dwordx4 %[vwq], s[78:79]\n"
        "v_mfma_f32_32x32x16_bf16 a[32:47], a[180:183], v[80:83], a[32:47]\n"
        "s_add_u32 m0, m0, 1024\n"
        "s_add_u32 s78, s78, 8192\n"
        "s_addc_u32 s79, s79, 0\n"
        "global_load_lds_dwordx4 %[vwq], s[78:79]\n"
        "s_add_u32 m0, m0, 1024\n"
        "s_add_u32 s78, s78, 8192\n"
        "s_addc_u32 s79, s79, 0\n"
        "global_load_lds_dwordx4 %[vwq], s[78:79]\n"
        "v_mfma_f32_32x32x16_bf16 a[48:63], a[180:183], v[84:87], a[48:63]\n"
        "s_add_u32 m0, m0, 1024\n"
        "s_add_u32 s78, s78, 8192\n"
        "s_addc_u32 s79, s79, 0\n"
        "global_load_lds_dwordx4 %[vwq], s[78:79]\n"
        "s_add_u32 m0, m0, 1024\n"
        "s_add_u32 s78, s78, 8192\n"
        "s_addc_u32 s79, s79, 0\n"
        "global_load_lds_dwordx4 %[vwq], s[78:79]\n"
        "s_waitcnt vmcnt(16)\n"
        "v_readfirstlane_b32 s73, v254\n"
        "3:\n"
        "s_cmp_ge_u32 s73, 16\n"
        "s_cbranch_scc1 4f\n"
        "global_load_dword v254, v255, %[sfl] sc1\n"
        "s_waitcnt vmcnt(0)\n"
        "v_readfirstlane_b32 s73, v254\n"
        "s_sub_u32 s72, s72, 1\n"
        "s_cbranch_scc0 3b\n"
        "4:\n"
        "s_add_u32 s74, %[a0l], 3072\n"
        "s_addc_u32 s75, %[a0h], 0\n"
        "global_load_dwordx4 v[128:131], %[voff], s[74:75] offset:0 sc1\n"
        "s_add_u32 s76, %[a1l], 3072\n"
        "s_addc_u32 s77, %[a1h], 0\n"
        "global_load_dwordx4 v[132:135], %[voff], s[76:77] offset:0 sc1\n"
        "global_load_dwordx4 v[136:139], %[voff], s[74:75] offset:1024 sc1\n"
        "global_load_dwordx4 v[140:143], %[voff], s[76:77] offset:1024 sc1\n"
        "s_waitcnt vmcnt(29)\n"
        "v_mfma_f32_32x32x16_bf16 a[0:15], a[184:187], v[88:91], a[0:15]\n"
        "global_load_dwordx4 v[144:147], %[voff], s[74:75] offset:2048 sc1\n"
        "v_mfma_f32_32x32x16_bf16 a[16:31], a[184:187], v[92:95], a[16:31]\n"
        "v_mfma_f32_32x32x16_bf16 a[32:47], a[188:191], v[88:91], a[32:47]\n"
        "global_load_dwordx4 v[148:151], %[voff], s[76:77] offset:2048 sc1\n"
        "v_mfma_f32_32x32x16_bf16 a[48:63], a[188:191], v[92:95], a[48:63]\n"
        "s_waitcnt vmcnt(29)\n"
        "v_mfma_f32_32x32x16_bf16 a[0:15], a[192:195], v[96:99], a[0:15]\n"
        "global_load_dwordx4 v[152:155], %[voff], s[74:75] offset:3072 sc1\n"
        "v_mfma_f32_32x32x16_bf16 a[16:31], a[192:195], v[100:103], a[16:31]\n"
        "v_mfma_f32_32x32x16_bf16 a[32:47], a[196:199], v[96:99], a[32:47]\n"
        "global_load_dwordx4 v[156:159], %[voff], s[76:77] offset:3072 sc1\n"
        "v_mfma_f32_32x32x16_bf16 a[48:63], a[196:199], v[100:103], a[48:63]\n"
        "s_waitcnt vmcnt(29)\n"
        "v_mfma_f32_32x32x16_bf16 a[0:15], a[200:203], v[104:107], a[0:15]\n"
        "s_add_u32 s74, %[a0l], 10240\n"
        "s_addc_u32 s75, %[a0h], 0\n"
        "global_load_dwordx4 v[160:163], %[voff], s[74:75] offset:0 sc1\n"
        "v_mfma_f32_32x32x16_bf16 a[16:31], a[200:203], v[108:111], a[16:31]\n"
        "v_mfma_f32_32x32x16_bf16 a[32:47], a[204:207], v[104:107], a[32:47]\n"
        "s_add_u32 s76, %[a1l], 10240\n"
        "s_addc_u32 s77, %[a1h], 0\n"
        "global_load_dwordx4 v[164:167], %[voff], s[76:77] offset:0 sc1\n"
        "v_mfma_f32_32x32x16_bf16 a[48:63], a[204:207], v[108:111], a[48:63]\n"
        "s_waitcnt vmcnt(29)\n"
        "v_mfma_f32_32x32x16_bf16 a[0:15], a[208:211], v[112:115], a[0:15]\n"
        "global_load_dwordx4 v[168:171], %[voff], s[74:75] offset:1024 sc1\n"
        "v_mfma_f32_32x32x16_bf16 a[16:31], a[208:211], v[116:119], a[16:31]\n"
        "v_mfma_f32_32x32x16_bf16 a[32:47], a[212:215], v[112:115], a[32:47]\n"
        "global_load_dwordx4 v[172:175], %[voff], s[76:77] offset:1024 sc1\n"
        "v_mfma_f32_32x32x16_bf16 a[48:63], a[212:215], v[116:119], a[48:63]\n"
        "s_waitcnt vmcnt(29)\n"
        "v_mfma_f32_32x32x16_bf16 a[0:15], a[216:219], v[120:123], a[0:15]\n"
        "global_load_dwordx4 v[176:179], %[voff], s[74:75] offset:2048 sc1\n"
        "v_mfma_f32_32x32x16_bf16 a[16:31], a[216:219], v[124:127], a[16:31]\n"
        "v_mfma_f32_32x32x16_bf16 a[32:47], a[220:223], v[120:123], a[32:47]\n"
        "global_load_dwordx4 v[180:183], %[voff], s[76:77] offset:2048 sc1\n"
        "v_mfma_f32_32x32x16_bf16 a[48:63], a[220:223], v[124:127], a[48:63]\n"
        "s_waitcnt vmcnt(12)\n"
        "v_mfma_f32_32x32x16_bf16 a[0:15], a[224:227], v[128:131], a[0:15]\n"
        "global_load_dwordx4 v[184:187], %[voff], s[74:75] offset:3072 sc1\n"
        "v_mfma_f32_32x32x16_bf16 a[16:31], a[224:227], v[132:135], a[16:31]\n"
        "v_mfma_f32_32x32x16_bf16 a[32:47], a[228:231], v[128:131], a[32:47]\n"
        "global_load_dwordx4 v[188:191], %[voff], s[76:77] offset:3072 sc1\n"
        "v_mfma_f32_32x32x16_bf16 a[48:63], a[228:231], v[132:135], a[48:63]\n"
        "s_waitcnt vmcnt(12)\n"
        "v_mfma_f32_32x32x16_bf16 a[0:15], a[232:235], v[136:139], a[0:15]\n"
        "s_add_u32 s74, %[a0l], 17408\n"
        "s_addc_u32 s75, %[a0h], 0\n"
        "global_load_dwordx4 v[192:195], %[voff], s[74:75] offset:0 sc1\n"
        "v_mfma_f32_32x32x16_bf16 a[16:31], a[232:235], v[140:143], a[16:31]\n"
        "v_mfma_f32_32x32x16_bf16 a[32:47], a[236:239], v[136:139], a[32:47]\n"
        "s_add_u32 s76, %[a1l], 17408\n"
        "s_addc_u32 s77, %[a1h], 0\n"
        "global_load_dwordx4 v[196:199], %[voff], s[76:77] offset:0 sc1\n"
        "v_mfma_f32_32x32x16_bf16 a[48:63], a[236:239], v[140:143], a[48:63]\n"
        "s_waitcnt vmcnt(12)\n"
        "v_mfma_f32_32x32x16_bf16 a[0:15], a[240:243], v[144:147], a[0:15]\n"
        "global_load_dwordx4 v[200:203], %[voff], s[74:75] offset:1024 sc1\n"
        "v_mfma_f32_32x32x16_bf16 a[16:31], a[240:243], v[148:151], a[16:31]\n"
        "v_mfma_f32_32x32x16_bf16 a[32:47], a[244:247], v[144:147], a[32:47]\n"
        "global_load_dwordx4 v[204:207], %[voff], s[76:77] offset:1024 sc1\n"
        "v_mfma_f32_32x32x16_bf16 a[48:63], a[244:247], v[148:151], a[48:63]\n"
        "s_waitcnt vmcnt(12)\n"
        "v_mfma_f32_32x32x16_bf16 a[0:15], a[248:251], v[152:155], a[0:15]\n"
        "global_load_dwordx4 v[208:211], %[voff], s[74:75] offset:2048 sc1\n"
        "v_mfma_f32_32x32x16_bf16 a[16:31], a[248:251], v[156:159], a[16:31]\n"
        "v_mfma_f32_32x32x16_bf16 a[32:47], a[252:255], v[152:155], a[32:47]\n"
        "global_load_dwordx4 v[212:215], %[voff], s[76:77] offset:2048 sc1\n"
        "v_mfma_f32_32x32x16_bf16 a[48:63], a[252:255], v[156:159], a[48:63]\n"
        "s_waitcnt vmcnt(12)\n"
        "s_waitcnt lgkmcnt(4)\n"
        "v_mfma_f32_32x32x16_bf16 a[0:15], a[64:67], v[160:163], a[0:15]\n"
        "global_load_dwordx4 v[216:219], %[voff], s[74:75] offset:3072 sc1\n"
        "v_mfma_f32_32x32x16_bf16 a[16:31], a[64:67], v[164:167], a[16:31]\n"
        "ds_read_b128 a[88:91], %[vla] offset:3072\n"
        "ds_read_b128 a[92:95], %[vlc] offset:0\n"
        "v_mfma_f32_32x32x16_bf16 a[32:47], a[68:71], v[160:163], a[32:47]\n"
        "global_load_dwordx4 v[220:223], %[voff], s[76:77] offset:3072 sc1\n"
        "v_mfma_f32_32x32x16_bf16 a[48:63], a[68:71], v[164:167], a[48:63]\n"
        "s_waitcnt vmcnt(12)\n"
        "s_waitcnt lgkmcnt(4)\n"
        "v_mfma_f32_32x32x16_bf16 a[0:15], a[72:75], v[168:171], a[0:15]\n"
        "s_add_u32 s74, %[a0l], 24576\n"
        "s_addc_u32 s75, %[a0h], 0\n"
        "global_load_dwordx4 v[224:227], %[voff], s[74:75] offset:0 sc1\n"
        "v_mfma_f32_32x32x16_bf16 a[16:31], a[72:75], v[172:175], a[16:31]\n"
        "ds_read_b128 a[64:67], %[vla] offset:4096\n"
        "ds_read_b128 a[68:71], %[vlc] offset:1024\n"
        "v_mfma_f32_32x32x16_bf16 a[32:47], a[76:79], v[168:171], a[32:47]\n"
        "s_add_u32 s76, %[a1l], 24576\n"
        "s_addc_u32 s77, %[a1h], 0\n"
        "global_load_dwordx4 v[228:231], %[voff], s[76:77] offset:0 sc1\n"
        "v_mfma_f32_32x32x16_bf16 a[48:63], a[76:79], v[172:175], a[48:63]\n"
        "s_waitcnt vmcnt(12)\n"
        "s_waitcnt lgkmcnt(4)\n"
        "v_mfma_f32_32x32x16_bf16 a[0:15], a[80:83], v[176:179], a[0:15]\n"
        "global_load_dwordx4 v[232:235], %[voff], s[74:75] offset:1024 sc1\n"
        "v_mfma_f32_32x32x16_bf16 a[16:31], a[80:83], v[180:183], a[16:31]\n"
        "ds_read_b128 a[72:75], %[vla] offset:5120\n"
        "ds_read_b128 a[76:79], %[vlc] offset:2048\n"
        "v_mfma_f32_32x32x16_bf16 a[32:47], a[84:87], v[176:179], a[32:47]\n"
        "global_load_dwordx4 v[236:239], %[voff], s[76:77] offset:1024 sc1\n"
        "v_mfma_f32_32x32x16_bf16 a[48:63], a[84:87], v[180:183], a[48:63]\n"
        "s_waitcnt vmcnt(12)\n"
        "s_waitcnt lgkmcnt(4)\n"
        "v_mfma_f32_32x32x16_bf16 a[0:15], a[88:91], v[184:187], a[0:15]\n"
        "global_load_dwordx4 v[240:243], %[voff], s[74:75] offset:2048 sc1\n"
        "v_mfma_f32_32x32x16_bf16 a[16:31], a[88:91], v[188:191], a[16:31]\n"
        "ds_read_b128 a[80:83], %[vla] offset:6144\n"
        "ds_read_b128 a[84:87], %[vlc] offset:3072\n"
        "v_mfma_f32_32x32x16_bf16 a[32:47], a[92:95], v[184:187], a[32:47]\n"
        "global_load_dwordx4 v[244:247], %[voff], s[76:77] offset:2048 sc1\n"
        "v_mfma_f32_32x32x16_bf16 a[48:63], a[92:95], v[188:191], a[48:63]\n"
        "s_waitcnt vmcnt(12)\n"
        "s_waitcnt lgkmcnt(4)\n"
        "v_mfma_f32_32x32x16_bf16 a[0:15], a[64:67], v[192:195], a[0:15]\n"
        "global_load_dwordx4 v[248:251], %[voff], s[74:75] offset:3072 sc1\n"
        "v_mfma_f32_32x32x16_bf16 a[16:31], a[64:67], v[196:199], a[16:31]\n"
        "ds_read_b128 a[88:91], %[vlb] offset:0\n"
        "ds_read_b128 a[92:95], %[vlc] offset:4096\n"
        "v_mfma_f32_32x32x16_bf16 a[32:47], a[68:71], v[192:195], a[32:47]\n"
        "global_load_dwordx4 v[252:255], %[voff], s[76:77] offset:3072 sc1\n"
        "v_mfma_f32_32x32x16_bf16 a[48:63], a[68:71], v[196:199], a[48:63]\n"
        "s_waitcnt vmcnt(12)\n"
        "s_waitcnt lgkmcnt(4)\n"
        "v_mfma_f32_32x32x16_bf16 a[0:15], a[72:75], v[200:203], a[0:15]\n"
        "v_mfma_f32_32x32x16_bf16 a[16:31], a[72:75], v[204:207], a[16:31]\n"
        "ds_read_b128 a[64:67], %[vlb] offset:1024\n"
        "ds_read_b128 a[68:71], %[vlc] offset:5120\n"
        "v_mfma_f32_32x32x16_bf16 a[32:47], a[76:79], v[200:203], a[32:47]\n"
        "v_mfma_f32_32x32x16_bf16 a[48:63], a[76:79], v[204:207], a[48:63]\n"
        "s_waitcnt vmcnt(10)\n"
        "s_waitcnt lgkmcnt(4)\n"
        "v_mfma_f32_32x32x16_bf16 a[0:15], a[80:83], v[208:211], a[0:15]\n"
        "v_mfma_f32_32x32x16_bf16 a[16:31], a[80:83], v[212:215], a[16:31]\n"
        "ds_read_b128 a[72:75], %[vlb] offset:2048\n"
        "ds_read_b128 a[76:79], %[vlc] offset:6144\n"
        "v_mfma_f32_32x32x16_bf16 a[32:47], a[84:87], v[208:211], a[32:47]\n"
        "v_mfma_f32_32x32x16_bf16 a[48:63], a[84:87], v[212:215], a[48:63]\n"
        "s_waitcnt vmcnt(8)\n"
        "s_waitcnt lgkmcnt(4)\n"
        "v_mfma_f32_32x32x16_bf16 a[0:15], a[88:91], v[216:219], a[0:15]\n"
        "global_load_dwordx4 v[24:27], %[voff], %[sxr] offset:0 sc1\n"
        "v_mfma_f32_32x32x16_bf16 a[16:31], a[88:91], v[220:223], a[16:31]\n"
        "ds_read_b128 a[80:83], %[vlb] offset:3072\n"
        "ds_read_b128 a[84:87], %[vlc] offset:7168\n"
        "v_mfma_f32_32x32x16_bf16 a[32:47], a[92:95], v[216:219], a[32:47]\n"
        "global_load_dwordx4 v[28:31], %[voff], %[sxr] offset:1024 sc1\n"
        "v_mfma_f32_32x32x16_bf16 a[48:63], a[92:95], v[220:223], a[48:63]\n"
        "s_waitcnt vmcnt(8)\n"
        "s_waitcnt lgkmcnt(4)\n"
        "v_mfma_f32_32x32x16_bf16 a[0:15], a[64:67], v[224:227], a[0:15]\n"
        "v_mfma_f32_32x32x16_bf16 a[16:31], a[64:67], v[228:231], a[16:31]\n"
        "ds_read_b128 a[88:91], %[vlb] offset:4096\n"
        "v_mfma_f32_32x32x16_bf16 a[32:47], a[68:71], v[224:227], a[32:47]\n"
        "v_mfma_f32_32x32x16_bf16 a[48:63], a[68:71], v[228:231], a[48:63]\n"
        "s_waitcnt vmcnt(6)\n"
        "s_waitcnt lgkmcnt(3)\n"
        "v_mfma_f32_32x32x16_bf16 a[16:31], a[72:75], v[236:239], a[16:31]\n"
        "v_mfma_f32_32x32x16_bf16 a[32:47], a[76:79], v[232:235], a[32:47]\n"
        "v_mfma_f32_32x32x16_bf16 a[48:63], a[76:79], v[236:239], a[48:63]\n"
        "s_waitcnt vmcnt(4)\n"
        "s_waitcnt lgkmcnt(1)\n"
        "v_mfma_f32_32x32x16_bf16 a[16:31], a[80:83], v[244:247], a[16:31]\n"
        "v_mfma_f32_32x32x16_bf16 a[32:47], a[84:87], v[240:243], a[32:47]\n"
        "v_mfma_f32_32x32x16_bf16 a[48:63], a[84:87], v[244:247], a[48:63]\n"
        "s_waitcnt vmcnt(2)\n"
        "s_waitcnt lgkmcnt(0)\n"
        "v_mfma_f32_32x32x16_bf16 a[16:31], a[88:91], v[252:255], a[16:31]\n"
        "v_mfma_f32_32x32x16_bf16 a[32:47], a[96:99], v[248:251], a[32:47]\n"
        "v_mfma_f32_32x32x16_bf16 a[48:63], a[96:99], v[252:255], a[48:63]\n"
        "v_mfma_f32_32x32x16_bf16 a[0:15], a[72:75], v[232:235], a[0:15]\n"
        "s_nop 8\n"
        "ds_write_b128 %[vlc], a[16:19] offset:0\n"
        "ds_write_b128 %[vlc], a[20:23] offset:1024\n"
        "ds_write_b128 %[vlc], a[24:27] offset:2048\n"
        "ds_write_b128 %[vlc], a[28:31] offset:3072\n"
        "v_mfma_f32_32x32x16_bf16 a[0:15], a[80:83], v[240:243], a[0:15]\n"
        "s_nop 3\n"
        "ds_write_b128 %[vlc], a[32:35] offset:4096\n"
        "ds_write_b128 %[vlc], a[36:39] offset:5120\n"
        "ds_write_b128 %[vlc], a[40:43] offset:6144\n"
        "ds_write_b128 %[vlc], a[44:47] offset:7168\n"
        "v_mfma_f32_32x32x16_bf16 a[0:15], a[88:91], v[248:251], a[0:15]\n"
        "s_nop 3\n"
        "ds_write_b128 %[vlb], a[48:51] offset:0\n"
        "ds_write_b128 %[vlb], a[52:55] offset:1024\n"
        "ds_write_b128 %[vlb], a[56:59] offset:2048\n"
        "ds_write_b128 %[vlb], a[60:63] offset:3072\n"
        "s_nop 3\n"
        "v_accvgpr_read_b32 v64, a0\n"
        "v_accvgpr_read_b32 v65, a1\n"
        "v_accvgpr_read_b32 v66, a2\n"
        "v_accvgpr_read_b32 v67, a3\n"
        "v_accvgpr_read_b32 v68, a4\n"
        "v_accvgpr_read_b32 v69, a5\n"
        "v_accvgpr_read_b32 v70, a6\n"
        "v_accvgpr_read_b32 v71, a7\n"
        "v_accvgpr_read_b32 v72, a8\n"
        "v_accvgpr_read_b32 v73, a9\n"
        "v_accvgpr_read_b32 v74, a10\n"
        "v_accvgpr_read_b32 v75, a11\n"
        "v_accvgpr_read_b32 v76, a12\n"
        "v_accvgpr_read_b32 v77, a13\n"
        "v_accvgpr_read_b32 v78, a14\n"
        "v_accvgpr_read_b32 v79, a15\n"
        "s_waitcnt vmcnt(0) lgkmcnt(0)\n"
        "s_barrier\n"
                RESD_OPS);
            else asm volatile(
        "s_mov_b32 s72, 0x100000\n"
        "v_mov_b32 v255, 0\n"
        "s_mov_b32 s73, %[early]\n"
        "1:\n"
        "s_cmp_ge_u32 s73, 16\n"
        "s_cbranch_scc1 2f\n"
        "global_load_dword v254, v255, %[sfl] offset:64 sc1\n"
        "s_waitcnt vmcnt(0)\n"
        "v_readfirstlane_b32 s73, v254\n"
        "s_sub_u32 s72, s72, 1\n"
        "s_cbranch_scc0 1b\n"
        "2:\n"
        "ds_read_b128 a[64:67], %[vla] offset:0\n"
        "ds_read_b128 a[68:71], %[vlb] offset:5120\n"
        "ds_read_b128 a[72:75], %[vla] offset:1024\n"
        "ds_read_b128 a[76:79], %[vlb] offset:6144\n"
        "ds_read_b128 a[80:83], %[vla] offset:2048\n"
        "ds_read_b128 a[84:87], %[vlb] offset:7168\n"
        "s_add_u32 s74, %[a0l], 3072\n"
        "s_addc_u32 s75, %[a0h], 0\n"
        "global_load_dwordx4 v[32:35], %[voff], s[74:75] offset:-3072 sc1\n"
        "s_add_u32 s76, %[a1l], 3072\n"
        "s_addc_u32 s77, %[a1h], 0\n"
        "global_load_dwordx4 v[36:39], %[voff], s[76:77] offset:-3072 sc1\n"
        "global_load_dwordx4 v[40:43], %[voff], s[74:75] offset:-2048 sc1\n"
        "global_load_dwordx4 v[44:47], %[voff], s[76:77] offset:-2048 sc1\n"
        "global_load_dwordx4 v[48:51], %[voff], s[74:75] offset:-1024 sc1\n"
        "global_load_dwordx4 v[52:55], %[voff], s[76:77] offset:-1024 sc1\n"
        "s_add_u32 s74, %[a0l], 10240\n"
        "s_addc_u32 s75, %[a0h], 0\n"
        "global_load_dwordx4 v[56:59], %[voff], s[74:75] offset:-3072 sc1\n"
        "s_add_u32 s76, %[a1l], 10240\n"
        "s_addc_u32 s77, %[a1h], 0\n"
        "global_load_dwordx4 v[60:63], %[voff], s[76:77] offset:-3072 sc1\n"
        "global_load_dwordx4 v[64:67], %[voff], s[74:75] offset:-2048 sc1\n"
        "global_load_dwordx4 v[68:71], %[voff], s[76:77] offset:-2048 sc1\n"
        "global_load_dwordx4 v[72:75], %[voff], s[74:75] offset:-1024 sc1\n"
        "global_load_dwordx4 v[76:79], %[voff], s[76:77] offset:-1024 sc1\n"
        "s_add_u32 s74, %[a0l], 17408\n"
        "s_addc_u32 s75, %[a0h], 0\n"
        "global_load_dwordx4 v[80:83], %[voff], s[74:75] offset:-3072 sc1\n"
        "s_add_u32 s76, %[a1l], 17408\n"
        "s_addc_u32 s77, %[a1h], 0\n"
        "global_load_dwordx4 v[84:87], %[voff], s[76:77] offset:-3072 sc1\n"
        "s_waitcnt vmcnt(12)\n"
        "v_mfma_f32_32x32x16_bf16 a[0:15], a[128:131], v[32:35], 0\n"
        "global_load_dwordx4 v[88:91], %[voff], s[74:75] offset:-2048 sc1\n"
        "v_mfma_f32_32x32x16_bf16 a[16:31], a[128:131], v[36:39], 0\n"
        "v_mfma_f32_32x32x16_bf16 a[32:47], a[132:135], v[32:35], 0\n"
        "global_load_dwordx4 v[92:95], %[voff], s[76:77] offset:-2048 sc1\n"
        "v_mfma_f32_32x32x16_bf16 a[48:63], a[132:135], v[36:39], 0\n"
        "s_waitcnt vmcnt(12)\n"
        "v_mfma_f32_32x32x16_bf16 a[0:15], a[136:139], v[40:43], a[0:15]\n"
        "global_load_dwordx4 v[96:99], %[voff], s[74:75] offset:-1024 sc1\n"
        "v_mfma_f32_32x32x16_bf16 a[16:31], a[136:139], v[44:47], a[16:31]\n"
        "v_mfma_f32_32x32x16_bf16 a[32:47], a[140:143], v[40:43], a[32:47]\n"
        "global_load_dwordx4 v[100:103], %[voff], s[76:77] offset:-1024 sc1\n"
        "v_mfma_f32_32x32x16_bf16 a[48:63], a[140:143], v[44:47], a[48:63]\n"
        "s_waitcnt vmcnt(12)\n"
        "v_mfma_f32_32x32x16_bf16 a[0:15], a[144:147], v[48:51], a[0:15]\n"
        "s_add_u32 s74, %[a0l], 24576\n"
        "s_addc_u32 s75, %[a0h], 0\n"
        "global_load_dwordx4 v[104:107], %[voff], s[74:75] offset:-3072 sc1\n"
        "v_mfma_f32_32x32x16_bf16 a[16:31], a[144:147], v[52:55], a[16:31]\n"
        "v_mfma_f32_32x32x16_bf16 a[32:47], a[148:151], v[48:51], a[32:47]\n"
        "s_add_u32 s76, %[a1l], 24576\n"
        "s_addc_u32 s77, %[a1h], 0\n"
        "global_load_dwordx4 v[108:111], %[voff], s[76:77] offset:-3072 sc1\n"
        "v_mfma_f32_32x32x16_bf16 a[48:63], a[148:151], v[52:55], a[48:63]\n"
        "s_waitcnt vmcnt(12)\n"
        "v_mfma_f32_32x32x16_bf16 a[0:15], a[152:155], v[56:59], a[0:15]\n"
        "global_load_dwordx4 v[112:115], %[voff], s[74:75] offset:-2048 sc1\n"
        "v_mfma_f32_32x32x16_bf16 a[16:31], a[152:155], v[60:63], a[16:31]\n"
        "v_mfma_f32_32x32x16_bf16 a[32:47], a[156:159], v[56:59], a[32:47]\n"
        "global_load_dwordx4 v[116:119], %[voff], s[76:77] offset:-2048 sc1\n"
        "v_mfma_f32_32x32x16_bf16 a[48:63], a[156:159], v[60:63], a[48:63]\n"
        "s_waitcnt vmcnt(12)\n"
        "v_mfma_f32_32x32x16_bf16 a[0:15], a[160:163], v[64:67], a[0:15]\n"
        "global_load_dwordx4 v[120:123], %[voff], s[74:75] offset:-1024 sc1\n"
        "v_mfma_f32_32x32x16_bf16 a[16:31], a[160:163], v[68:71], a[16:31]\n"
        "v_mfma_f32_32x32x16_bf16 a[32:47], a[164:167], v[64:67], a[32:47]\n"
        "global_load_dwordx4 v[124:127], %[voff], s[76:77] offset:-1024 sc1\n"
        "v_mfma_f32_32x32x16_bf16 a[48:63], a[164:167], v[68:71], a[48:63]\n"
        "global_load_dword v254, v255, %[sfl] sc1\n"
        "s_waitcnt vmcnt(13)\n"
        "v_mfma_f32_32x32x16_bf16 a[0:15], a[168:171], v[72:75], a[0:15]\n"
        "v_mfma_f32_32x32x16_bf16 a[16:31], a[168:171], v[76:79], a[16:31]\n"
        "v_mfma_f32_32x32x16_bf16 a[32:47], a[172:175], v[72:75], a[32:47]\n"
        "v_mfma_f32_32x32x16_bf16 a[48:63], a[172:175], v[76:79], a[48:63]\n"
        "s_waitcnt vmcnt(11)\n"
        "v_mfma_f32_32x32x16_bf16 a[0:15], a[176:179], v[80:83], a[0:15]\n"
        "v_mfma_f32_32x32x16_bf16 a[16:31], a[176:179], v[84:87], a[16:31]\n"
        "v_mfma_f32_32x32x16_bf16 a[32:47], a[180:183], v[80:83], a[32:47]\n"
        "v_mfma_f32_32x32x16_bf16 a[48:63], a[180:183], v[84:87], a[48:63]\n"
        "s_waitcnt vmcnt(0)\n"
        "v_readfirstlane_b32 s73, v254\n"
        "3:\n"
        "s_cmp_ge_u32 s73, 16\n"
        "s_cbranch_scc1 4f\n"
        "global_load_dword v254, v255, %[sfl] sc1\n"
        "s_waitcnt vmcnt(0)\n"
        "v_readfirstlane_b32 s73, v254\n"
        "s_sub_u32 s72, s72, 1\n"
        "s_cbranch_scc0 3b\n"
        "4:\n"
        "s_add_u32 s74, %[a0l], 3072\n"
        "s_addc_u32 s75, %[a0h], 0\n"
        "global_load_dwordx4 v[128:131], %[voff], s[74:75] offset:0 sc1\n"
        "s_add_u32 s76, %[a1l], 3072\n"
        "s_addc_u32 s77, %[a1h], 0\n"
        "global_load_dwordx4 v[132:135], %[voff], s[76:77] offset:0 sc1\n"
        "global_load_dwordx4 v[136:139], %[voff], s[74:75] offset:1024 sc1\n"
        "global_load_dwordx4 v[140:143], %[voff], s[76:77] offset:1024 sc1\n"
        "s_waitcnt vmcnt(13)\n"
        "v_mfma_f32_32x32x16_bf16 a[0:15], a[184:187], v[88:91], a[0:15]\n"
        "global_load_dwordx4 v[144:147], %[voff], s[74:75] offset:2048 sc1\n"
        "v_mfma_f32_32x32x16_bf16 a[16:31], a[184:187], v[92:95], a[16:31]\n"
        "v_mfma_f32_32x32x16_bf16 a[32:47], a[188:191], v[88:91], a[32:47]\n"
        "global_load_dwordx4 v[148:151], %[voff], s[76:77] offset:2048 sc1\n"
        "v_mfma_f32_32x32x16_bf16 a[48:63], a[188:191], v[92:95], a[48:63]\n"
        "s_waitcnt vmcnt(13)\n"
        "v_mfma_f32_32x32x16_bf16 a[0:15], a[192:195], v[96:99], a[0:15]\n"
        "global_load_dwordx4 v[152:155], %[voff], s[74:75] offset:3072 sc1\n"
        "v_mfma_f32_32x32x16_bf16 a[16:31], a[192:195], v[100:103], a[16:31]\n"
        "v_mfma_f32_32x32x16_bf16 a[32:47], a[196:199], v[96:99], a[32:47]\n"
        "global_load_dwordx4 v[156:159], %[voff], s[76:77] offset:3072 sc1\n"
        "v_mfma_f32_32x32x16_bf16 a[48:63], a[196:199], v[100:103], a[48:63]\n"
        "s_waitcnt vmcnt(13)\n"
        "v_mfma_f32_32x32x16_bf16 a[0:15], a[200:203], v[104:107], a[0:15]\n"
        "s_add_u32 s74, %[a0l], 10240\n"
        "s_addc_u32 s75, %[a0h], 0\n"
        "global_load_dwordx4 v[160:163], %[voff], s[74:75] offset:0 sc1\n"
        "v_mfma_f32_32x32x16_bf16 a[16:31], a[200:203], v[108:111], a[16:31]\n"
        "v_mfma_f32_32x32x16_bf16 a[32:47], a[204:207], v[104:107], a[32:47]\n"
        "s_add_u32 s76, %[a1l], 10240\n"
        "s_addc_u32 s77, %[a1h], 0\n"
        "global_load_dwordx4 v[164:167], %[voff], s[76:77] offset:0 sc1\n"
        "v_mfma_f32_32x32x16_bf16 a[48:63], a[204:207], v[108:111], a[48:63]\n"
        "s_waitcnt vmcnt(13)\n"
        "v_mfma_f32_32x32x16_bf16 a[0:15], a[208:211], v[112:115], a[0:15]\n"
        "global_load_dwordx4 v[168:171], %[voff], s[74:75] offset:1024 sc1\n"
        "v_mfma_f32_32x32x16_bf16 a[16:31], a[208:211], v[116:119], a[16:31]\n"
        "v_mfma_f32_32x32x16_bf16 a[32:47], a[212:215], v[112:115], a[32:47]\n"
        "global_load_dwordx4 v[172:175], %[voff], s[76:77] offset:1024 sc1\n"
        "v_mfma_f32_32x32x16_bf16 a[48:63], a[212:215], v[116:119], a[48:63]\n"
        "s_waitcnt vmcnt(13)\n"
        "v_mfma_f32_32x32x16_bf16 a[0:15], a[216:219], v[120:123], a[0:15]\n"
        "global_load_dwordx4 v[176:179], %[voff], s[74:75] offset:2048 sc1\n"
        "v_mfma_f32_32x32x16_bf16 a[16:31], a[216:219], v[124:127], a[16:31]\n"
        "v_mfma_f32_32x32x16_bf16 a[32:47], a[220:223], v[120:123], a[32:47]\n"
        "global_load_dwordx4 v[180:183], %[voff], s[76:77] offset:2048 sc1\n"
        "v_mfma_f32_32x32x16_bf16 a[48:63], a[220:223], v[124:127], a[48:63]\n"
        "s_waitcnt vmcnt(12)\n"
        "v_mfma_f32_32x32x16_bf16 a[0:15], a[224:227], v[128:131], a[0:15]\n"
        "global_load_dwordx4 v[184:187], %[voff], s[74:75] offset:3072 sc1\n"
        "v_mfma_f32_32x32x16_bf16 a[16:31], a[224:227], v[132:135], a[16:31]\n"
        "v_mfma_f32_32x32x16_bf16 a[32:47], a[228:231], v[128:131], a[32:47]\n"
        "global_load_dwordx4 v[188:191], %[voff], s[76:77] offset:3072 sc1\n"
        "v_mfma_f32_32x32x16_bf16 a[48:63], a[228:231], v[132:135], a[48:63]\n"
        "s_waitcnt vmcnt(12)\n"
        "v_mfma_f32_32x32x16_bf16 a[0:15], a[232:235], v[136:139], a[0:15]\n"
        "s_add_u32 s74, %[a0l], 17408\n"
        "s_addc_u32 s75, %[a0h], 0\n"
        "global_load_dwordx4 v[192:195], %[voff], s[74:75] offset:0 sc1\n"
        "v_mfma_f32_32x32x16_bf16 a[16:31], a[232:235], v[140:143], a[16:31]\n"
        "v_mfma_f32_32x32x16_bf16 a[32:47], a[236:239], v[136:139], a[32:47]\n"
        "s_add_u32 s76, %[a1l], 17408\n"
        "s_addc_u32 s77, %[a1h], 0\n"
        "global_load_dwordx4 v[196:199], %[voff], s[76:77] offset:0 sc1\n"
        "v_mfma_f32_32x32x16_bf16 a[48:63], a[236:239], v[140:143], a[48:63]\n"
        "s_waitcnt vmcnt(12)\n"
        "v_mfma_f32_32x32x16_bf16 a[0:15], a[240:243], v[144:147], a[0:15]\n"
        "global_load_dwordx4 v[200:203], %[voff], s[74:75] offset:1024 sc1\n"
        "v_mfma_f32_32x32x16_bf16 a[16:31], a[240:243], v[148:151], a[16:31]\n"
        "v_mfma_f32_32x32x16_bf16 a[32:47], a[244:247], v[144:147], a[32:47]\n"
        "global_load_dwordx4 v[204:207], %[voff], s[76:77] offset:1024 sc1\n"
        "v_mfma_f32_32x32x16_bf16 a[48:63], a[244:247], v[148:151], a[48:63]\n"
        "s_waitcnt vmcnt(12)\n"
        "v_mfma_f32_32x32x16_bf16 a[0:15], a[248:251], v[152:155], a[0:15]\n"
        "global_load_dwordx4 v[208:211], %[voff], s[74:75] offset:2048 sc1\n"
        "v_mfma_f32_32x32x16_bf16 a[16:31], a[248:251], v[156:159], a[16:31]\n"
        "v_mfma_f32_32x32x16_bf16 a[32:47], a[252:255], v[152:155], a[32:47]\n"
        "global_load_dwordx4 v[212:215], %[voff], s[76:77] offset:2048 sc1\n"
        "v_mfma_f32_32x32x16_bf16 a[48:63], a[252:255], v[156:159], a[48:63]\n"
        "s_waitcnt vmcnt(12)\n"
        "s_waitcnt lgkmcnt(4)\n"
        "v_mfma_f32_32x32x16_bf16 a[0:15], a[64:67], v[160:163], a[0:15]\n"
        "global_load_dwordx4 v[216:219], %[voff], s[74:75] offset:3072 sc1\n"
        "v_mfma_f32_32x32x16_bf16 a[16:31], a[64:67], v[164:167], a[16:31]\n"
        "ds_read_b128 a[88:91], %[vla] offset:3072\n"
        "ds_read_b128 a[92:95], %[vlc] offset:0\n"
        "v_mfma_f32_32x32x16_bf16 a[32:47], a[68:71], v[160:163], a[32:47]\n"
        "global_load_dwordx4 v[220:223], %[voff], s[76:77] offset:3072 sc1\n"
        "v_mfma_f32_32x32x16_bf16 a[48:63], a[68:71], v[164:167], a[48:63]\n"
        "s_waitcnt vmcnt(12)\n"
        "s_waitcnt lgkmcnt(4)\n"
        "v_mfma_f32_32x32x16_bf16 a[0:15], a[72:75], v[168:171], a[0:15]\n"
        "s_add_u32 s74, %[a0l], 24576\n"
        "s_addc_u32 s75, %[a0h], 0\n"
        "global_load_dwordx4 v[224:227], %[voff], s[74:75] offset:0 sc1\n"
        "v_mfma_f32_32x32x16_bf16 a[16:31], a[72:75], v[172:175], a[16:31]\n"
        "ds_read_b128 a[64:67], %[vla] offset:4096\n"
        "ds_read_b128 a[68:71], %[vlc] offset:1024\n"
        "v_mfma_f32_32x32x16_bf16 a[32:47], a[76:79], v[168:171], a[32:47]\n"
        "s_add_u32 s76, %[a1l], 24576\n"
        "s_addc_u32 s77, %[a1h], 0\n"
        "global_load_dwordx4 v[228:231], %[voff], s[76:77] offset:0 sc1\n"
        "v_mfma_f32_32x32x16_bf16 a[48:63], a[76:79], v[172:175], a[48:63]\n"
        "s_waitcnt vmcnt(12)\n"
        "s_waitcnt lgkmcnt(4)\n"
        "v_mfma_f32_32x32x16_bf16 a[0:15], a[80:83], v[176:179], a[0:15]\n"
        "global_load_dwordx4 v[232:235], %[voff], s[74:75] offset:1024 sc1\n"
        "v_mfma_f32_32x32x16_bf16 a[16:31], a[80:83], v[180:183], a[16:31]\n"
        "ds_read_b128 a[72:75], %[vla] offset:5120\n"
        "ds_read_b128 a[76:79], %[vlc] offset:2048\n"
        "v_mfma_f32_32x32x16_bf16 a[32:47], a[84:87], v[176:179], a[32:47]\n"
        "global_load_dwordx4 v[236:239], %[voff], s[76:77] offset:1024 sc1\n"
        "v_mfma_f32_32x32x16_bf16 a[48:63], a[84:87], v[180:183], a[48:63]\n"
        "s_waitcnt vmcnt(12)\n"
        "s_waitcnt lgkmcnt(4)\n"
        "v_mfma_f32_32x32x16_bf16 a[0:15], a[88:91], v[184:187], a[0:15]\n"
        "global_load_dwordx4 v[240:243], %[voff], s[74:75] offset:2048 sc1\n"
        "v_mfma_f32_32x32x16_bf16 a[16:31], a[88:91], v[188:191], a[16:31]\n"
        "ds_read_b128 a[80:83], %[vla] offset:6144\n"
        "ds_read_b128 a[84:87], %[vlc] offset:3072\n"
        "v_mfma_f32_32x32x16_bf16 a[32:47], a[92:95], v[184:187], a[32:47]\n"
        "global_load_dwordx4 v[244:247], %[voff], s[76:77] offset:2048 sc1\n"
        "v_mfma_f32_32x32x16_bf16 a[48:63], a[92:95], v[188:191], a[48:63]\n"
        "s_waitcnt vmcnt(12)\n"
        "s_waitcnt lgkmcnt(4)\n"
        "v_mfma_f32_32x32x16_bf16 a[0:15], a[64:67], v[192:195], a[0:15]\n"
        "global_load_dwordx4 v[248:251], %[voff], s[74:75] offset:3072 sc1\n"
        "v_mfma_f32_32x32x16_bf16 a[16:31], a[64:67], v[196:199], a[16:31]\n"
        "ds_read_b128 a[88:91], %[vlb] offset:0\n"
        "ds_read_b128 a[92:95], %[vlc] offset:4096\n"
        "v_mfma_f32_32x32x16_bf16 a[32:47], a[68:71], v[192:195], a[32:47]\n"
        "global_load_dwordx4 v[252:255], %[voff], s[76:77] offset:3072 sc1\n"
        "v_mfma_f32_32x32x16_bf16 a[48:63], a[68:71], v[196:199], a[48:63]\n"
        "s_waitcnt vmcnt(12)\n"
        "s_waitcnt lgkmcnt(4)\n"
        "v_mfma_f32_32x32x16_bf16 a[0:15], a[72:75], v[200:203], a[0:15]\n"
        "v_mfma_f32_32x32x16_bf16 a[16:31], a[72:75], v[204:207], a[16:31]\n"
        "ds_read_b128 a[64:67], %[vlb] offset:1024\n"
        "ds_read_b128 a[68:71], %[vlc] offset:5120\n"
        "v_mfma_f32_32x32x16_bf16 a[32:47], a[76:79], v[200:203], a[32:47]\n"
        "v_mfma_f32_32x32x16_bf16 a[48:63], a[76:79], v[204:207], a[48:63]\n"
        "s_waitcnt vmcnt(10)\n"
        "s_waitcnt lgkmcnt(4)\n"
        "v_mfma_f32_32x32x16_bf16 a[0:15], a[80:83], v[208:211], a[0:15]\n"
        "v_mfma_f32_32x32x16_bf16 a[16:31], a[80:83], v[212:215], a[16:31]\n"
        "ds_read_b128 a[72:75], %[vlb] offset:2048\n"
        "ds_read_b128 a[76:79], %[vlc] offset:6144\n"
        "v_mfma_f32_32x32x16_bf16 a[32:47], a[84:87], v[208:211], a[32:47]\n"
        "v_mfma_f32_32x32x16_bf16 a[48:63], a[84:87], v[212:215], a[48:63]\n"
        "s_waitcnt vmcnt(8)\n"
        "s_waitcnt lgkmcnt(4)\n"
        "v_mfma_f32_32x32x16_bf16 a[0:15], a[88:91], v[216:219], a[0:15]\n"
        "global_load_dwordx4 v[24:27], %[voff], %[sxr] offset:0 sc1\n"
        "v_mfma_f32_32x32x16_bf16 a[16:31], a[88:91], v[220:223], a[16:31]\n"
        "ds_read_b128 a[80:83], %[vlb] offset:3072\n"
        "ds_read_b128 a[84:87], %[vlc] offset:7168\n"
        "v_mfma_f32_32x32x16_bf16 a[32:47], a[92:95], v[216:219], a[32:47]\n"
        "global_load_dwordx4 v[28:31], %[voff], %[sxr] offset:1024 sc1\n"
        "v_mfma_f32_32x32x16_bf16 a[48:63], a[92:95], v[220:223], a[48:63]\n"
        "s_waitcnt vmcnt(8)\n"
        "s_waitcnt lgkmcnt(4)\n"
        "v_mfma_f32_32x32x16_bf16 a[0:15], a[64:67], v[224:227], a[0:15]\n"
        "v_mfma_f32_32x32x16_bf16 a[16:31], a[64:67], v[228:231], a[16:31]\n"
        "ds_read_b128 a[88:91], %[vlb] offset:4096\n"
        "v_mfma_f32_32x32x16_bf16 a[32:47], a[68:71], v[224:227], a[32:47]\n"
        "v_mfma_f32_32x32x16_bf16 a[48:63], a[68:71], v[228:231], a[48:63]\n"
        "s_waitcnt vmcnt(6)\n"
        "s_waitcnt lgkmcnt(3)\n"
        "v_mfma_f32_32x32x16_bf16 a[16:31], a[72:75], v[236:239], a[16:31]\n"
        "v_mfma_f32_32x32x16_bf16 a[32:47], a[76:79], v[232:235], a[32:47]\n"
        "v_mfma_f32_32x32x16_bf16 a[48:63], a[76:79], v[236:239], a[48:63]\n"
        "s_waitcnt vmcnt(4)\n"
        "s_waitcnt lgkmcnt(1)\n"
        "v_mfma_f32_32x32x16_bf16 a[16:31], a[80:83], v[244:247], a[16:31]\n"
        "v_mfma_f32_32x32x16_bf16 a[32:47], a[84:87], v[240:243], a[32:47]\n"
        "v_mfma_f32_32x32x16_bf16 a[48:63], a[84:87], v[244:247], a[48:63]\n"
        "s_waitcnt vmcnt(2)\n"
        "s_waitcnt lgkmcnt(0)\n"
        "v_mfma_f32_32x32x16_bf16 a[16:31], a[88:91], v[252:255], a[16:31]\n"
        "v_mfma_f32_32x32x16_bf16 a[32:47], a[96:99], v[248:251], a[32:47]\n"
        "v_mfma_f32_32x32x16_bf16 a[48:63], a[96:99], v[252:255], a[48:63]\n"
        "v_mfma_f32_32x32x16_bf16 a[0:15], a[72:75], v[232:235], a[0:15]\n"
        "s_nop 8\n"
        "ds_write_b128 %[vlc], a[16:19] offset:0\n"
        "ds_write_b128 %[vlc], a[20:23] offset:1024\n"
        "ds_write_b128 %[vlc], a[24:27] offset:2048\n"
        "ds_write_b128 %[vlc], a[28:31] offset:3072\n"
        "v_mfma_f32_32x32x16_bf16 a[0:15], a[80:83], v[240:243], a[0:15]\n"
        "s_nop 3\n"
        "ds_write_b128 %[vlc], a[32:35] offset:4096\n"
        "ds_write_b128 %[vlc], a[36:39] offset:5120\n"
        "ds_write_b128 %[vlc], a[40:43] offset:6144\n"
        "ds_write_b128 %[vlc], a[44:47] offset:7168\n"
        "v_mfma_f32_32x32x16_bf16 a[0:15], a[88:91], v[248:251], a[0:15]\n"
        "s_nop 3\n"
        "ds_write_b128 %[vlb], a[48:51] offset:0\n"
        "ds_write_b128 %[vlb], a[52:55] offset:1024\n"
        "ds_write_b128 %[vlb], a[56:59] offset:2048\n"
        "ds_write_b128 %[vlb], a[60:63] offset:3072\n"
        "s_nop 3\n"
        "v_accvgpr_read_b32 v64, a0\n"
        "v_accvgpr_read_b32 v65, a1\n"
        "v_accvgpr_read_b32 v66, a2\n"
        "v_accvgpr_read_b32 v67, a3\n"
        "v_accvgpr_read_b32 v68, a4\n"
        "v_accvgpr_read_b32 v69, a5\n"
        "v_accvgpr_read_b32 v70, a6\n"
        "v_accvgpr_read_b32 v71, a7\n"
        "v_accvgpr_read_b32 v72, a8\n"
        "v_accvgpr_read_b32 v73, a9\n"
        "v_accvgpr_read_b32 v74, a10\n"
        "v_accvgpr_read_b32 v75, a11\n"
        "v_accvgpr_read_b32 v76, a12\n"
        "v_accvgpr_read_b32 v77, a13\n"
        "v_accvgpr_read_b32 v78, a14\n"
        "v_accvgpr_read_b32 v79, a15\n"
        "s_waitcnt vmcnt(0) lgkmcnt(0)\n"
        "s_barrier\n"
                RESD_OPS);
            SBAR();
        }
        {
            const LAS f32x4 *r1 = (const LAS f32x4*)(uintptr_t)fin_r1, *r2 = (const LAS f32x4*)(uintptr_t)fin_r2, *r3 = (const LAS f32x4*)(uintptr_t)fin_r3;
            float y[16];
#pragma unroll
            for (int g = 0; g < 4; g++) {
                const f32x4 p = r1[g * 64] + (r2[g * 64] + r3[g * 64]);
#pragma unroll
                for (int e = 0; e < 4; e++) y[4 * g + e] = c0d[4 * g + e] + p[e];
            }
#pragma unroll
            for (int i = 0; i < 8; i++) xr[i] = xd[i];
            res_add(y, xr);
            if (l0 == 0) {
                st16x2(fin_st, voff1, u32x4{xr[0], xr[1], xr[2], xr[3]}, u32x4{xr[4], xr[5], xr[6], xr[7]});
                CTX();
                if (c8 < 2) {
                    glds<4, false>((const char*)wk + wid * 4096, voff, lds0 + 131072 + wid * 8192);
                    glds<4, false>((const char*)wv + wid * 4096, voff, lds0 + 131072 + wid * 8192 + 4096);
                }
                arrive_block(fin_fl, wid);
            } else {
                asm volatile("global_store_dwordx2 %0, %1, %5\n\tglobal_store_dwordx2 %0, %2, %5 offset:16\n\t"
                             "global_store_dwordx2 %0, %3, %5 offset:32\n\tglobal_store_dwordx2 %0, %4, %5 offset:48"
                             :: "v"(fin_v), "v"(u32x2{xr[0], xr[1]}), "v"(u32x2{xr[2], xr[3]}), "v"(u32x2{xr[4], xr[5]}), "v"(u32x2{xr[6], xr[7]}), "s"(fin_st) : "memory");
            }
        }
        if (l0 == 1) break;
        l0 = 1;
    }
}

extern "C" void kernel_launch(void* const* d_in, const int* in_sizes, int n_in, void* d_out, int out_size, void* d_ws,
                              size_t ws_size, hipStream_t stream) {
    const uint16_t* x = (const uint16_t*)d_in[0];
    const uint16_t* wq = (const uint16_t*)d_in[1];
    const uint16_t* wk = (const uint16_t*)d_in[2];
    const uint16_t* wv = (const uint16_t*)d_in[3];
    const uint16_t* wo = (const uint16_t*)d_in[4];
    const uint16_t* w1 = (const uint16_t*)d_in[6];
    const uint16_t* w3 = (const uint16_t*)d_in[7];
    const uint16_t* w2 = (const uint16_t*)d_in[8];
    char* ws = (char*)d_ws;
    (void)hipFuncSetAttribute((const void*)k_main, hipFuncAttributeMaxDynamicSharedMemorySize, 160 * 1024);
    k_main<<<256, 256, 160 * 1024, stream>>>(ws, (uint16_t*)d_out, x, wq, wk, wv, wo, w1, w3, w2);
}
```
